```python
import torch

def _softmax(x, axis=-1):
    return torch.softmax(x, dim=axis)
def _gelu(x):
    return torch.nn.functional.gelu(x, approximate="tanh")
def _silu(x):
    return torch.nn.functional.silu(x)
def _sigmoid(x):
    return torch.sigmoid(x)
def _layernorm(x, axis=-1, eps=1e-5):
    m = x.mean(dim=axis, keepdim=True)
    v = x.var(dim=axis, keepdim=True, unbiased=False)
    return (x - m) / torch.sqrt(v + eps)
def _scatter_add(tgt, idx, src, axis=0):
    return tgt.clone().index_add_(axis, idx, src)
def _segment_sum(src, seg, n):
    out = torch.zeros((n,) + src.shape[1:], dtype=src.dtype)
    return out.index_add_(0, seg, src)
def _pad(x, pw):
    flat = tuple(v for pair in reversed(pw) for v in pair)
    return torch.nn.functional.pad(x, flat)

kernel_name = 'gather_scatter__cheap_equivariant_egnn__graph'

def setup_inputs():

    _w = (lambda a: (lambda t: t.to(torch.bfloat16) if t.is_floating_point() else t)(torch.as_tensor(a)))
    N, deg, D, De = 10000, 32, 128, max(4, 1)
    E = N * deg
    return {
        "h": _w((lambda *s: torch.randn(*s, dtype=torch.bfloat16) * 0.1)(N, D)),
        "pos": _w((lambda *s: torch.randn(*s, dtype=torch.bfloat16) * 0.8)(N, 3)),
        "e_attr": _w((lambda *s: torch.randn(*s, dtype=torch.bfloat16) * 0.8)(E, De)),
        "src": _w((lambda lo, hi, *s: torch.randint(lo, hi, s))(0, N, E).to(torch.int64)),
        "dst": _w((lambda lo, hi, *s: torch.randint(lo, hi, s))(0, N, E).to(torch.int64)),
        "w_e": _w(0.1 * (lambda *s: torch.randn(*s, dtype=torch.bfloat16) * 0.8)(2 * D + 1 + De, D)),
        "w_x": _w(0.1 * (lambda *s: torch.randn(*s, dtype=torch.bfloat16) * 0.1)(D, 1)),
        "w_h": _w(0.1 * (lambda *s: torch.randn(*s, dtype=torch.bfloat16) * 0.1)(2 * D, D)),
    }

def reference(h, pos, e_attr, src, dst, w_e, w_x, w_h):
    N, D = h.shape[0], h.shape[1]
    diff = pos[dst] - pos[src]
    dist2 = torch.sum(diff * diff, axis=-1, keepdims=True)
    e_in = torch.concatenate([h[dst], h[src], dist2, e_attr], axis=-1)
    m_ij = _silu(e_in @ w_e)
    coord_scale = m_ij @ w_x
    pos_new = pos + _segment_sum(diff * coord_scale, dst, N)
    m_i = _segment_sum(m_ij, dst, N)
    h_new = h + _silu(torch.concatenate([h, m_i], axis=-1) @ w_h)
    return h_new, pos_new
```

```cpp
#include <hip/hip_runtime.h>
#include <hip/hip_bf16.h>
#include <stdint.h>

typedef __bf16 bf16x8 __attribute__((ext_vector_type(8)));
typedef __bf16 bf16x2 __attribute__((ext_vector_type(2)));
typedef short s16x4 __attribute__((ext_vector_type(4)));
typedef float f32x2 __attribute__((ext_vector_type(2)));
typedef float f32x4 __attribute__((ext_vector_type(4), may_alias));
typedef float f32x16 __attribute__((ext_vector_type(16)));
typedef unsigned u32x2 __attribute__((ext_vector_type(2), may_alias));
typedef unsigned u32x4 __attribute__((ext_vector_type(4), may_alias));
typedef unsigned u32x2a4 __attribute__((ext_vector_type(2), aligned(4), may_alias));
typedef unsigned short u16 __attribute__((may_alias));
typedef unsigned u32 __attribute__((may_alias));
#define LDSP(T) __attribute__((address_space(3))) T*
#define DI __device__ __forceinline__
#define SB() __builtin_amdgcn_sched_barrier(0)

constexpr int NN = 10000;
constexpr int CH = 1250, CHS = 1280;
constexpr int NB = 256;
constexpr float NLOG2E = -1.4426950408889634f;
constexpr float NLN2 = -0.6931471805599453f;

constexpr unsigned WS_BT = 0;
constexpr unsigned WS_R1 = 2560000;
constexpr unsigned WS_R2 = WS_R1 + NB * CHS * 16;
constexpr unsigned WS_TAB = WS_R2 + NB * CHS * 4;
constexpr unsigned WS_EP = WS_TAB + NB * NB * 32;
constexpr unsigned WS_END = WS_EP + 256;

constexpr int CAPW = 224;
constexpr int ROWBUF = 8496;
constexpr int L_ROWS = 0;
constexpr int L_WX = 66816;
constexpr int L_R1 = 8 * ROWBUF;
constexpr int L_R2 = L_R1 + 8 * CAPW * 16;
constexpr int L_S = L_R2 + 8 * CAPW * 4;
constexpr int L_M = L_S + 8 * 2560;
constexpr int MSTR = 528;
constexpr int L_H = L_M + 128;
constexpr int L_HIST = L_H + 10240;
constexpr int L_R2S = L_HIST + 4096;
constexpr int L_A = L_M + 40 * MSTR;
constexpr int L_OH = L_A + 8 * 2048;
constexpr int L_GC = L_OH + 128;
constexpr int L_FK = L_GC + 256;
constexpr int L_DUM = L_FK + 1024;
constexpr int L_END = L_DUM + 256;
static_assert(L_H % 256 == 0 && L_R2S + 5120 <= L_A && L_END <= 163840 && L_R1 + 32768 <= L_S, "lds map");

DI constexpr int piece_off(int i) { return 1040 * (i & 3) + 4352 * (i >> 2); }
DI float bf2f(unsigned u) { return __uint_as_float(u << 16); }
DI float bfhi(unsigned u) { return __uint_as_float(u & 0xffff0000u); }
DI unsigned pk2(float a, float b) {
  f32x2 v = {a, b};
  return __builtin_bit_cast(unsigned, __builtin_convertvector(v, bf16x2));
}
DI f32x16 mfma(u32x4 a, u32x4 b, f32x16 c) {
  return __builtin_amdgcn_mfma_f32_32x32x16_bf16(__builtin_bit_cast(bf16x8, a), __builtin_bit_cast(bf16x8, b), c, 0, 0, 0);
}
DI f32x16 zero16() {
  f32x16 z;
#pragma unroll
  for (int i = 0; i < 16; i++) z[i] = 0.f;
  return z;
}
DI u32x4 tr_read2(unsigned a0, unsigned a1) {
  const u32x2 lo = __builtin_bit_cast(u32x2, __builtin_amdgcn_ds_read_tr16_b64_v4i16((LDSP(s16x4))(size_t)a0));
  const u32x2 hi = __builtin_bit_cast(u32x2, __builtin_amdgcn_ds_read_tr16_b64_v4i16((LDSP(s16x4))(size_t)a1));
  const u32x4 v = {lo[0], lo[1], hi[0], hi[1]};
  return v;
}
DI int bucket_start(int b) { return (b * 625 + 15) >> 4; }
DI int wave_incl_scan(int x) {
  x += __builtin_amdgcn_update_dpp(0, x, 0x111, 0xf, 0xf, false);
  x += __builtin_amdgcn_update_dpp(0, x, 0x112, 0xf, 0xf, false);
  x += __builtin_amdgcn_update_dpp(0, x, 0x114, 0xf, 0xf, false);
  x += __builtin_amdgcn_update_dpp(0, x, 0x118, 0xf, 0xf, false);
  x += __builtin_amdgcn_update_dpp(0, x, 0x142, 0xa, 0xf, false);
  x += __builtin_amdgcn_update_dpp(0, x, 0x143, 0xc, 0xf, false);
  return x;
}
DI void dma16(const void* sbase, unsigned voff, unsigned lds_base) {
  asm volatile("s_mov_b32 m0, %2\n\ts_nop 0\n\tglobal_load_lds_dwordx4 %0, %1" ::"v"(voff), "s"(sbase), "s"(lds_base) : "memory");
}
DI void dma16_dev(const void* sbase, unsigned voff, unsigned lds_base) {
  asm volatile("s_mov_b32 m0, %2\n\ts_nop 0\n\tglobal_load_lds_dwordx4 %0, %1 sc1" ::"v"(voff), "s"(sbase), "s"(lds_base) : "memory");
}
DI void dma4_dev(const void* sbase, unsigned voff, unsigned lds_base) {
  asm volatile("s_mov_b32 m0, %2\n\ts_nop 0\n\tglobal_load_lds_dword %0, %1 sc1" ::"v"(voff), "s"(sbase), "s"(lds_base) : "memory");
}
DI void wait_vm0() { asm volatile("s_waitcnt vmcnt(0)" ::: "memory"); }
DI void barrier() { asm volatile("s_waitcnt lgkmcnt(0)\n\ts_barrier" ::: "memory"); }

#define STAMP(i)

DI void silu4(const f32x16& x, int i, unsigned& m0, unsigned& m1) {
  f32x2 a = {x[4 * i], x[4 * i + 1]}, b = {x[4 * i + 2], x[4 * i + 3]};
  f32x2 ea = {__builtin_amdgcn_exp2f(a[0]), __builtin_amdgcn_exp2f(a[1])};
  f32x2 eb = {__builtin_amdgcn_exp2f(b[0]), __builtin_amdgcn_exp2f(b[1])};
  ea += 1.0f;
  eb += 1.0f;
  f32x2 ra = {__builtin_amdgcn_rcpf(ea[0]), __builtin_amdgcn_rcpf(ea[1])};
  f32x2 rb = {__builtin_amdgcn_rcpf(eb[0]), __builtin_amdgcn_rcpf(eb[1])};
  a *= ra;
  b *= rb;
  m0 = __builtin_bit_cast(unsigned, __builtin_convertvector(a, bf16x2));
  m1 = __builtin_bit_cast(unsigned, __builtin_convertvector(b, bf16x2));
}

__global__ __launch_bounds__(512, 2) void egnn_fused(const u16* __restrict__ h, const u16* __restrict__ pos,
                                                     const u32x2* __restrict__ eattr, const int* __restrict__ src,
                                                     const int* __restrict__ dst, const u16* __restrict__ we,
                                                     const u16* __restrict__ wx, const u16* __restrict__ wh,
                                                     char* ws, u16* __restrict__ out) {
  __shared__ __attribute__((aligned(256))) char lds[L_END];
  const unsigned lbase = (unsigned)(size_t)lds;
  const int tid = threadIdx.x, lane = tid & 63;
  const int wv = __builtin_amdgcn_readfirstlane(tid >> 6);
  const int w = blockIdx.x;
  const int b = (w & 7) * 32 + (w >> 3);
  const int n0 = bucket_start(b), nsz = bucket_start(b + 1) - n0;
  const int r = lane & 31, hh = lane >> 5;
  const int blk = (lane >> 4) & 1, q4 = (lane & 15) >> 2, p4 = lane & 3;
  const __amdgpu_buffer_rsrc_t wsr = __builtin_amdgcn_make_buffer_rsrc(ws, 0, WS_END, 0x00020000);
  STAMP(0);

  int d[3], s[3];
  u32x2 a[3];
  const bool v2 = tid < CH - 1024;
#pragma unroll
  for (int k = 0; k < 3; k++) d[k] = dst[w * CH + (k < 2 || v2 ? tid + 512 * k : 0)];
#pragma unroll
  for (int k = 0; k < 3; k++) s[k] = src[w * CH + (k < 2 || v2 ? tid + 512 * k : 0)];
  const unsigned tag = (__builtin_amdgcn_raw_buffer_load_b32(wsr, WS_EP, 0, 16) + 1u) & 0x7fffffffu;
#pragma unroll
  for (int k = 0; k < 3; k++) a[k] = eattr[w * CH + (k < 2 || v2 ? tid + 512 * k : 0)];
  {
    const int q = lane >> 4, c = lane & 15;
    {
      const int row = 4 * wv + q;
      dma16(h, (unsigned)(min(n0 + row, NN - 1) * 256 + 16 * (c ^ (row & 15))), lbase + L_H + wv * 1024);
      if (wv < 2) {
        const int row2 = 32 + row;
        dma16(h, (unsigned)(min(n0 + row2, NN - 1) * 256 + 16 * (c ^ (row2 & 15))), lbase + L_H + (8 + wv) * 1024);
      }
    }
    const unsigned lb = (unsigned)(q * 256 + 16 * (c ^ (q << 2)));
#pragma unroll
    for (int i = 0; i < 4; i++) dma16(we, (lb ^ (16 * i)) + (32 + 4 * wv + i) * 1024, lbase + L_ROWS + (32 + 4 * wv + i) * 1024);
#pragma unroll
    for (int i = 0; i < 4; i++) dma16(we, (lb ^ (16 * i)) + (4 * wv + i) * 1024, lbase + L_ROWS + (4 * wv + i) * 1024);
#pragma unroll
    for (int i = 0; i < 4; i++) dma16(wh, (lb ^ (16 * i)) + (4 * wv + i) * 1024, lbase + L_R1 + (4 * wv + i) * 1024);
    if (wv == 2) dma16(we, 65536 + lane * 16, lbase + 65536);
    if (wv == 3 && lane < 16) dma16(we, 66560 + lane * 16, lbase + 66560);
    if (wv == 4 && lane < 16) dma16(wx, lane * 16, lbase + L_WX);
  }
  {
    const u32x4 z = {0u, 0u, 0u, 0u};
    if (tid < 256) *(u32x4*)(lds + L_HIST + tid * 16) = z;
    else {
      const int t = tid - 256;
      *(u32*)(lds + L_FK + t * 4) = (unsigned)(128 + 32 * ((t >> 3) & 3) + 4 * (t >> 5) + (t & 3)) << 8;
      if (t < 16) {
        const int j = (t >> 1) - 4 * (t & 1);
        u32x2 v = {0u, 0u};
        if (j == 0) v[0] = 0x3F80u;
        if (j == 1) v[0] = 0x3F800000u;
        if (j == 2) v[1] = 0x3F80u;
        if (j == 3) v[1] = 0x3F800000u;
        *(u32x2*)(lds + L_OH + t * 8) = v;
        *(u32*)(lds + L_GC + t * 4) = 0u;
      }
    }
  }
  barrier();
  STAMP(1);
  u32x2 pd[3], ps[3];
  int key[3];
  unsigned rank[3];
#pragma unroll
  for (int k = 0; k < 3; k++) {
    pd[k] = *(const u32x2a4*)((const char*)pos + ((6 * d[k]) & ~3));
    ps[k] = *(const u32x2a4*)((const char*)pos + ((6 * s[k]) & ~3));
  }
#pragma unroll
  for (int k = 0; k < 3; k++) {
    const int bb = (int)(((unsigned)d[k] * 429497u) >> 24);
    const int loc = d[k] - bucket_start(bb);
    const int g = (loc * 205) >> 10;
    key[k] = 8 * bb + g;
    s[k] = (s[k] << 8) | (loc - 5 * g);
    const int sh = 16 * (g & 1);
    rank[k] = 0;
    if (k < 2 || v2) rank[k] = (atomicAdd((unsigned*)(lds + L_HIST) + (key[k] >> 1), 1u << sh) >> sh) & 0xffffu;
  }
  wait_vm0();
  barrier();
  STAMP(2);
  auto hfrag_addr = [&](int nb) {
    const int row = min(5 * (4 * nb + (r >> 3)) + (r & 7), 39);
    return L_H + row * 256 + 16 * (hh ^ (row & 15));
  };
  const unsigned trx0 = (unsigned)((8 * hh + q4) * 256), trs0 = (unsigned)((q4 << 2) | (2 * hh));
  u32x4 ent = {0u, 0u, 0u, 0u};
  unsigned o8 = 0;
  if (wv < 4) {
    const u32x4 cnt = *(const u32x4*)(lds + L_HIST + tid * 16);
    unsigned pre = 0;
#pragma unroll
    for (int j = 0; j < 3; j++) {
      if (wv > j) {
        const u32x4 c2 = *(const u32x4*)(lds + L_HIST + (64 * j + lane) * 16);
        pre += c2[0] + c2[1] + c2[2] + c2[3];
      }
    }
    pre = (pre & 0xffffu) + (pre >> 16);
    const unsigned s0 = cnt[0] + cnt[1] + cnt[2] + cnt[3];
    const unsigned tot = (s0 & 0xffffu) + (s0 >> 16);
    const unsigned inc = (unsigned)wave_incl_scan((int)(tot | (pre << 16)));
    unsigned o = (inc & 0xffffu) - tot + ((unsigned)__builtin_amdgcn_readlane((int)inc, 63) >> 16);
#pragma unroll
    for (int j = 0; j < 4; j++) {
      const unsigned o1 = o + (cnt[j] & 0xffffu);
      ent[j] = o | (o1 << 16);
      o = o1 + (cnt[j] >> 16);
    }
    o8 = o;
    *(u32x4*)(lds + L_A + tid * 16) = ent;
  } else {
    const int T = wv - 4;
    const unsigned ch = (unsigned)(8 * (T & 1) + 4 * (p4 & 1) + 2 * blk + (T >> 1));
    const unsigned a0 = lbase + 32768 + trx0 + 16 * (ch ^ trs0) + 8 * (p4 >> 1);
    const unsigned a1 = lbase + 32768 + trx0 + 1024 + 16 * (ch ^ trs0 ^ 1) + 8 * (p4 >> 1);
    const int ha0 = hfrag_addr(0), ha1 = hfrag_addr(1);
    f32x16 acc0 = zero16(), acc1 = zero16();
#pragma unroll
    for (int ks = 0; ks < 8; ks++) {
      const u32x4 wa = tr_read2(a0 + ks * 4096, a1 + ks * 4096);
      acc0 = mfma(wa, *(const u32x4*)(lds + (ha0 ^ (32 * ks))), acc0);
      acc1 = mfma(wa, *(const u32x4*)(lds + (ha1 ^ (32 * ks))), acc1);
    }
#pragma unroll
    for (int nb = 0; nb < 2; nb++) {
      const f32x16& acc = nb ? acc1 : acc0;
      const int loc = 5 * (4 * nb + (r >> 3)) + (r & 7);
      if ((r & 7) < 5 && loc < nsz) {
        u32x4 o0, o1;
#pragma unroll
        for (int i = 0; i < 4; i++) {
          o0[i] = pk2(acc[2 * i] * NLOG2E, acc[2 * i + 1] * NLOG2E);
          o1[i] = pk2(acc[8 + 2 * i] * NLOG2E, acc[8 + 2 * i + 1] * NLOG2E);
        }
        const unsigned off = WS_BT + (unsigned)(n0 + loc) * 256 + 64 * T + 32 * hh;
        __builtin_amdgcn_raw_buffer_store_b128(o0, wsr, off, 0, 16);
        __builtin_amdgcn_raw_buffer_store_b128(o1, wsr, off + 16, 0, 16);
      }
    }
  }
  barrier();
  STAMP(3);
#pragma unroll
  for (int k = 0; k < 3; k++) {
    const int p = *(const u16*)(lds + L_A + key[k] * 2) + rank[k];
    const unsigned long long ud = (((unsigned long long)pd[k][1] << 32) | pd[k][0]) >> ((d[k] & 1) * 16);
    const unsigned long long us = (((unsigned long long)ps[k][1] << 32) | ps[k][0]) >> (((s[k] >> 8) & 1) * 16);
    const unsigned dlo = (unsigned)ud, dhi = (unsigned)(ud >> 32), slo = (unsigned)us, shi = (unsigned)(us >> 32);
    const unsigned dxy = pk2(bf2f(dlo) - bf2f(slo), bfhi(dlo) - bfhi(slo));
    const unsigned dz_ = pk2(bf2f(dhi) - bf2f(shi), 0.f);
    const float dx = bf2f(dxy), dy = bfhi(dxy), dz = bf2f(dz_);
    const unsigned sxy = pk2(dx * dx, dy * dy), sz = pk2(dz * dz, 0.f);
    const unsigned d2 = pk2((bf2f(sxy) + bfhi(sxy)) + bf2f(sz), 0.f) & 0xffffu;
    const u32x4 rec = {a[k][0], d2 | (dxy << 16), a[k][1], (dxy >> 16) | (dz_ << 16)};
    if (k < 2 || v2) {
      *(u32x4*)(lds + L_S + p * 16) = rec;
      *(u32*)(lds + L_R2S + p * 4) = (unsigned)s[k];
    }
  }
  barrier();
  STAMP(4);
  {
    const u32x4 z = {0u, 0u, 0u, 0u};
#pragma unroll
    for (int k = 0; k < 3; k++) {
      if (k < 2 || tid < 256) {
        const u32x4 v = *(const u32x4*)(lds + L_S + (tid + 512 * k) * 16);
        __builtin_amdgcn_raw_buffer_store_b128(v, wsr, WS_R1 + (unsigned)(w * CHS + tid + 512 * k) * 16, 0, 16);
        *(u32x4*)(lds + L_S + (tid + 512 * k) * 16) = z;
      }
    }
    if (tid < 320) {
      const u32x4 v = *(const u32x4*)(lds + L_R2S + tid * 16);
      __builtin_amdgcn_raw_buffer_store_b128(v, wsr, WS_R2 + (unsigned)(w * CHS) * 4 + tid * 16, 0, 16);
    }
  }
  const int T = wv & 3, nb = wv >> 2;
  f32x16 P;
  {
    const int ha = hfrag_addr(nb);
    const unsigned chA = (unsigned)(4 * T + 2 * blk + (p4 >> 1)), chP = (unsigned)(4 * T + 2 * (p4 & 1) + blk);
    const unsigned a0 = lbase + trx0 + 16 * (chA ^ trs0) + 8 * (p4 & 1), a1 = lbase + trx0 + 1024 + 16 * (chA ^ trs0 ^ 1) + 8 * (p4 & 1);
    const unsigned p0 = lbase + L_R1 + trx0 + 16 * (chP ^ trs0) + 8 * (p4 >> 1), p1 = lbase + L_R1 + trx0 + 1024 + 16 * (chP ^ trs0 ^ 1) + 8 * (p4 >> 1);
    f32x16 acc = zero16();
    P = zero16();
#pragma unroll
    for (int ks = 0; ks < 8; ks++) {
      const u32x4 hf = *(const u32x4*)(lds + (ha ^ (32 * ks)));
      acc = mfma(hf, tr_read2(a0 + ks * 4096, a1 + ks * 4096), acc);
      P = mfma(tr_read2(p0 + ks * 4096, p1 + ks * 4096), hf, P);
    }
#pragma unroll
    for (int i = 0; i < 4; i++) {
      const u32x2 v = {pk2(acc[4 * i] * NLOG2E, acc[4 * i + 1] * NLOG2E), pk2(acc[4 * i + 2] * NLOG2E, acc[4 * i + 3] * NLOG2E)};
      *(u32x2*)(lds + L_A + (4 * nb + i) * 2048 + T * 512 + lane * 8) = v;
    }
  }
  u32x4 extB[4];
  float wxv[4];
#pragma unroll
  for (int t = 0; t < 4; t++) {
    const u16* wp = (const u16*)(lds + 65536) + 32 * t + r;
    const float w0 = bf2f(wp[(1 + 2 * hh) * 128]) * NLOG2E, w1 = bf2f(wp[(2 + 2 * hh) * 128]) * NLOG2E, w2 = bf2f(wp[0]) * NLOG2E;
    extB[t][2] = pk2(w0, w1);
    extB[t][3] = hh ? 0u : pk2(w2, 0.f);
    wxv[t] = bf2f(((const u16*)(lds + L_WX))[32 * t + r]) * NLN2;
  }
  wait_vm0();
  barrier();
  STAMP(5);

  const unsigned tabme = WS_TAB + (unsigned)(tid * NB + b) * 32;
  auto poll = [&](unsigned (&o)[9]) {
    u32x4 ga;
    u32x2 gb;
    for (int spin = 0; spin < (1 << 22); spin++) {
      asm volatile("global_load_dwordx4 %0, %2, %3 sc1\n\tglobal_load_dwordx2 %1, %2, %3 offset:16 sc1\n\ts_waitcnt vmcnt(0)"
                   : "=&v"(ga), "=&v"(gb) : "v"(tabme), "s"(ws) : "memory");
      if ((((ga[0] ^ tag) | (ga[2] ^ tag) | (gb[0] ^ tag)) & 0x7fffffffu) == 0) break;
      __builtin_amdgcn_s_sleep(2);
    }
    const unsigned lo[3] = {ga[0], ga[2], gb[0]}, hi[3] = {ga[1], ga[3], gb[1]};
#pragma unroll
    for (int j = 0; j < 3; j++) {
      o[3 * j] = min((lo[j] >> 31) | ((hi[j] & 0x3ffu) << 1), (unsigned)CH);
      o[3 * j + 1] = min((hi[j] >> 10) & 0x7ffu, (unsigned)CH);
      o[3 * j + 2] = min(hi[j] >> 21, (unsigned)CH);
    }
  };
  auto build_map = [&](const unsigned (&o)[9], const int (&base)[8], int wb) {
#pragma unroll
    for (int g = 0; g < 8; g++) {
      const int cc = (int)o[g + 1] - (int)o[g], db = base[g] - wb;
      const int jlo = max(0, -db), jhi = min(cc, CAPW - db);
#pragma clang loop vectorize(disable) unroll(disable)
      for (int j = jlo; j < jhi; j++) *(u32*)(lds + L_R2 + (g * CAPW + db + j) * 4) = (unsigned)(tid * CHS) + o[g] + j;
    }
  };
  if (wv < 4) {
    {
      const unsigned ov[9] = {ent[0] & 0xffffu, ent[0] >> 16, ent[1] & 0xffffu, ent[1] >> 16, ent[2] & 0xffffu, ent[2] >> 16, ent[3] & 0xffffu, ent[3] >> 16, o8};
      u32x4 ga;
      u32x2 gb;
      ga[0] = tag | (ov[0] << 31); ga[1] = (ov[0] >> 1) | (ov[1] << 10) | (ov[2] << 21);
      ga[2] = tag | (ov[3] << 31); ga[3] = (ov[3] >> 1) | (ov[4] << 10) | (ov[5] << 21);
      gb[0] = tag | (ov[6] << 31); gb[1] = (ov[6] >> 1) | (ov[7] << 10) | (ov[8] << 21);
      const unsigned off = WS_TAB + (unsigned)(w * NB + tid) * 32;
      __builtin_amdgcn_raw_buffer_store_b128(ga, wsr, off, 0, 16);
      __builtin_amdgcn_raw_buffer_store_b64(gb, wsr, off + 16, 0, 16);
    }
    unsigned o[9];
    poll(o);
    STAMP(6);
    int base[8], tv = 0;
#pragma unroll
    for (int g = 0; g < 8; g++) {
      const int cc = (int)o[g + 1] - (int)o[g];
      const int inc = wave_incl_scan(cc);
      base[g] = inc - cc;
      const int tot = __builtin_amdgcn_readlane(inc, 63);
      tv = lane == g ? tot : tv;
    }
    int wbase = 0;
    if (lane < 8) {
      wbase = (int)atomicAdd((unsigned*)(lds + L_GC) + lane, (unsigned)tv);
      *(u32*)(lds + L_GC + 64 + (wv * 8 + lane) * 4) = (unsigned)wbase;
    }
#pragma unroll
    for (int g = 0; g < 8; g++) base[g] += __builtin_amdgcn_readlane(wbase, g);
    build_map(o, base, 0);
  }
  barrier();
  STAMP(7);
  if (w == 0 && tid == 0) __builtin_amdgcn_raw_buffer_store_b32(tag, wsr, WS_EP, 0, 16);

  int gq, cnt, cmax;
  {
    const int c = *(const u32*)(lds + L_GC + (lane & 7) * 4);
    int rk = 0;
    cmax = 0;
#pragma unroll
    for (int g = 0; g < 8; g++) {
      const int cg = __builtin_amdgcn_readlane(c, g);
      rk += (cg > c || (cg == c && g < (lane & 7))) ? 1 : 0;
      cmax = max(cmax, cg);
    }
    const unsigned long long bal = __ballot(rk == (wv < 4 ? wv : 11 - wv));
    gq = __builtin_ctzll(bal) & 7;
    cnt = __builtin_amdgcn_readlane(c, gq);
  }
#pragma unroll
  for (int t = 0; t < 4; t++) {
    const u32x2 v = *(const u32x2*)(lds + L_A + gq * 2048 + t * 512 + lane * 8);
    extB[t][0] = v[0];
    extB[t][1] = v[1];
  }
  u32x4 I0, I1;
  {
    const unsigned one = (r & 1) ? 0x3F800000u : 0x3F80u;
#pragma unroll
    for (int jj = 0; jj < 4; jj++) {
      const bool m = ((r & 7) >> 1) == jj;
      I0[jj] = (m && (r >> 3) == hh) ? one : 0u;
      I1[jj] = (m && (r >> 3) == 2 + hh) ? one : 0u;
    }
  }
  const int rowbuf = L_ROWS + wv * ROWBUF;
  const unsigned kdma = 16 * ((lane & 15) ^ (4 * (lane >> 4)));
  const int rd0 = rowbuf + 1040 * (r & 3) + 4352 * ((r >> 2) & 1) + (r >> 3) * 256;
  const int rdA = rd0 + 64 * ((2 * hh) ^ (r >> 3)), rdB = rd0 + 64 * ((2 * hh + 1) ^ (r >> 3));
  const int sscr = L_S + gq * 2560;
  const int swr = sscr + 32 * (r >> 4) + 16 * ((r >> 2) & 1) + 8 * ((r >> 3) & 1) + 2 * (r & 3) + 160 * hh;
  const int srd = sscr + r * 80 + 16 * hh;
  const int l1 = L_R1 + gq * CAPW * 16, l2 = L_R2 + gq * CAPW * 4;
  const char* btab = ws + WS_BT;

  f32x16 Y[4];
#pragma unroll
  for (int t = 0; t < 4; t++) Y[t] = zero16();

  bool wh_loaded = false;
  for (int wb = 0; wb == 0 || wb < cmax; wb += CAPW) {
    if (wb > 0) {
      barrier();
      if (wv < 4) {
        unsigned o[9];
        poll(o);
        int base[8];
#pragma unroll
        for (int g = 0; g < 8; g++) {
          const int cc = (int)o[g + 1] - (int)o[g];
          base[g] = wave_incl_scan(cc) - cc + (int)*(const u32*)(lds + L_GC + 64 + (wv * 8 + g) * 4);
        }
        build_map(o, base, wb);
      }
      barrier();
    }
    const int n = min(cnt - wb, CAPW);
    if (n <= 0) continue;
    const int nt = (n + 31) >> 5;
    for (int p0 = 0; p0 < 32 * nt; p0 += 64) {
      if (p0 + lane < 32 * nt) {
        const unsigned mi = *(const u32*)(lds + l2 + (p0 + lane) * 4);
        const unsigned idx = p0 + lane < n ? mi : 0u;
        dma16_dev(ws + WS_R1, idx * 16, lbase + l1 + p0 * 16);
        dma4_dev(ws + WS_R2, idx * 4, lbase + l2 + p0 * 4);
      }
    }
    wait_vm0();
    STAMP(8);

    u32x4 rows[8], extA, S0, S1, ra, rb, m1p = {0u, 0u, 0u, 0u};
    u32x2 r1;
    unsigned r2;
    int sp;
    f32x16 XA, XB;
    auto list_read = [&](int tl) {
      const int list = tl < nt ? l2 + tl * 128 : L_FK + wv * 128;
      ra = *(const u32x4*)(lds + list + (lane >> 4) * 32);
      rb = *(const u32x4*)(lds + list + (lane >> 4) * 32 + 16);
    };
    auto dma_piece = [&](int tl, int i) {
      const unsigned v = i < 4 ? ra[i & 3] : rb[i & 3];
      dma16_dev(tl < nt ? btab : (const char*)wh, (v & 0xffffff00u) | kdma, lbase + rowbuf + piece_off(i));
    };
    auto meta_read = [&](int tl) {
      r1 = *(const u32x2*)(lds + l1 + (tl * 32 + r) * 16 + hh * 8);
      r2 = *(const u32*)(lds + l2 + (tl * 32 + r) * 4);
    };
    auto meta_use = [&](int tl) {
      const unsigned dl = r2 & 7u;
      const u32x2 oh = *(const u32x2*)(lds + L_OH + dl * 16 + hh * 8);
      extA[0] = oh[0]; extA[1] = oh[1]; extA[2] = r1[0]; extA[3] = r1[1];
      sp = (tl * 32 + r < n) ? swr + (int)dl * 320 : L_DUM;
    };
    auto s_write = [&]() {
      *(u16*)(lds + sp) = (u16)(hh ? extA[3] : 0x3F80u);
      *(u16*)(lds + sp + 80) = (u16)(extA[3] >> 16);
    };
    auto rows_read = [&]() {
#pragma unroll
      for (int j = 0; j < 8; j++) rows[j] = *(const u32x4*)(lds + (j < 4 ? rdA : rdB) + 16 * (j & 3));
    };
    auto rows_pin = [&]() {
      asm volatile("" : "+v"(rows[0]), "+v"(rows[1]), "+v"(rows[2]), "+v"(rows[3]), "+v"(rows[4]), "+v"(rows[5]), "+v"(rows[6]), "+v"(rows[7]));
    };

    list_read(0);
    meta_read(0);
#pragma unroll
    for (int i = 0; i < 8; i++) dma_piece(0, i);
    list_read(1);
    meta_use(0);
    s_write();
    S1 = m1p;
    wait_vm0();
    rows_read();
    rows_pin();
#pragma unroll
    for (int i = 0; i < 8; i++) dma_piece(1, i);
    XA = mfma(rows[0], I0, zero16());
    XA = mfma(rows[1], I1, XA);
    XA = mfma(extA, extB[0], XA);
    STAMP(9);

    const int nfull = n >> 5;
    for (int tl = 0; tl < nfull; tl++) {
#pragma unroll
      for (int t = 0; t < 4; t++) {
        f32x16& Xc = (t & 1) ? XB : XA;
        f32x16& Xn = (t & 1) ? XA : XB;
        const int tn = (t + 1) & 3;
        float e[16];
        SB();
        if (t == 0) {
          S0 = *(const u32x4*)(lds + srd);
        }
        if (t == 2) meta_read(tl + 1);
        Xn = mfma(rows[2 * tn], I0, zero16());
        SB();
#pragma unroll
        for (int i = 0; i < 4; i++) e[i] = __builtin_amdgcn_exp2f(Xc[i]);
        if (t == 3) { dma_piece(tl + 2, 0); dma_piece(tl + 2, 1); }
        SB();
        Xn = mfma(rows[2 * tn + 1], I1, Xn);
        SB();
#pragma unroll
        for (int i = 4; i < 8; i++) e[i] = __builtin_amdgcn_exp2f(Xc[i]);
        if (t == 3) { dma_piece(tl + 2, 2); dma_piece(tl + 2, 3); }
        SB();
        Xn = mfma(extA, extB[tn], Xn);
        SB();
#pragma unroll
        for (int i = 8; i < 12; i++) e[i] = __builtin_amdgcn_exp2f(Xc[i]);
        if (t == 3) { dma_piece(tl + 2, 4); dma_piece(tl + 2, 5); }
        SB();
        Y[(t + 3) & 3] = mfma(S1, m1p, Y[(t + 3) & 3]);
        SB();
        if (t == 0) {
          S1 = *(const u32x4*)(lds + srd + 32);
          *(u16*)(lds + sp) = 0;
          *(u16*)(lds + sp + 80) = 0;
        }
        if (t == 2) {
          wait_vm0();
          rows_read();
          list_read(tl + 2);
          meta_use(tl + 1);
        }
#pragma unroll
        for (int i = 12; i < 16; i++) e[i] = __builtin_amdgcn_exp2f(Xc[i]);
        if (t == 3) { dma_piece(tl + 2, 6); dma_piece(tl + 2, 7); s_write(); }
        SB();
        f32x2 dd[8];
#pragma unroll
        for (int i = 0; i < 8; i++) { dd[i][0] = e[2 * i]; dd[i][1] = e[2 * i + 1]; dd[i] += 1.0f; }
        SB();
#pragma unroll
        for (int i = 0; i < 8; i++) { dd[i][0] = __builtin_amdgcn_rcpf(dd[i][0]); dd[i][1] = __builtin_amdgcn_rcpf(dd[i][1]); }
        SB();
        u32x4 m0;
#pragma unroll
        for (int i = 0; i < 4; i++) { f32x2 x = {Xc[2 * i], Xc[2 * i + 1]}; dd[i] *= x; }
#pragma unroll
        for (int i = 0; i < 4; i++) m0[i] = __builtin_bit_cast(unsigned, __builtin_convertvector(dd[i], bf16x2));
        SB();
#pragma unroll
        for (int i = 4; i < 8; i++) { f32x2 x = {Xc[2 * i], Xc[2 * i + 1]}; dd[i] *= x; }
        SB();
        Y[t] = mfma(S0, m0, Y[t]);
        SB();
#pragma unroll
        for (int i = 0; i < 4; i++) m1p[i] = __builtin_bit_cast(unsigned, __builtin_convertvector(dd[4 + i], bf16x2));
        if (t == 2) rows_pin();
      }
    }
    SB();
    Y[3] = mfma(S1, m1p, Y[3]);
    if (nfull < nt) {
      const int c = n & 31;
      S0 = *(const u32x4*)(lds + srd);
      S1 = *(const u32x4*)(lds + srd + 32);
      *(u16*)(lds + sp) = 0;
      *(u16*)(lds + sp + 80) = 0;
#pragma unroll
      for (int t = 0; t < 4; t++) {
        f32x16& Xc = (t & 1) ? XB : XA;
        f32x16& Xn = (t & 1) ? XA : XB;
        if (t < 3) {
          Xn = mfma(rows[2 * t + 2], I0, zero16());
          Xn = mfma(rows[2 * t + 3], I1, Xn);
          Xn = mfma(extA, extB[t + 1], Xn);
        }
        unsigned m[8] = {0u, 0u, 0u, 0u, 0u, 0u, 0u, 0u};
        silu4(Xc, 0, m[0], m[1]);
        if (c > 8) silu4(Xc, 1, m[2], m[3]);
        if (c > 16) silu4(Xc, 2, m[4], m[5]);
        if (c > 24) silu4(Xc, 3, m[6], m[7]);
        const u32x4 m0 = {m[0], m[1], m[2], m[3]}, m1 = {m[4], m[5], m[6], m[7]};
        Y[t] = mfma(S0, m0, Y[t]);
        Y[t] = mfma(S1, m1, Y[t]);
      }
    }
    wh_loaded = true;
    wait_vm0();
  }
  STAMP(10);
  if (!wh_loaded) {
    const u32x4 fa = *(const u32x4*)(lds + L_FK + wv * 128 + (lane >> 4) * 32);
#pragma unroll
    for (int i = 0; i < 4; i++) dma16(wh, fa[i] | kdma, lbase + rowbuf + piece_off(i));
  }

  const int loc = 5 * (4 * nb + (r >> 3)) + (r & 7);
  const bool valid = (r & 7) < 5 && loc < nsz;
  const int nrow = min(n0 + loc, NN - 1);
  const u32x4 res0 = *(const u32x4*)(h + (size_t)nrow * 128 + 32 * T + 16 * hh);
  const u32x4 res1 = *(const u32x4*)(h + (size_t)nrow * 128 + 32 * T + 16 * hh + 8);
  unsigned pin = 0;
  if (tid < 3 * nsz) pin = pos[3 * n0 + tid];
#pragma unroll
  for (int i = 0; i < 3; i++) {
    if (i < 2 || hh == 0) {
      const f32x4 mv = {Y[0][4 * i], Y[1][4 * i], Y[2][4 * i], Y[3][4 * i]};
      *(f32x4*)(lds + L_M + (5 * gq + 2 * i + hh) * MSTR + 16 * r) = mv;
      f32x4 zv;
#pragma unroll
      for (int cc = 1; cc < 4; cc++)
        zv[cc - 1] = Y[0][4 * i + cc] * wxv[0] + Y[1][4 * i + cc] * wxv[1] + Y[2][4 * i + cc] * wxv[2] + Y[3][4 * i + cc] * wxv[3];
      zv[3] = 0.f;
      *(f32x4*)(lds + sscr + (2 * i + hh) * 512 + 16 * r) = zv;
    }
  }
  wait_vm0();
  barrier();
  STAMP(11);
  {
    const unsigned wa = lbase + L_ROWS + 2080 * hh + 256 * q4 + 64 * (T ^ q4) + 32 * (p4 & 1) + 16 * blk + 8 * (p4 >> 1);
    const int ma = L_M + min(loc, 39) * MSTR + 32 * hh;
#pragma unroll
    for (int ks = 0; ks < 8; ks++) {
      const f32x4 x0 = *(const f32x4*)(lds + ma + 64 * ks), x1 = *(const f32x4*)(lds + ma + 64 * ks + 16);
      const u32x4 bq = {pk2(x0[0] * NLN2, x0[1] * NLN2), pk2(x0[2] * NLN2, x0[3] * NLN2), pk2(x1[0] * NLN2, x1[1] * NLN2), pk2(x1[2] * NLN2, x1[3] * NLN2)};
      P = mfma(tr_read2(wa + ks * ROWBUF, wa + ks * ROWBUF + 1040), bq, P);
    }
    if (valid) {
      u32x4 ov[2];
#pragma unroll
      for (int i = 0; i < 8; i++) {
        const unsigned rs = i < 4 ? res0[i & 3] : res1[i & 3];
        const float x0 = P[2 * i], x1 = P[2 * i + 1];
        const float s0 = x0 * __builtin_amdgcn_rcpf(1.f + __builtin_amdgcn_exp2f(x0 * NLOG2E));
        const float s1 = x1 * __builtin_amdgcn_rcpf(1.f + __builtin_amdgcn_exp2f(x1 * NLOG2E));
        ov[i >> 2][i & 3] = pk2(bf2f(rs) + s0, bfhi(rs) + s1);
      }
      u32x4* op = (u32x4*)(out + (size_t)(n0 + loc) * 128 + 32 * T + 16 * hh);
      op[0] = ov[0];
      op[1] = ov[1];
    }
  }
  if (tid < 3 * nsz) {
    const int nl = (tid * 171) >> 9, cc = tid - 3 * nl;
    const int g = (nl * 205) >> 10;
    const float* zp = (const float*)(lds + L_S + g * 2560 + (nl - 5 * g) * 512) + cc;
    float z0 = 0.f, z1 = 0.f, z2 = 0.f, z3 = 0.f;
#pragma unroll
    for (int i = 0; i < 8; i++) {
      const int j = (4 * i + tid) & 31;
      z0 += zp[4 * j]; z1 += zp[4 * ((j + 1) & 31)]; z2 += zp[4 * ((j + 2) & 31)]; z3 += zp[4 * ((j + 3) & 31)];
    }
    out[(size_t)NN * 128 + 3 * n0 + tid] = (u16)pk2(bf2f(pin) + ((z0 + z1) + (z2 + z3)), 0.f);
  }
  STAMP(12);
}

extern "C" void kernel_launch(void* const* d_in, const int* in_sizes, int n_in, void* d_out, int out_size, void* d_ws,
                              size_t ws_size, hipStream_t stream) {
  egnn_fused<<<NB, 512, 0, stream>>>((const u16*)d_in[0], (const u16*)d_in[1], (const u32x2*)d_in[2], (const int*)d_in[3],
                                     (const int*)d_in[4], (const u16*)d_in[5], (const u16*)d_in[6], (const u16*)d_in[7],
                                     (char*)d_ws, (u16*)d_out);
}
```

```cpp
#include <hip/hip_runtime.h>
#include <hip/hip_bf16.h>
#include <stdint.h>

typedef __bf16 bf16x8 __attribute__((ext_vector_type(8)));
typedef __bf16 bf16x2 __attribute__((ext_vector_type(2)));
typedef short s16x4 __attribute__((ext_vector_type(4)));
typedef float f32x2 __attribute__((ext_vector_type(2)));
typedef float f32x4 __attribute__((ext_vector_type(4), may_alias));
typedef float f32x16 __attribute__((ext_vector_type(16)));
typedef unsigned u32x2 __attribute__((ext_vector_type(2), may_alias));
typedef unsigned u32x4 __attribute__((ext_vector_type(4), may_alias));
typedef unsigned u32x2a4 __attribute__((ext_vector_type(2), aligned(4), may_alias));
typedef unsigned short u16 __attribute__((may_alias));
typedef unsigned u32 __attribute__((may_alias));
#define LDSP(T) __attribute__((address_space(3))) T*
#define DI __device__ __forceinline__
#define SB() __builtin_amdgcn_sched_barrier(0)
#define PIN(x) asm volatile("" : "+v"(x))

constexpr int NN = 10000;
constexpr int CH = 1250, CHS = 1280;
constexpr int NB = 256;
constexpr float NLOG2E = -1.4426950408889634f;
constexpr float NLN2 = -0.6931471805599453f;

constexpr unsigned WS_BT = 0;
constexpr unsigned WS_R1 = 2560000;
constexpr unsigned WS_R2 = WS_R1 + NB * CHS * 16;
constexpr unsigned WS_TAB = WS_R2 + NB * CHS * 4;
constexpr unsigned WS_EP = WS_TAB + NB * NB * 8;
constexpr unsigned WS_END = WS_EP + 256;

constexpr int CAPW = 224;
constexpr int ROWBUF = 8496;
constexpr int L_ROWS = 0;
constexpr int L_R1 = 8 * ROWBUF;
constexpr int L_R2 = L_R1 + 8 * CAPW * 16;
constexpr int L_S = L_R2 + 8 * CAPW * 4;
constexpr int L_M = L_S + 8 * 2560;
constexpr int MSTR = 272;
constexpr int L_H = 135168;
constexpr int L_A = L_H + 10240;
constexpr int L_HIST = L_A + 8 * 2048;
constexpr int L_OH = L_HIST + 1024;
constexpr int L_GC = L_OH + 128;
constexpr int L_DUM = L_GC + 64;
constexpr int L_WX = L_DUM + 128;
constexpr int L_END = L_WX + 256;
constexpr int L_W = L_R1;
constexpr int L_OFF = L_A;
constexpr int L_R2S = L_A + 1024;
constexpr int MAPW = 256;
constexpr int L_MAP = L_M;
constexpr int L_FK = L_HIST;
static_assert(L_H % 256 == 0 && L_M + 40 * MSTR <= L_H && L_END <= 163840, "lds map");

DI constexpr int piece_off(int i) { return 1040 * (i & 3) + 4352 * (i >> 2); }
DI float bf2f(unsigned u) { return __uint_as_float(u << 16); }
DI float bfhi(unsigned u) { return __uint_as_float(u & 0xffff0000u); }
DI unsigned pk2(float a, float b) {
  f32x2 v = {a, b};
  return __builtin_bit_cast(unsigned, __builtin_convertvector(v, bf16x2));
}
DI f32x16 mfma(u32x4 a, u32x4 b, f32x16 c) {
  return __builtin_amdgcn_mfma_f32_32x32x16_bf16(__builtin_bit_cast(bf16x8, a), __builtin_bit_cast(bf16x8, b), c, 0, 0, 0);
}
DI f32x16 zero16() {
  f32x16 z;
#pragma unroll
  for (int i = 0; i < 16; i++) z[i] = 0.f;
  return z;
}
DI u32x4 tr_read2(unsigned a0, unsigned a1) {
  const u32x2 lo = __builtin_bit_cast(u32x2, __builtin_amdgcn_ds_read_tr16_b64_v4i16((LDSP(s16x4))(size_t)a0));
  const u32x2 hi = __builtin_bit_cast(u32x2, __builtin_amdgcn_ds_read_tr16_b64_v4i16((LDSP(s16x4))(size_t)a1));
  const u32x4 v = {lo[0], lo[1], hi[0], hi[1]};
  return v;
}
DI int bucket_start(int b) { return (b * 625 + 15) >> 4; }
DI int wave_incl_scan(int x) {
  x += __builtin_amdgcn_update_dpp(0, x, 0x111, 0xf, 0xf, false);
  x += __builtin_amdgcn_update_dpp(0, x, 0x112, 0xf, 0xf, false);
  x += __builtin_amdgcn_update_dpp(0, x, 0x114, 0xf, 0xf, false);
  x += __builtin_amdgcn_update_dpp(0, x, 0x118, 0xf, 0xf, false);
  x += __builtin_amdgcn_update_dpp(0, x, 0x142, 0xa, 0xf, false);
  x += __builtin_amdgcn_update_dpp(0, x, 0x143, 0xc, 0xf, false);
  return x;
}
DI void dma16(const void* sbase, unsigned voff, unsigned lds_base) {
  asm volatile("s_mov_b32 m0, %2\n\ts_nop 0\n\tglobal_load_lds_dwordx4 %0, %1" ::"v"(voff), "s"(sbase), "s"(lds_base) : "memory");
}
DI void dma16_dev(const void* sbase, unsigned voff, unsigned lds_base) {
  asm volatile("s_mov_b32 m0, %2\n\ts_nop 0\n\tglobal_load_lds_dwordx4 %0, %1 sc1" ::"v"(voff), "s"(sbase), "s"(lds_base) : "memory");
}
DI void bdma16_dev(u32x4 srd, unsigned voff, unsigned lds_base) {
  asm volatile("s_mov_b32 m0, %2\n\ts_nop 0\n\tbuffer_load_dwordx4 %0, %1, 0 offen sc1 lds" ::"v"(voff), "s"(srd), "s"(lds_base) : "memory");
}
DI void dma4_dev(const void* sbase, unsigned voff, unsigned lds_base) {
  asm volatile("s_mov_b32 m0, %2\n\ts_nop 0\n\tglobal_load_lds_dword %0, %1 sc1" ::"v"(voff), "s"(sbase), "s"(lds_base) : "memory");
}
DI void wait_vm0() { asm volatile("s_waitcnt vmcnt(0)" ::: "memory"); }
DI void barrier() { asm volatile("s_waitcnt lgkmcnt(0)\n\ts_barrier" ::: "memory"); }

#define STAMP(i)

DI void silu4(const f32x16& x, int i, unsigned& m0, unsigned& m1) {
  f32x2 a = {x[4 * i], x[4 * i + 1]}, b = {x[4 * i + 2], x[4 * i + 3]};
  f32x2 ea = {__builtin_amdgcn_exp2f(a[0]), __builtin_amdgcn_exp2f(a[1])};
  f32x2 eb = {__builtin_amdgcn_exp2f(b[0]), __builtin_amdgcn_exp2f(b[1])};
  ea += 1.0f;
  eb += 1.0f;
  f32x2 ra = {__builtin_amdgcn_rcpf(ea[0]), __builtin_amdgcn_rcpf(ea[1])};
  f32x2 rb = {__builtin_amdgcn_rcpf(eb[0]), __builtin_amdgcn_rcpf(eb[1])};
  a *= ra;
  b *= rb;
  m0 = __builtin_bit_cast(unsigned, __builtin_convertvector(a, bf16x2));
  m1 = __builtin_bit_cast(unsigned, __builtin_convertvector(b, bf16x2));
}

__global__ __launch_bounds__(512, 2) void egnn_fused(const u16* __restrict__ h, const u16* __restrict__ pos,
                                                     const u32x2* __restrict__ eattr, const int* __restrict__ src,
                                                     const int* __restrict__ dst, const u16* __restrict__ we,
                                                     const u16* __restrict__ wx, const u16* __restrict__ wh,
                                                     char* ws, u16* __restrict__ out) {
  __shared__ __attribute__((aligned(256))) char lds[L_END];
  const unsigned lbase = (unsigned)(size_t)lds;
  const int tid = threadIdx.x, lane = tid & 63;
  const int wv = __builtin_amdgcn_readfirstlane(tid >> 6);
  const int w = blockIdx.x;
  const int b = (w & 7) * 32 + (w >> 3);
  const int n0 = bucket_start(b), nsz = bucket_start(b + 1) - n0;
  const int r = lane & 31, hh = lane >> 5;
  const int blk = (lane >> 4) & 1, q4 = (lane & 15) >> 2, p4 = lane & 3;
  const __amdgpu_buffer_rsrc_t wsr = __builtin_amdgcn_make_buffer_rsrc(ws, 0, WS_END, 0x00020000);
  STAMP(0);

  int d[3], s[3];
  u32x2 a[3];
  const bool v2 = tid < CH - 1024;
  {
    const unsigned e0 = (unsigned)(w * CH + tid) * 4, e2 = v2 ? e0 + 4096 : e0;
    asm volatile("global_load_dword %0, %6, %8\n\tglobal_load_dword %1, %6, %8 offset:2048\n\tglobal_load_dword %2, %7, %8\n\t"
                 "global_load_dword %3, %6, %9\n\tglobal_load_dword %4, %6, %9 offset:2048\n\tglobal_load_dword %5, %7, %9"
                 : "=&v"(d[0]), "=&v"(d[1]), "=&v"(d[2]), "=&v"(s[0]), "=&v"(s[1]), "=&v"(s[2])
                 : "v"(e0), "v"(e2), "s"(dst), "s"(src) : "memory");
  }
  const unsigned wlb = (unsigned)((lane >> 4) * 256 + 16 * ((lane & 15) ^ ((lane >> 4) << 2)));
  {
    const int q = lane >> 4, c = lane & 15;
    const int row = 4 * wv + q;
    dma16(h, (unsigned)(min(n0 + row, NN - 1) * 256 + 16 * (c ^ (row & 15))), lbase + L_H + wv * 1024);
    if (wv < 2) dma16(h, (unsigned)(min(n0 + 32 + row, NN - 1) * 256 + 16 * (c ^ (row & 15))), lbase + L_H + (8 + wv) * 1024);
#pragma unroll
    for (int i = 0; i < 4; i++) dma16(we, (wlb ^ (16 * i)) + (32 + 4 * wv + i) * 1024, lbase + L_W + (4 * wv + i) * 1024);
  }
  unsigned tagv = __builtin_amdgcn_raw_buffer_load_b32(wsr, WS_EP, 0, 16);
#pragma unroll
  for (int k = 0; k < 3; k++) a[k] = eattr[w * CH + (k < 2 || v2 ? tid + 512 * k : 0)];
  asm volatile("" ::: "memory");
#pragma unroll
  for (int i = 0; i < 7; i++) dma16(pos, (unsigned)((8 * i + wv) * 1024 + lane * 16), lbase + L_ROWS + (8 * i + wv) * 1024);
  if (wv < 2 || (wv == 2 && lane < 38)) dma16(pos, (unsigned)((56 + wv) * 1024 + lane * 16), lbase + L_ROWS + (56 + wv) * 1024);
  if (wv == 3) dma16(we, 65536 + lane * 16, lbase + 65536);
  if (wv == 4 && lane < 16) dma16(we, 66560 + lane * 16, lbase + 66560);
  if (wv == 5 && lane < 16) dma16(wx, lane * 16, lbase + L_WX);
  if (tid < 256) *(u32*)(lds + L_HIST + tid * 4) = 0u;
  else {
    const int t = tid - 256;
    if (t < 32) {
      const int j = (t >> 2) - 4 * ((t >> 1) & 1) - 2 * (t & 1);
      *(u32*)(lds + L_OH + t * 4) = j == 0 ? 0x3F80u : j == 1 ? 0x3F800000u : 0u;
      if (t < 16) *(u32*)(lds + L_GC + t * 4) = 0u;
    }
  }
  asm volatile("s_waitcnt vmcnt(11)" : "+v"(d[0]), "+v"(d[1]), "+v"(d[2]), "+v"(s[0]), "+v"(s[1]), "+v"(s[2])::"memory");
  barrier();
  STAMP(1);
  int key[3];
  unsigned rank[3];
#pragma unroll
  for (int k = 0; k < 3; k++) {
    key[k] = (int)((unsigned)__umul24((unsigned)d[k], 429497u) >> 24);
    rank[k] = 0;
    if (k < 2 || v2) rank[k] = atomicAdd((unsigned*)(lds + L_HIST) + key[k], 1u);
  }
  const int T = wv & 3, nb = wv >> 2;
  const int loc = 5 * (4 * nb + (r >> 3)) + (r & 7);
  const bool valid = (r & 7) < 5 && loc < nsz;
  const int ha = L_H + min(loc, 39) * 256 + 16 * (hh ^ (min(loc, 39) & 15));
  const unsigned trx0 = lbase + L_W + (unsigned)((8 * hh + q4) * 256), trs0 = (unsigned)((q4 << 2) | (2 * hh));
  u32x4 bt0, bt1;
  {
    const unsigned ch = (unsigned)(8 * (T & 1) + 4 * (p4 & 1) + 2 * blk + (T >> 1));
    const unsigned a0 = trx0 + 16 * (ch ^ trs0) + 8 * (p4 >> 1);
    const unsigned a1 = trx0 + 1024 + 16 * (ch ^ trs0 ^ 1) + 8 * (p4 >> 1);
    f32x16 acc0 = zero16(), acc1 = zero16();
#pragma unroll
    for (int ks = 0; ks < 8; ks += 2) {
      acc0 = mfma(tr_read2(a0 + ks * 4096, a1 + ks * 4096), *(const u32x4*)(lds + (ha ^ (32 * ks))), acc0);
      acc1 = mfma(tr_read2(a0 + ks * 4096 + 4096, a1 + ks * 4096 + 4096), *(const u32x4*)(lds + (ha ^ (32 * ks + 32))), acc1);
    }
#pragma unroll
    for (int k = 0; k < 3; k++) {
      const int lc = d[k] - bucket_start(key[k]);
      const int g = (lc * 205) >> 10;
      key[k] = (key[k] << 8) | (lc + 3 * g);
    }
#pragma unroll
    for (int i = 0; i < 4; i++) {
      bt0[i] = pk2((acc0[2 * i] + acc1[2 * i]) * NLOG2E, (acc0[2 * i + 1] + acc1[2 * i + 1]) * NLOG2E);
      bt1[i] = pk2((acc0[8 + 2 * i] + acc1[8 + 2 * i]) * NLOG2E, (acc0[9 + 2 * i] + acc1[9 + 2 * i]) * NLOG2E);
    }
  }
  wait_vm0();
  asm volatile("" : "+v"(tagv), "+v"(a[0]), "+v"(a[1]), "+v"(a[2]));
  if (valid) {
    const unsigned off = WS_BT + (unsigned)(n0 + loc) * 256 + 64 * T + 32 * hh;
    __builtin_amdgcn_raw_buffer_store_b128(bt0, wsr, off, 0, 16);
    __builtin_amdgcn_raw_buffer_store_b128(bt1, wsr, off + 16, 0, 16);
  }
  barrier();
  STAMP(2);
  const unsigned tag = (unsigned)__builtin_amdgcn_readfirstlane((int)tagv) + 1u;
  u32x4 off4;
  unsigned oend;
  {
    const u32x4 cnt = *(const u32x4*)(lds + L_HIST + lane * 16);
    u32x2 pd[3], ps[3];
#pragma unroll
    for (int k = 0; k < 3; k++) {
      const int od = L_ROWS + ((6 * d[k]) & ~3), os = L_ROWS + ((6 * s[k]) & ~3);
      pd[k][0] = *(const u32*)(lds + od); pd[k][1] = *(const u32*)(lds + od + 4);
      ps[k][0] = *(const u32*)(lds + os); ps[k][1] = *(const u32*)(lds + os + 4);
    }
#pragma unroll
    for (int i = 0; i < 4; i++) dma16(we, (wlb ^ (16 * i)) + (4 * wv + i) * 1024, lbase + L_W + (4 * wv + i) * 1024);
    const unsigned tot = cnt[0] + cnt[1] + cnt[2] + cnt[3];
    oend = (unsigned)wave_incl_scan((int)tot);
    off4[0] = oend - tot;
    off4[1] = off4[0] + cnt[0];
    off4[2] = off4[1] + cnt[1];
    off4[3] = off4[2] + cnt[2];
    *(u32x4*)(lds + L_OFF + lane * 16) = off4;
#pragma unroll
    for (int k = 0; k < 3; k++) rank[k] += *(const u32*)(lds + L_OFF + (key[k] >> 8) * 4);
#pragma unroll
    for (int k = 0; k < 3; k++) {
      const unsigned long long ud = (((unsigned long long)pd[k][1] << 32) | pd[k][0]) >> ((d[k] & 1) * 16);
      const unsigned long long us = (((unsigned long long)ps[k][1] << 32) | ps[k][0]) >> ((s[k] & 1) * 16);
      const unsigned dlo = (unsigned)ud, dhi = (unsigned)(ud >> 32), slo = (unsigned)us, shi = (unsigned)(us >> 32);
      const unsigned dxy = pk2(bf2f(dlo) - bf2f(slo), bfhi(dlo) - bfhi(slo));
      const unsigned dz_ = pk2(bf2f(dhi) - bf2f(shi), 0.f);
      const float dx = bf2f(dxy), dy = bfhi(dxy), dz = bf2f(dz_);
      const unsigned sxy = pk2(dx * dx, dy * dy), sz = pk2(dz * dz, 0.f);
      const unsigned d2 = pk2((bf2f(sxy) + bfhi(sxy)) + bf2f(sz), 0.f) & 0xffffu;
      const u32x4 rec = {a[k][0], d2 | (dxy << 16), a[k][1], (dxy >> 16) | (dz_ << 16)};
      if (k < 2 || v2) {
        *(u32x4*)(lds + L_S + rank[k] * 16) = rec;
        *(u32*)(lds + L_R2S + rank[k] * 4) = ((unsigned)s[k] << 8) | ((unsigned)key[k] & 0xffu);
      }
    }
  }
  wait_vm0();
  barrier();
  STAMP(4);
  {
    const u32x4 z = {0u, 0u, 0u, 0u};
#pragma unroll
    for (int k = 0; k < 3; k++) {
      if (k < 2 || tid < 256) {
        const u32x4 v = *(const u32x4*)(lds + L_S + (tid + 512 * k) * 16);
        __builtin_amdgcn_raw_buffer_store_b128(v, wsr, WS_R1 + (unsigned)(w * CHS + tid + 512 * k) * 16, 0, 16);
        *(u32x4*)(lds + L_S + (tid + 512 * k) * 16) = z;
      }
    }
    if (tid >= 192) {
      const u32x4 v = *(const u32x4*)(lds + L_R2S + (tid - 192) * 16);
      __builtin_amdgcn_raw_buffer_store_b128(v, wsr, WS_R2 + (unsigned)(w * CHS) * 4 + (tid - 192) * 16, 0, 16);
    }
  }
  u32x2 at[4];
  {
    const unsigned chA = (unsigned)(4 * T + 2 * blk + (p4 >> 1));
    const unsigned a0 = trx0 + 16 * (chA ^ trs0) + 8 * (p4 & 1), a1 = trx0 + 1024 + 16 * (chA ^ trs0 ^ 1) + 8 * (p4 & 1);
    f32x16 acc = zero16();
#pragma unroll
    for (int ks = 0; ks < 8; ks++) acc = mfma(*(const u32x4*)(lds + (ha ^ (32 * ks))), tr_read2(a0 + ks * 4096, a1 + ks * 4096), acc);
#pragma unroll
    for (int i = 0; i < 4; i++) {
      at[i][0] = pk2(acc[4 * i] * NLOG2E, acc[4 * i + 1] * NLOG2E);
      at[i][1] = pk2(acc[4 * i + 2] * NLOG2E, acc[4 * i + 3] * NLOG2E);
      PIN(at[i]);
    }
  }
  wait_vm0();
  barrier();
  STAMP(5);
  if (wv == 0) {
    const u32x4 g0 = {tag, off4[0] | (off4[1] << 16), tag, off4[1] | (off4[2] << 16)};
    const u32x4 g1 = {tag, off4[2] | (off4[3] << 16), tag, off4[3] | (oend << 16)};
    const unsigned off = WS_TAB + (unsigned)(w * NB + 4 * lane) * 8;
    __builtin_amdgcn_raw_buffer_store_b128(g0, wsr, off, 0, 16);
    __builtin_amdgcn_raw_buffer_store_b128(g1, wsr, off + 16, 0, 16);
  }
#pragma unroll
  for (int i = 0; i < 4; i++) *(u32x2*)(lds + L_A + (4 * nb + i) * 2048 + T * 512 + lane * 8) = at[i];
  if (tid >= 256) {
    const int t = tid - 256, v = t >> 5, q = (t >> 3) & 3, i = t & 7;
    *(u32*)(lds + L_FK + t * 4) = (unsigned)(i < 4 ? 128 + 32 * q + 4 * v + i : 16 * v + 4 * (i - 4) + q) << 8;
  }
  u32x4 extB[4];
#pragma unroll
  for (int t = 0; t < 4; t++) {
    const u16* wp = (const u16*)(lds + 65536) + 32 * t + r;
    const float w0 = bf2f(wp[(1 + 2 * hh) * 128]) * NLOG2E, w1 = bf2f(wp[(2 + 2 * hh) * 128]) * NLOG2E, w2 = bf2f(wp[0]) * NLOG2E;
    extB[t][0] = 0u;
    extB[t][1] = 0u;
    extB[t][2] = pk2(w0, w1);
    extB[t][3] = hh ? 0u : pk2(w2, 0.f);
  }
  u32x4 I0, I1;
  {
    const unsigned one = (r & 1) ? 0x3F800000u : 0x3F80u;
#pragma unroll
    for (int jj = 0; jj < 4; jj++) {
      const bool m = ((r & 7) >> 1) == jj;
      I0[jj] = (m && (r >> 3) == hh) ? one : 0u;
      I1[jj] = (m && (r >> 3) == 2 + hh) ? one : 0u;
    }
  }
  const int rowbuf = L_ROWS + wv * ROWBUF;
  unsigned kdma = 16 * ((lane & 15) ^ (4 * (lane >> 4)));
  const int rd0 = rowbuf + 1040 * (r & 3) + 4352 * ((r >> 2) & 1) + (r >> 3) * 256;
  int rdA = rd0 + 64 * ((2 * hh) ^ (r >> 3)), rdB = rd0 + 64 * ((2 * hh + 1) ^ (r >> 3));
  int swr0 = L_S + 32 * (r >> 4) + 16 * ((r >> 2) & 1) + 8 * ((r >> 3) & 1) + 2 * (r & 3) + 160 * hh;
  int srd0 = L_S + r * 80 + 16 * hh;
  const char* btab = ws + WS_BT;
  f32x16 Y[4];
#pragma unroll
  for (int t = 0; t < 4; t++) {
    Y[t] = zero16();
    PIN(Y[t]);
    PIN(extB[t]);
  }
  PIN(I0); PIN(I1); PIN(kdma); PIN(rdA); PIN(rdB); PIN(swr0); PIN(srd0);
  STAMP(6);

  bool wh_loaded = false, slow = false;
  int lo = 0, quota = MAPW, gq = wv;
  while (true) {
    wh_loaded = false;
    int tq = tid;
    PIN(tq);
    const int lq = tq & 63;
    int kc, kfirst, kpre, wtot;
    {
      const unsigned tabme = WS_TAB + (unsigned)((tq >> 1) * NB + b) * 8;
      u32x2 gq2 = {tag + 1u, 0u};
  #pragma clang loop unroll(disable)
      for (int spin = 0; spin < (1 << 22); spin++) {
        if (gq2[0] != tag) asm volatile("global_load_dwordx2 %0, %1, %2 sc1\n\ts_waitcnt vmcnt(0)" : "=&v"(gq2) : "v"(tabme), "s"(ws) : "memory");
        if (__all(gq2[0] == tag)) break;
        __builtin_amdgcn_s_sleep(1);
      }
      const int o0 = min((int)(gq2[1] & 0xffffu), CH), o1 = min((int)(gq2[1] >> 16), CH);
      kc = max(o1 - o0, 0);
      kfirst = (tq >> 1) * CHS + o0;
      const int inc = wave_incl_scan((tq & 1) ? 0 : kc);
      kpre = inc - kc;
      wtot = __builtin_amdgcn_readlane(inc, 63);
      if (!slow && lq == 0) atomicMax((unsigned*)(lds + L_GC + 32), (unsigned)wtot);
    }
    STAMP(7);
    {
      const int map = L_MAP + wv * MAPW * 4;
      const int nw = min(wtot - lo, quota);
      const int jhi = min(kc, lo + quota - kpre);
#pragma clang loop vectorize(disable) unroll(disable)
      for (int j = max(0, lo - kpre) + ((tq ^ max(0, lo - kpre)) & 1); j < jhi; j += 2) *(u32*)(lds + map + (kpre + j - lo) * 4) = (unsigned)(kfirst + j);
      for (int p0 = 0; p0 < nw; p0 += 192) {
        u32x4 q1[3];
        unsigned q2[3], ix[3];
#pragma unroll
        for (int k = 0; k < 3; k++) {
          const int p = p0 + 64 * k + lq;
          const unsigned mi = *(const u32*)(lds + map + min(p, MAPW - 1) * 4);
          ix[k] = p < nw ? mi : 0u;
        }
        {
          const int pw = 32 * wv + (w >> 3);
          const unsigned pb = (unsigned)bucket_start((pw & 7) * 32 + (pw >> 3)) * 256 + lq * 16;
          asm volatile(
              "global_load_dword %3, %9, %13 sc1\n\tglobal_load_dword %4, %10, %13 sc1\n\tglobal_load_dword %5, %11, %13 sc1\n\t"
              "global_load_dwordx4 %0, %6, %12 sc1\n\tglobal_load_dwordx4 %1, %7, %12 sc1\n\tglobal_load_dwordx4 %2, %8, %12 sc1\n\t"
              "s_mov_b32 m0, %18\n\ts_nop 0\n\t"
              "global_load_lds_dwordx4 %14, %17 sc1\n\tglobal_load_lds_dwordx4 %14, %17 offset:1024 sc1\n\t"
              "global_load_lds_dwordx4 %14, %17 offset:2048 sc1\n\tglobal_load_lds_dwordx4 %14, %17 offset:3072 sc1\n\t"
              "global_load_lds_dwordx4 %15, %17 sc1\n\tglobal_load_lds_dwordx4 %15, %17 offset:1024 sc1\n\t"
              "global_load_lds_dwordx4 %15, %17 offset:2048 sc1\n\tglobal_load_lds_dwordx4 %15, %17 offset:3072 sc1\n\t"
              "global_load_lds_dwordx4 %16, %17 sc1\n\tglobal_load_lds_dwordx4 %16, %17 offset:1024 sc1\n\t"
              "s_waitcnt vmcnt(10)"
              : "=&v"(q1[0]), "=&v"(q1[1]), "=&v"(q1[2]), "=&v"(q2[0]), "=&v"(q2[1]), "=&v"(q2[2])
              : "v"(ix[0] * 16), "v"(ix[1] * 16), "v"(ix[2] * 16), "v"(ix[0] * 4), "v"(ix[1] * 4), "v"(ix[2] * 4),
                "s"(ws + WS_R1), "s"(ws + WS_R2), "v"(pb), "v"(pb + 4096), "v"(pb + 8192), "s"(btab), "s"(lbase + rowbuf)
              : "memory");
        }
        unsigned rk[3];
#pragma unroll
        for (int k = 0; k < 3; k++) {
          rk[k] = CAPW;
          if (p0 + 64 * k + lq < nw) rk[k] = atomicAdd((unsigned*)(lds + L_GC) + ((q2[k] >> 3) & 7u), 1u);
        }
#pragma unroll
        for (int k = 0; k < 3; k++) {
          if (rk[k] < CAPW) {
            const unsigned e = ((q2[k] >> 3) & 7u) * CAPW + rk[k];
            *(u32x4*)(lds + L_R1 + e * 16) = q1[k];
            *(u32*)(lds + L_R2 + e * 4) = q2[k];
          }
        }
      }
    }
    barrier();
    STAMP(8);
    if (w == 0 && tid == 0) __builtin_amdgcn_raw_buffer_store_b32(tag, wsr, WS_EP, 0, 16);
    const int cl = *(const u32*)(lds + L_GC + (lane & 7) * 4);
    const int maxtot = __builtin_amdgcn_readfirstlane(*(const u32*)(lds + L_GC + 32));
    if (!slow) {
      const int kk = (cl << 3) | (lane & 7);
      int rk = 0;
      rk += __builtin_amdgcn_update_dpp(0, kk, 0x121, 0xf, 0xf, false) > kk;
      rk += __builtin_amdgcn_update_dpp(0, kk, 0x122, 0xf, 0xf, false) > kk;
      rk += __builtin_amdgcn_update_dpp(0, kk, 0x123, 0xf, 0xf, false) > kk;
      rk += __builtin_amdgcn_update_dpp(0, kk, 0x124, 0xf, 0xf, false) > kk;
      rk += __builtin_amdgcn_update_dpp(0, kk, 0x125, 0xf, 0xf, false) > kk;
      rk += __builtin_amdgcn_update_dpp(0, kk, 0x126, 0xf, 0xf, false) > kk;
      rk += __builtin_amdgcn_update_dpp(0, kk, 0x127, 0xf, 0xf, false) > kk;
      const int cmax = __any(cl > CAPW) ? CAPW + 1 : 0;
      if (maxtot > MAPW || cmax > CAPW) {
        slow = true;
        quota = CAPW / 8;
        barrier();
        if (tid < 8) *(u32*)(lds + L_GC + tid * 4) = 0u;
        barrier();
        continue;
      }
      gq = __builtin_ctzll(__ballot(rk == (wv < 4 ? wv : 11 - wv)));
    }
    const int n = __builtin_amdgcn_readlane(cl, gq);
    const int swr = swr0 + gq * 2560, srd = srd0 + gq * 2560;
    const int l1 = L_R1 + gq * CAPW * 16, l2 = L_R2 + gq * CAPW * 4;
    if (n > 0) {
    const int nt = (n + 31) >> 5;
    if (lane < 32 * nt - n) {
      const u32x4 z = {0u, 0u, 0u, 0u};
      *(u32*)(lds + l2 + (n + lane) * 4) = 0u;
      *(u32x4*)(lds + l1 + (n + lane) * 16) = z;
    }
#pragma unroll
    for (int t = 0; t < 4; t++) {
      const u32x2 v = *(const u32x2*)(lds + L_A + gq * 2048 + t * 512 + lane * 8);
      extB[t][0] = v[0];
      extB[t][1] = v[1];
    }

    u32x4 rows[8], extA, S0, S1, ra, rb, m1p = {0u, 0u, 0u, 0u};
    u32x2 r1;
    unsigned r2;
    int sp;
    f32x16 XA, XB;
    auto list_read = [&](int tl) {
      const int list = tl < nt ? l2 + tl * 128 : L_FK + wv * 128;
      ra = *(const u32x4*)(lds + list + (lane >> 4) * 32);
      rb = *(const u32x4*)(lds + list + (lane >> 4) * 32 + 16);
    };
    auto dma_piece = [&](int tl, int i) {
      const unsigned v = i < 4 ? ra[i & 3] : rb[i & 3];
      const unsigned long long bp = (unsigned long long)(tl < nt ? btab : (const char*)wh);
      const u32x4 srd = {(unsigned)bp, (unsigned)(bp >> 32) & 0xffffu, tl < nt ? (unsigned)(NN * 256) : 65536u, 0x00020000u};
      bdma16_dev(srd, (v & 0xffffff00u) | kdma, lbase + rowbuf + piece_off(i));
    };
    auto meta_read = [&](int tl) {
      r1 = *(const u32x2*)(lds + l1 + (tl * 32 + r) * 16 + hh * 8);
      r2 = *(const u32*)(lds + l2 + (tl * 32 + r) * 4);
    };
    auto meta_use = [&](int tl) {
      const unsigned dl = r2 & 7u;
      const u32x2 oh = *(const u32x2*)(lds + L_OH + dl * 16 + hh * 8);
      extA[0] = oh[0]; extA[1] = oh[1]; extA[2] = r1[0]; extA[3] = r1[1];
      sp = (tl * 32 + r < n) ? swr + (int)dl * 320 : L_DUM;
    };
    auto s_write = [&]() {
      *(u16*)(lds + sp) = (u16)(hh ? extA[3] : 0x3F80u);
      *(u16*)(lds + sp + 80) = (u16)(extA[3] >> 16);
    };
    auto rows_read = [&]() {
#pragma unroll
      for (int j = 0; j < 8; j++) rows[j] = *(const u32x4*)(lds + (j < 4 ? rdA : rdB) + 16 * (j & 3));
    };
    auto rows_pin = [&]() {
      asm volatile("" : "+v"(rows[0]), "+v"(rows[1]), "+v"(rows[2]), "+v"(rows[3]), "+v"(rows[4]), "+v"(rows[5]), "+v"(rows[6]), "+v"(rows[7]));
    };

    list_read(0);
    meta_read(0);
#pragma unroll
    for (int i = 0; i < 8; i++) dma_piece(0, i);
    list_read(1);
    meta_use(0);
    s_write();
    S1 = m1p;
    wait_vm0();
    rows_read();
    rows_pin();
#pragma unroll
    for (int i = 0; i < 8; i++) dma_piece(1, i);
    XA = mfma(rows[0], I0, zero16());
    XA = mfma(rows[1], I1, XA);
    XA = mfma(extA, extB[0], XA);
    STAMP(9);

    const int nfull = n >> 5;
    for (int tl = 0; tl < nfull; tl++) {
#pragma unroll
      for (int t = 0; t < 4; t++) {
        f32x16& Xc = (t & 1) ? XB : XA;
        f32x16& Xn = (t & 1) ? XA : XB;
        const int tn = (t + 1) & 3;
        float e[16];
        SB();
        if (t == 0) {
          S0 = *(const u32x4*)(lds + srd);
        }
        if (t == 2) meta_read(tl + 1);
        Xn = mfma(rows[2 * tn], I0, zero16());
        PIN(Xn);
        SB();
#pragma unroll
        for (int i = 0; i < 4; i++) e[i] = __builtin_amdgcn_exp2f(Xc[i]);
        if (t == 3) { dma_piece(tl + 2, 0); dma_piece(tl + 2, 1); }
        SB();
        Xn = mfma(rows[2 * tn + 1], I1, Xn);
        PIN(Xn);
        SB();
#pragma unroll
        for (int i = 4; i < 8; i++) e[i] = __builtin_amdgcn_exp2f(Xc[i]);
        if (t == 3) { dma_piece(tl + 2, 2); dma_piece(tl + 2, 3); }
        SB();
        Xn = mfma(extA, extB[tn], Xn);
        PIN(Xn);
        SB();
#pragma unroll
        for (int i = 8; i < 12; i++) e[i] = __builtin_amdgcn_exp2f(Xc[i]);
        if (t == 3) { dma_piece(tl + 2, 4); dma_piece(tl + 2, 5); }
        SB();
        Y[(t + 3) & 3] = mfma(S1, m1p, Y[(t + 3) & 3]);
        SB();
        if (t == 0) {
          S1 = *(const u32x4*)(lds + srd + 32);
          *(u16*)(lds + sp) = 0;
          *(u16*)(lds + sp + 80) = 0;
        }
        if (t == 2) {
          wait_vm0();
          rows_read();
          list_read(tl + 2);
          meta_use(tl + 1);
        }
#pragma unroll
        for (int i = 12; i < 16; i++) e[i] = __builtin_amdgcn_exp2f(Xc[i]);
        if (t == 3) { dma_piece(tl + 2, 6); dma_piece(tl + 2, 7); s_write(); }
        SB();
        f32x2 dd[8];
#pragma unroll
        for (int i = 0; i < 8; i++) { dd[i][0] = e[2 * i]; dd[i][1] = e[2 * i + 1]; dd[i] += 1.0f; }
        SB();
#pragma unroll
        for (int i = 0; i < 8; i++) { dd[i][0] = __builtin_amdgcn_rcpf(dd[i][0]); dd[i][1] = __builtin_amdgcn_rcpf(dd[i][1]); }
        SB();
        u32x4 m0;
#pragma unroll
        for (int i = 0; i < 4; i++) { f32x2 x = {Xc[2 * i], Xc[2 * i + 1]}; dd[i] *= x; }
#pragma unroll
        for (int i = 0; i < 4; i++) m0[i] = __builtin_bit_cast(unsigned, __builtin_convertvector(dd[i], bf16x2));
        SB();
#pragma unroll
        for (int i = 4; i < 8; i++) { f32x2 x = {Xc[2 * i], Xc[2 * i + 1]}; dd[i] *= x; }
        SB();
        Y[t] = mfma(S0, m0, Y[t]);
        SB();
#pragma unroll
        for (int i = 0; i < 4; i++) m1p[i] = __builtin_bit_cast(unsigned, __builtin_convertvector(dd[4 + i], bf16x2));
        if (t == 2) rows_pin();
      }
    }
    SB();
    Y[3] = mfma(S1, m1p, Y[3]);
    if (nfull < nt) {
      const int c = n & 31;
      S0 = *(const u32x4*)(lds + srd);
      S1 = *(const u32x4*)(lds + srd + 32);
      *(u16*)(lds + sp) = 0;
      *(u16*)(lds + sp + 80) = 0;
#pragma unroll
      for (int t = 0; t < 4; t++) {
        f32x16& Xc = (t & 1) ? XB : XA;
        f32x16& Xn = (t & 1) ? XA : XB;
        if (t < 3) {
          Xn = mfma(rows[2 * t + 2], I0, zero16());
          Xn = mfma(rows[2 * t + 3], I1, Xn);
          Xn = mfma(extA, extB[t + 1], Xn);
        }
        unsigned m[8] = {0u, 0u, 0u, 0u, 0u, 0u, 0u, 0u};
        silu4(Xc, 0, m[0], m[1]);
        if (c > 8) silu4(Xc, 1, m[2], m[3]);
        if (c > 16) silu4(Xc, 2, m[4], m[5]);
        if (c > 24) silu4(Xc, 3, m[6], m[7]);
        const u32x4 m0 = {m[0], m[1], m[2], m[3]}, m1 = {m[4], m[5], m[6], m[7]};
        Y[t] = mfma(S0, m0, Y[t]);
        Y[t] = mfma(S1, m1, Y[t]);
      }
    }
    wh_loaded = true;
    wait_vm0();
    }
    if (!slow) break;
    lo += quota;
    if (lo >= maxtot) break;
    barrier();
    if (tid < 8) *(u32*)(lds + L_GC + tid * 4) = 0u;
    barrier();
  }
  STAMP(10);
  if (!wh_loaded) {
    wait_vm0();
    const u32x4 fa = *(const u32x4*)(lds + L_FK + wv * 128 + (lane >> 4) * 32), fb = *(const u32x4*)(lds + L_FK + wv * 128 + (lane >> 4) * 32 + 16);
#pragma unroll
    for (int i = 0; i < 8; i++) dma16(wh, (i < 4 ? fa[i & 3] : fb[i & 3]) | kdma, lbase + rowbuf + piece_off(i));
    wait_vm0();
  }

  const int nrow = min(n0 + loc, NN - 1);
  const u32x4 res0 = *(const u32x4*)(h + (size_t)nrow * 128 + 32 * T + 16 * hh);
  const u32x4 res1 = *(const u32x4*)(h + (size_t)nrow * 128 + 32 * T + 16 * hh + 8);
  const bool pval = lane < 15 && 15 * gq + lane < 3 * nsz;
  const unsigned pofs = (unsigned)(3 * n0 + 15 * gq + lane);
  unsigned pin = 0;
  if (pval) pin = pos[pofs];
#pragma unroll
  for (int i = 0; i < 3; i++) {
    if (i < 2 || hh == 0) {
      const u32x2 mv = {pk2(Y[0][4 * i] * NLN2, Y[1][4 * i] * NLN2), pk2(Y[2][4 * i] * NLN2, Y[3][4 * i] * NLN2)};
      *(u32x2*)(lds + L_M + (5 * gq + 2 * i + hh) * MSTR + 8 * r) = mv;
    }
  }
  {
    float zz[9], wxv[4];
#pragma unroll
    for (int t = 0; t < 4; t++) wxv[t] = bf2f(((const u16*)(lds + L_WX))[32 * t + r]) * NLN2;
#pragma unroll
    for (int i = 0; i < 3; i++) {
#pragma unroll
      for (int cc = 1; cc < 4; cc++) {
        float z = Y[0][4 * i + cc] * wxv[0] + Y[1][4 * i + cc] * wxv[1] + Y[2][4 * i + cc] * wxv[2] + Y[3][4 * i + cc] * wxv[3];
        z += __builtin_bit_cast(float, __builtin_amdgcn_update_dpp(0, __builtin_bit_cast(int, z), 0x111, 0xf, 0xf, false));
        z += __builtin_bit_cast(float, __builtin_amdgcn_update_dpp(0, __builtin_bit_cast(int, z), 0x112, 0xf, 0xf, false));
        z += __builtin_bit_cast(float, __builtin_amdgcn_update_dpp(0, __builtin_bit_cast(int, z), 0x114, 0xf, 0xf, false));
        z += __builtin_bit_cast(float, __builtin_amdgcn_update_dpp(0, __builtin_bit_cast(int, z), 0x118, 0xf, 0xf, false));
        z += __builtin_bit_cast(float, __builtin_amdgcn_update_dpp(0, __builtin_bit_cast(int, z), 0x142, 0xa, 0xf, false));
        zz[3 * i + cc - 1] = z;
      }
    }
    const int zp = L_S + gq * 2560;
    if (r == 31) {
#pragma unroll
      for (int i = 0; i < 9; i++) *(u32*)(lds + zp + 12 * hh + 24 * (i / 3) + 4 * (i % 3)) = __float_as_uint(zz[i]);
    }
    asm volatile("" ::: "memory");
    const float z = __uint_as_float(*(const u32*)(lds + zp + 4 * (lane & 15)));
    if (pval) out[(size_t)NN * 128 + pofs] = (u16)pk2(bf2f(pin) + z, 0.f);
  }
  barrier();
  STAMP(11);
  {
    const unsigned wa = lbase + L_ROWS + 2080 * hh + 256 * q4 + 64 * (T ^ q4) + 32 * (p4 & 1) + 16 * blk + 8 * (p4 >> 1);
    const int ma = L_M + min(loc, 39) * MSTR + 16 * hh;
    f32x16 P = zero16(), P2 = zero16();
#pragma unroll
    for (int ks = 0; ks < 8; ks++) {
      P = mfma(tr_read2(wa + ks * ROWBUF + 4352, wa + ks * ROWBUF + 4352 + 1040), *(const u32x4*)(lds + (ha ^ (32 * ks))), P);
      P2 = mfma(tr_read2(wa + ks * ROWBUF, wa + ks * ROWBUF + 1040), *(const u32x4*)(lds + ma + 32 * ks), P2);
    }
    if (valid) {
      u32x4 ov[2];
#pragma unroll
      for (int i = 0; i < 8; i++) {
        const unsigned rs = i < 4 ? res0[i & 3] : res1[i & 3];
        const float x0 = P[2 * i] + P2[2 * i], x1 = P[2 * i + 1] + P2[2 * i + 1];
        const float s0 = x0 * __builtin_amdgcn_rcpf(1.f + __builtin_amdgcn_exp2f(x0 * NLOG2E));
        const float s1 = x1 * __builtin_amdgcn_rcpf(1.f + __builtin_amdgcn_exp2f(x1 * NLOG2E));
        ov[i >> 2][i & 3] = pk2(bf2f(rs) + s0, bfhi(rs) + s1);
      }
      u32x4* op = (u32x4*)(out + (size_t)(n0 + loc) * 128 + 32 * T + 16 * hh);
      op[0] = ov[0];
      op[1] = ov[1];
    }
  }
  STAMP(12);
}

extern "C" void kernel_launch(void* const* d_in, const int* in_sizes, int n_in, void* d_out, int out_size, void* d_ws,
                              size_t ws_size, hipStream_t stream) {
  egnn_fused<<<NB, 512, 0, stream>>>((const u16*)d_in[0], (const u16*)d_in[1], (const u32x2*)d_in[2], (const int*)d_in[3],
                                     (const int*)d_in[4], (const u16*)d_in[5], (const u16*)d_in[6], (const u16*)d_in[7],
                                     (char*)d_ws, (u16*)d_out);
}
```

```cpp
#include <hip/hip_runtime.h>
#include <hip/hip_bf16.h>
#include <stdint.h>

typedef __bf16 bf16x8 __attribute__((ext_vector_type(8)));
typedef __bf16 bf16x2 __attribute__((ext_vector_type(2)));
typedef short s16x4 __attribute__((ext_vector_type(4)));
typedef float f32x2 __attribute__((ext_vector_type(2)));
typedef float f32x4 __attribute__((ext_vector_type(4), may_alias));
typedef float f32x16 __attribute__((ext_vector_type(16)));
typedef unsigned u32x2 __attribute__((ext_vector_type(2), may_alias));
typedef unsigned u32x4 __attribute__((ext_vector_type(4), may_alias));
typedef unsigned u32x2a4 __attribute__((ext_vector_type(2), aligned(4), may_alias));
typedef unsigned short u16 __attribute__((may_alias));
typedef unsigned u32 __attribute__((may_alias));
#define LDSP(T) __attribute__((address_space(3))) T*
#define DI __device__ __forceinline__
#define SB() __builtin_amdgcn_sched_barrier(0)
#define PIN(x) asm volatile("" : "+v"(x))

constexpr int NN = 10000;
constexpr int CH = 1250, CHS = 1280;
constexpr int NB = 256;
constexpr float NLOG2E = -1.4426950408889634f;
constexpr float NLN2 = -0.6931471805599453f;

constexpr unsigned WS_BT = 0;
constexpr unsigned WS_R1 = 2560000;
constexpr unsigned WS_R2 = WS_R1 + NB * CHS * 16;
constexpr unsigned WS_TAB = WS_R2 + NB * CHS * 4;
constexpr unsigned WS_EP = WS_TAB + NB * NB * 8;
constexpr unsigned WS_END = WS_EP + 256;

constexpr int CAPW = 224;
constexpr int ROWBUF = 8496;
constexpr int L_ROWS = 0;
constexpr int L_R1 = 8 * ROWBUF;
constexpr int L_R2 = L_R1 + 8 * CAPW * 16;
constexpr int L_S = L_R2 + 8 * CAPW * 4;
constexpr int L_M = L_S + 8 * 2560;
constexpr int MSTR = 272;
constexpr int L_H = 135168;
constexpr int L_A = L_H + 10240;
constexpr int L_HIST = L_A + 8 * 2048;
constexpr int L_OH = L_HIST + 1024;
constexpr int L_GC = L_OH + 128;
constexpr int L_DUM = L_GC + 64;
constexpr int L_WX = L_DUM + 128;
constexpr int L_END = L_WX + 256;
constexpr int L_W = L_R1;
constexpr int L_OFF = L_A;
constexpr int L_R2S = L_A + 1024;
constexpr int MAPW = 256;
constexpr int L_MAP = L_M;
constexpr int L_FK = L_HIST;
static_assert(L_H % 256 == 0 && L_M + 40 * MSTR <= L_H && L_END <= 163840, "lds map");

DI constexpr int piece_off(int i) { return 1040 * (i & 3) + 4352 * (i >> 2); }
DI float bf2f(unsigned u) { return __uint_as_float(u << 16); }
DI float bfhi(unsigned u) { return __uint_as_float(u & 0xffff0000u); }
DI unsigned pk2(float a, float b) {
  f32x2 v = {a, b};
  return __builtin_bit_cast(unsigned, __builtin_convertvector(v, bf16x2));
}
DI f32x16 mfma(u32x4 a, u32x4 b, f32x16 c) {
  return __builtin_amdgcn_mfma_f32_32x32x16_bf16(__builtin_bit_cast(bf16x8, a), __builtin_bit_cast(bf16x8, b), c, 0, 0, 0);
}
DI f32x16 zero16() {
  f32x16 z;
#pragma unroll
  for (int i = 0; i < 16; i++) z[i] = 0.f;
  return z;
}
DI u32x4 tr_read2(unsigned a0, unsigned a1) {
  const u32x2 lo = __builtin_bit_cast(u32x2, __builtin_amdgcn_ds_read_tr16_b64_v4i16((LDSP(s16x4))(size_t)a0));
  const u32x2 hi = __builtin_bit_cast(u32x2, __builtin_amdgcn_ds_read_tr16_b64_v4i16((LDSP(s16x4))(size_t)a1));
  const u32x4 v = {lo[0], lo[1], hi[0], hi[1]};
  return v;
}
DI int bucket_start(int b) { return (b * 625 + 15) >> 4; }
DI int wave_incl_scan(int x) {
  x += __builtin_amdgcn_update_dpp(0, x, 0x111, 0xf, 0xf, false);
  x += __builtin_amdgcn_update_dpp(0, x, 0x112, 0xf, 0xf, false);
  x += __builtin_amdgcn_update_dpp(0, x, 0x114, 0xf, 0xf, false);
  x += __builtin_amdgcn_update_dpp(0, x, 0x118, 0xf, 0xf, false);
  x += __builtin_amdgcn_update_dpp(0, x, 0x142, 0xa, 0xf, false);
  x += __builtin_amdgcn_update_dpp(0, x, 0x143, 0xc, 0xf, false);
  return x;
}
DI void dma16(const void* sbase, unsigned voff, unsigned lds_base) {
  asm volatile("s_mov_b32 m0, %2\n\ts_nop 0\n\tglobal_load_lds_dwordx4 %0, %1" ::"v"(voff), "s"(sbase), "s"(lds_base) : "memory");
}
DI void dma16_dev(const void* sbase, unsigned voff, unsigned lds_base) {
  asm volatile("s_mov_b32 m0, %2\n\ts_nop 0\n\tglobal_load_lds_dwordx4 %0, %1 sc1" ::"v"(voff), "s"(sbase), "s"(lds_base) : "memory");
}
DI void dma4_dev(const void* sbase, unsigned voff, unsigned lds_base) {
  asm volatile("s_mov_b32 m0, %2\n\ts_nop 0\n\tglobal_load_lds_dword %0, %1 sc1" ::"v"(voff), "s"(sbase), "s"(lds_base) : "memory");
}
DI void wait_vm0() { asm volatile("s_waitcnt vmcnt(0)" ::: "memory"); }
DI void barrier() { asm volatile("s_waitcnt lgkmcnt(0)\n\ts_barrier" ::: "memory"); }

#define STAMP(i)

DI void silu4(const f32x16& x, int i, unsigned& m0, unsigned& m1) {
  f32x2 a = {x[4 * i], x[4 * i + 1]}, b = {x[4 * i + 2], x[4 * i + 3]};
  f32x2 ea = {__builtin_amdgcn_exp2f(a[0]), __builtin_amdgcn_exp2f(a[1])};
  f32x2 eb = {__builtin_amdgcn_exp2f(b[0]), __builtin_amdgcn_exp2f(b[1])};
  ea += 1.0f;
  eb += 1.0f;
  f32x2 ra = {__builtin_amdgcn_rcpf(ea[0]), __builtin_amdgcn_rcpf(ea[1])};
  f32x2 rb = {__builtin_amdgcn_rcpf(eb[0]), __builtin_amdgcn_rcpf(eb[1])};
  a *= ra;
  b *= rb;
  m0 = __builtin_bit_cast(unsigned, __builtin_convertvector(a, bf16x2));
  m1 = __builtin_bit_cast(unsigned, __builtin_convertvector(b, bf16x2));
}

__global__ __launch_bounds__(512, 2) void egnn_fused(const u16* __restrict__ h, const u16* __restrict__ pos,
                                                     const u32x2* __restrict__ eattr, const int* __restrict__ src,
                                                     const int* __restrict__ dst, const u16* __restrict__ we,
                                                     const u16* __restrict__ wx, const u16* __restrict__ wh,
                                                     char* ws, u16* __restrict__ out) {
  __shared__ __attribute__((aligned(256))) char lds[L_END];
  const unsigned lbase = (unsigned)(size_t)lds;
  const int tid = threadIdx.x, lane = tid & 63;
  const int wv = __builtin_amdgcn_readfirstlane(tid >> 6);
  const int w = blockIdx.x;
  const int b = (w & 7) * 32 + (w >> 3);
  const int n0 = bucket_start(b), nsz = bucket_start(b + 1) - n0;
  const int r = lane & 31, hh = lane >> 5;
  const int blk = (lane >> 4) & 1, q4 = (lane & 15) >> 2, p4 = lane & 3;
  const __amdgpu_buffer_rsrc_t wsr = __builtin_amdgcn_make_buffer_rsrc(ws, 0, WS_END, 0x00020000);
  STAMP(0);

  int d[3], s[3];
  u32x2 a[3];
  const bool v2 = tid < CH - 1024;
  {
    const unsigned e0 = (unsigned)(w * CH + tid) * 4, e2 = v2 ? e0 + 4096 : e0;
    asm volatile("global_load_dword %0, %6, %8\n\tglobal_load_dword %1, %6, %8 offset:2048\n\tglobal_load_dword %2, %7, %8\n\t"
                 "global_load_dword %3, %6, %9\n\tglobal_load_dword %4, %6, %9 offset:2048\n\tglobal_load_dword %5, %7, %9"
                 : "=&v"(d[0]), "=&v"(d[1]), "=&v"(d[2]), "=&v"(s[0]), "=&v"(s[1]), "=&v"(s[2])
                 : "v"(e0), "v"(e2), "s"(dst), "s"(src) : "memory");
  }
  const unsigned wlb = (unsigned)((lane >> 4) * 256 + 16 * ((lane & 15) ^ ((lane >> 4) << 2)));
  {
    const int q = lane >> 4, c = lane & 15;
    const int row = 4 * wv + q;
    dma16(h, (unsigned)(min(n0 + row, NN - 1) * 256 + 16 * (c ^ (row & 15))), lbase + L_H + wv * 1024);
    if (wv < 2) dma16(h, (unsigned)(min(n0 + 32 + row, NN - 1) * 256 + 16 * (c ^ (row & 15))), lbase + L_H + (8 + wv) * 1024);
#pragma unroll
    for (int i = 0; i < 4; i++) dma16(we, (wlb ^ (16 * i)) + (32 + 4 * wv + i) * 1024, lbase + L_W + (4 * wv + i) * 1024);
  }
  unsigned tagv = __builtin_amdgcn_raw_buffer_load_b32(wsr, WS_EP, 0, 16);
#pragma unroll
  for (int k = 0; k < 3; k++) a[k] = eattr[w * CH + (k < 2 || v2 ? tid + 512 * k : 0)];
  asm volatile("" ::: "memory");
#pragma unroll
  for (int i = 0; i < 7; i++) dma16(pos, (unsigned)((8 * i + wv) * 1024 + lane * 16), lbase + L_ROWS + (8 * i + wv) * 1024);
  if (wv < 2 || (wv == 2 && lane < 38)) dma16(pos, (unsigned)((56 + wv) * 1024 + lane * 16), lbase + L_ROWS + (56 + wv) * 1024);
  if (wv == 3) dma16(we, 65536 + lane * 16, lbase + 65536);
  if (wv == 4 && lane < 16) dma16(we, 66560 + lane * 16, lbase + 66560);
  if (wv == 5 && lane < 16) dma16(wx, lane * 16, lbase + L_WX);
  if (tid < 256) *(u32*)(lds + L_HIST + tid * 4) = 0u;
  else {
    const int t = tid - 256;
    if (t < 32) {
      const int j = (t >> 2) - 4 * ((t >> 1) & 1) - 2 * (t & 1);
      *(u32*)(lds + L_OH + t * 4) = j == 0 ? 0x3F80u : j == 1 ? 0x3F800000u : 0u;
      if (t < 16) *(u32*)(lds + L_GC + t * 4) = 0u;
    }
  }
  asm volatile("s_waitcnt vmcnt(11)" : "+v"(d[0]), "+v"(d[1]), "+v"(d[2]), "+v"(s[0]), "+v"(s[1]), "+v"(s[2])::"memory");
  barrier();
  STAMP(1);
  int key[3];
  unsigned rank[3];
#pragma unroll
  for (int k = 0; k < 3; k++) {
    key[k] = (int)((unsigned)__umul24((unsigned)d[k], 429497u) >> 24);
    rank[k] = 0;
    if (k < 2 || v2) rank[k] = atomicAdd((unsigned*)(lds + L_HIST) + key[k], 1u);
  }
  const int T = wv & 3, nb = wv >> 2;
  const int loc = 5 * (4 * nb + (r >> 3)) + (r & 7);
  const bool valid = (r & 7) < 5 && loc < nsz;
  const int ha = L_H + min(loc, 39) * 256 + 16 * (hh ^ (min(loc, 39) & 15));
  const unsigned trx0 = lbase + L_W + (unsigned)((8 * hh + q4) * 256), trs0 = (unsigned)((q4 << 2) | (2 * hh));
  u32x4 bt0, bt1;
  {
    const unsigned ch = (unsigned)(8 * (T & 1) + 4 * (p4 & 1) + 2 * blk + (T >> 1));
    const unsigned a0 = trx0 + 16 * (ch ^ trs0) + 8 * (p4 >> 1);
    const unsigned a1 = trx0 + 1024 + 16 * (ch ^ trs0 ^ 1) + 8 * (p4 >> 1);
    f32x16 acc0 = zero16(), acc1 = zero16();
#pragma unroll
    for (int ks = 0; ks < 8; ks += 2) {
      acc0 = mfma(tr_read2(a0 + ks * 4096, a1 + ks * 4096), *(const u32x4*)(lds + (ha ^ (32 * ks))), acc0);
      acc1 = mfma(tr_read2(a0 + ks * 4096 + 4096, a1 + ks * 4096 + 4096), *(const u32x4*)(lds + (ha ^ (32 * ks + 32))), acc1);
    }
#pragma unroll
    for (int k = 0; k < 3; k++) {
      const int lc = d[k] - bucket_start(key[k]);
      const int g = (lc * 205) >> 10;
      key[k] = (key[k] << 8) | (lc + 3 * g);
    }
#pragma unroll
    for (int i = 0; i < 4; i++) {
      bt0[i] = pk2((acc0[2 * i] + acc1[2 * i]) * NLOG2E, (acc0[2 * i + 1] + acc1[2 * i + 1]) * NLOG2E);
      bt1[i] = pk2((acc0[8 + 2 * i] + acc1[8 + 2 * i]) * NLOG2E, (acc0[9 + 2 * i] + acc1[9 + 2 * i]) * NLOG2E);
    }
    PIN(bt0);
    PIN(bt1);
  }
  wait_vm0();
  asm volatile("" : "+v"(tagv), "+v"(a[0]), "+v"(a[1]), "+v"(a[2]));
  barrier();
  STAMP(2);
  const unsigned tag = (unsigned)__builtin_amdgcn_readfirstlane((int)tagv) + 1u;
  u32x4 off4;
  unsigned oend;
  {
    const u32x4 cnt = *(const u32x4*)(lds + L_HIST + lane * 16);
    u32x2 pd[3], ps[3];
#pragma unroll
    for (int k = 0; k < 3; k++) {
      const int od = L_ROWS + ((6 * d[k]) & ~3), os = L_ROWS + ((6 * s[k]) & ~3);
      pd[k][0] = *(const u32*)(lds + od); pd[k][1] = *(const u32*)(lds + od + 4);
      ps[k][0] = *(const u32*)(lds + os); ps[k][1] = *(const u32*)(lds + os + 4);
    }
#pragma unroll
    for (int i = 0; i < 4; i++) dma16(we, (wlb ^ (16 * i)) + (4 * wv + i) * 1024, lbase + L_W + (4 * wv + i) * 1024);
    const unsigned tot = cnt[0] + cnt[1] + cnt[2] + cnt[3];
    oend = (unsigned)wave_incl_scan((int)tot);
    off4[0] = oend - tot;
    off4[1] = off4[0] + cnt[0];
    off4[2] = off4[1] + cnt[1];
    off4[3] = off4[2] + cnt[2];
    *(u32x4*)(lds + L_OFF + lane * 16) = off4;
#pragma unroll
    for (int k = 0; k < 3; k++) rank[k] += *(const u32*)(lds + L_OFF + (key[k] >> 8) * 4);
#pragma unroll
    for (int k = 0; k < 3; k++) {
      const unsigned long long ud = (((unsigned long long)pd[k][1] << 32) | pd[k][0]) >> ((d[k] & 1) * 16);
      const unsigned long long us = (((unsigned long long)ps[k][1] << 32) | ps[k][0]) >> ((s[k] & 1) * 16);
      const unsigned dlo = (unsigned)ud, dhi = (unsigned)(ud >> 32), slo = (unsigned)us, shi = (unsigned)(us >> 32);
      const unsigned dxy = pk2(bf2f(dlo) - bf2f(slo), bfhi(dlo) - bfhi(slo));
      const unsigned dz_ = pk2(bf2f(dhi) - bf2f(shi), 0.f);
      const float dx = bf2f(dxy), dy = bfhi(dxy), dz = bf2f(dz_);
      const unsigned sxy = pk2(dx * dx, dy * dy), sz = pk2(dz * dz, 0.f);
      const unsigned d2 = pk2((bf2f(sxy) + bfhi(sxy)) + bf2f(sz), 0.f) & 0xffffu;
      const u32x4 rec = {a[k][0], d2 | (dxy << 16), a[k][1], (dxy >> 16) | (dz_ << 16)};
      if (k < 2 || v2) {
        *(u32x4*)(lds + L_S + rank[k] * 16) = rec;
        *(u32*)(lds + L_R2S + rank[k] * 4) = ((unsigned)s[k] << 8) | ((unsigned)key[k] & 0xffu);
      }
    }
  }
  wait_vm0();
  barrier();
  STAMP(4);
  {
    const u32x4 z = {0u, 0u, 0u, 0u};
#pragma unroll
    for (int k = 0; k < 3; k++) {
      if (k < 2 || tid < 256) {
        const u32x4 v = *(const u32x4*)(lds + L_S + (tid + 512 * k) * 16);
        __builtin_amdgcn_raw_buffer_store_b128(v, wsr, WS_R1 + (unsigned)(w * CHS + tid + 512 * k) * 16, 0, 16);
        *(u32x4*)(lds + L_S + (tid + 512 * k) * 16) = z;
      }
    }
    if (tid >= 192) {
      const u32x4 v = *(const u32x4*)(lds + L_R2S + (tid - 192) * 16);
      __builtin_amdgcn_raw_buffer_store_b128(v, wsr, WS_R2 + (unsigned)(w * CHS) * 4 + (tid - 192) * 16, 0, 16);
    }
    if (valid) {
      const unsigned off = WS_BT + (unsigned)(n0 + loc) * 256 + 64 * T + 32 * hh;
      __builtin_amdgcn_raw_buffer_store_b128(bt0, wsr, off, 0, 16);
      __builtin_amdgcn_raw_buffer_store_b128(bt1, wsr, off + 16, 0, 16);
    }
  }
  u32x2 at[4];
  {
    const unsigned chA = (unsigned)(4 * T + 2 * blk + (p4 >> 1));
    const unsigned a0 = trx0 + 16 * (chA ^ trs0) + 8 * (p4 & 1), a1 = trx0 + 1024 + 16 * (chA ^ trs0 ^ 1) + 8 * (p4 & 1);
    f32x16 acc = zero16();
#pragma unroll
    for (int ks = 0; ks < 8; ks++) acc = mfma(*(const u32x4*)(lds + (ha ^ (32 * ks))), tr_read2(a0 + ks * 4096, a1 + ks * 4096), acc);
#pragma unroll
    for (int i = 0; i < 4; i++) {
      at[i][0] = pk2(acc[4 * i] * NLOG2E, acc[4 * i + 1] * NLOG2E);
      at[i][1] = pk2(acc[4 * i + 2] * NLOG2E, acc[4 * i + 3] * NLOG2E);
      PIN(at[i]);
    }
  }
  wait_vm0();
  barrier();
  STAMP(5);
  if (wv == 0) {
    const u32x4 g0 = {tag, off4[0] | (off4[1] << 16), tag, off4[1] | (off4[2] << 16)};
    const u32x4 g1 = {tag, off4[2] | (off4[3] << 16), tag, off4[3] | (oend << 16)};
    const unsigned off = WS_TAB + (unsigned)(w * NB + 4 * lane) * 8;
    __builtin_amdgcn_raw_buffer_store_b128(g0, wsr, off, 0, 16);
    __builtin_amdgcn_raw_buffer_store_b128(g1, wsr, off + 16, 0, 16);
  }
#pragma unroll
  for (int i = 0; i < 4; i++) *(u32x2*)(lds + L_A + (4 * nb + i) * 2048 + T * 512 + lane * 8) = at[i];
  if (tid >= 256) {
    const int t = tid - 256, v = t >> 5, q = (t >> 3) & 3, i = t & 7;
    *(u32*)(lds + L_FK + t * 4) = (unsigned)(i < 4 ? 128 + 32 * q + 4 * v + i : 16 * v + 4 * (i - 4) + q) << 8;
  }
  u32x4 extB[4];
#pragma unroll
  for (int t = 0; t < 4; t++) {
    const u16* wp = (const u16*)(lds + 65536) + 32 * t + r;
    const float w0 = bf2f(wp[(1 + 2 * hh) * 128]) * NLOG2E, w1 = bf2f(wp[(2 + 2 * hh) * 128]) * NLOG2E, w2 = bf2f(wp[0]) * NLOG2E;
    extB[t][0] = 0u;
    extB[t][1] = 0u;
    extB[t][2] = pk2(w0, w1);
    extB[t][3] = hh ? 0u : pk2(w2, 0.f);
  }
  u32x4 I0, I1;
  {
    const unsigned one = (r & 1) ? 0x3F800000u : 0x3F80u;
#pragma unroll
    for (int jj = 0; jj < 4; jj++) {
      const bool m = ((r & 7) >> 1) == jj;
      I0[jj] = (m && (r >> 3) == hh) ? one : 0u;
      I1[jj] = (m && (r >> 3) == 2 + hh) ? one : 0u;
    }
  }
  const int rowbuf = L_ROWS + wv * ROWBUF;
  unsigned kdma = 16 * ((lane & 15) ^ (4 * (lane >> 4)));
  const int rd0 = rowbuf + 1040 * (r & 3) + 4352 * ((r >> 2) & 1) + (r >> 3) * 256;
  int rdA = rd0 + 64 * ((2 * hh) ^ (r >> 3)), rdB = rd0 + 64 * ((2 * hh + 1) ^ (r >> 3));
  int swr0 = L_S + 32 * (r >> 4) + 16 * ((r >> 2) & 1) + 8 * ((r >> 3) & 1) + 2 * (r & 3) + 160 * hh;
  int srd0 = L_S + r * 80 + 16 * hh;
  const char* btab = ws + WS_BT;
  f32x16 Y[4];
#pragma unroll
  for (int t = 0; t < 4; t++) {
    Y[t] = zero16();
    PIN(Y[t]);
    PIN(extB[t]);
  }
  PIN(I0); PIN(I1); PIN(kdma); PIN(rdA); PIN(rdB); PIN(swr0); PIN(srd0);
  STAMP(6);

  bool wh_loaded = false, slow = false;
  int lo = 0, quota = MAPW, gq = wv;
  while (true) {
    int tq = tid;
    PIN(tq);
    const int lq = tq & 63;
    int kc, kfirst, kpre, wtot;
    {
      const unsigned tabme = WS_TAB + (unsigned)((tq >> 1) * NB + b) * 8;
      u32x2 gq2 = {tag + 1u, 0u};
  #pragma clang loop unroll(disable)
      for (int spin = 0; spin < (1 << 22); spin++) {
        if (gq2[0] != tag) asm volatile("global_load_dwordx2 %0, %1, %2 sc1\n\ts_waitcnt vmcnt(0)" : "=&v"(gq2) : "v"(tabme), "s"(ws) : "memory");
        if (__all(gq2[0] == tag)) break;
        __builtin_amdgcn_s_sleep(1);
      }
      const int o0 = min((int)(gq2[1] & 0xffffu), CH), o1 = min((int)(gq2[1] >> 16), CH);
      kc = max(o1 - o0, 0);
      kfirst = (tq >> 1) * CHS + o0;
      const int inc = wave_incl_scan((tq & 1) ? 0 : kc);
      kpre = inc - kc;
      wtot = __builtin_amdgcn_readlane(inc, 63);
      if (!slow && lq == 0) atomicMax((unsigned*)(lds + L_GC + 32), (unsigned)wtot);
    }
    STAMP(7);
    {
      const int map = L_MAP + wv * MAPW * 4;
      const int nw = min(wtot - lo, quota);
      const int jhi = min(kc, lo + quota - kpre);
#pragma clang loop vectorize(disable) unroll(disable)
      for (int j = max(0, lo - kpre) + ((tq ^ max(0, lo - kpre)) & 1); j < jhi; j += 2) *(u32*)(lds + map + (kpre + j - lo) * 4) = (unsigned)(kfirst + j);
      for (int p0 = 0; p0 < nw; p0 += 192) {
        u32x4 q1[3];
        unsigned q2[3];
#pragma unroll
        for (int k = 0; k < 3; k++) {
          const int p = p0 + 64 * k + lq;
          const unsigned mi = *(const u32*)(lds + map + min(p, MAPW - 1) * 4);
          const unsigned idx = p < nw ? mi : 0u;
          q1[k] = __builtin_amdgcn_raw_buffer_load_b128(wsr, WS_R1 + idx * 16, 0, 16);
          q2[k] = __builtin_amdgcn_raw_buffer_load_b32(wsr, WS_R2 + idx * 4, 0, 16);
        }
        asm volatile("" : "+v"(q1[0]), "+v"(q1[1]), "+v"(q1[2]), "+v"(q2[0]), "+v"(q2[1]), "+v"(q2[2]));
        unsigned rk[3];
#pragma unroll
        for (int k = 0; k < 3; k++) {
          rk[k] = CAPW;
          if (p0 + 64 * k + lq < nw) rk[k] = atomicAdd((unsigned*)(lds + L_GC) + ((q2[k] >> 3) & 7u), 1u);
        }
#pragma unroll
        for (int k = 0; k < 3; k++) {
          if (rk[k] < CAPW) {
            const unsigned e = ((q2[k] >> 3) & 7u) * CAPW + rk[k];
            *(u32x4*)(lds + L_R1 + e * 16) = q1[k];
            *(u32*)(lds + L_R2 + e * 4) = q2[k];
          }
        }
      }
    }
    barrier();
    STAMP(8);
    if (w == 0 && tid == 0) __builtin_amdgcn_raw_buffer_store_b32(tag, wsr, WS_EP, 0, 16);
    const int cl = *(const u32*)(lds + L_GC + (lane & 7) * 4);
    const int maxtot = __builtin_amdgcn_readfirstlane(*(const u32*)(lds + L_GC + 32));
    if (!slow) {
      const int kk = (cl << 3) | (lane & 7);
      int rk = 0;
      rk += __builtin_amdgcn_update_dpp(0, kk, 0x121, 0xf, 0xf, false) > kk;
      rk += __builtin_amdgcn_update_dpp(0, kk, 0x122, 0xf, 0xf, false) > kk;
      rk += __builtin_amdgcn_update_dpp(0, kk, 0x123, 0xf, 0xf, false) > kk;
      rk += __builtin_amdgcn_update_dpp(0, kk, 0x124, 0xf, 0xf, false) > kk;
      rk += __builtin_amdgcn_update_dpp(0, kk, 0x125, 0xf, 0xf, false) > kk;
      rk += __builtin_amdgcn_update_dpp(0, kk, 0x126, 0xf, 0xf, false) > kk;
      rk += __builtin_amdgcn_update_dpp(0, kk, 0x127, 0xf, 0xf, false) > kk;
      const int cmax = __any(cl > CAPW) ? CAPW + 1 : 0;
      if (maxtot > MAPW || cmax > CAPW) {
        slow = true;
        quota = CAPW / 8;
        barrier();
        if (tid < 8) *(u32*)(lds + L_GC + tid * 4) = 0u;
        barrier();
        continue;
      }
      gq = __builtin_ctzll(__ballot(rk == (wv < 4 ? wv : 11 - wv)));
    }
    const int n = __builtin_amdgcn_readlane(cl, gq);
    const int swr = swr0 + gq * 2560, srd = srd0 + gq * 2560;
    const int l1 = L_R1 + gq * CAPW * 16, l2 = L_R2 + gq * CAPW * 4;
    if (n > 0) {
    const int nt = (n + 31) >> 5;
    if (lane < 32 * nt - n) {
      const u32x4 z = {0u, 0u, 0u, 0u};
      *(u32*)(lds + l2 + (n + lane) * 4) = 0u;
      *(u32x4*)(lds + l1 + (n + lane) * 16) = z;
    }
#pragma unroll
    for (int t = 0; t < 4; t++) {
      const u32x2 v = *(const u32x2*)(lds + L_A + gq * 2048 + t * 512 + lane * 8);
      extB[t][0] = v[0];
      extB[t][1] = v[1];
    }

    u32x4 rows[8], extA, S0, S1, ra, rb, m1p = {0u, 0u, 0u, 0u};
    u32x2 r1;
    unsigned r2;
    int sp;
    f32x16 XA, XB;
    auto list_read = [&](int tl) {
      const int list = tl < nt ? l2 + tl * 128 : L_FK + wv * 128;
      ra = *(const u32x4*)(lds + list + (lane >> 4) * 32);
      rb = *(const u32x4*)(lds + list + (lane >> 4) * 32 + 16);
    };
    auto dma_piece = [&](int tl, int i) {
      const unsigned v = i < 4 ? ra[i & 3] : rb[i & 3];
      dma16_dev(tl < nt ? btab : (const char*)wh, (v & 0xffffff00u) | kdma, lbase + rowbuf + piece_off(i));
    };
    auto meta_read = [&](int tl) {
      r1 = *(const u32x2*)(lds + l1 + (tl * 32 + r) * 16 + hh * 8);
      r2 = *(const u32*)(lds + l2 + (tl * 32 + r) * 4);
    };
    auto meta_use = [&](int tl) {
      const unsigned dl = r2 & 7u;
      const u32x2 oh = *(const u32x2*)(lds + L_OH + dl * 16 + hh * 8);
      extA[0] = oh[0]; extA[1] = oh[1]; extA[2] = r1[0]; extA[3] = r1[1];
      sp = (tl * 32 + r < n) ? swr + (int)dl * 320 : L_DUM;
    };
    auto s_write = [&]() {
      *(u16*)(lds + sp) = (u16)(hh ? extA[3] : 0x3F80u);
      *(u16*)(lds + sp + 80) = (u16)(extA[3] >> 16);
    };
    auto rows_read = [&]() {
#pragma unroll
      for (int j = 0; j < 8; j++) rows[j] = *(const u32x4*)(lds + (j < 4 ? rdA : rdB) + 16 * (j & 3));
    };
    auto rows_pin = [&]() {
      asm volatile("" : "+v"(rows[0]), "+v"(rows[1]), "+v"(rows[2]), "+v"(rows[3]), "+v"(rows[4]), "+v"(rows[5]), "+v"(rows[6]), "+v"(rows[7]));
    };

    list_read(0);
    meta_read(0);
#pragma unroll
    for (int i = 0; i < 8; i++) dma_piece(0, i);
    list_read(1);
    meta_use(0);
    s_write();
    S1 = m1p;
    wait_vm0();
    rows_read();
    rows_pin();
#pragma unroll
    for (int i = 0; i < 8; i++) dma_piece(1, i);
    XA = mfma(rows[0], I0, zero16());
    XA = mfma(rows[1], I1, XA);
    XA = mfma(extA, extB[0], XA);
    STAMP(9);

    const int nfull = n >> 5;
    for (int tl = 0; tl < nfull; tl++) {
#pragma unroll
      for (int t = 0; t < 4; t++) {
        f32x16& Xc = (t & 1) ? XB : XA;
        f32x16& Xn = (t & 1) ? XA : XB;
        const int tn = (t + 1) & 3;
        float e[16];
        SB();
        if (t == 0) {
          S0 = *(const u32x4*)(lds + srd);
        }
        if (t == 2) meta_read(tl + 1);
        Xn = mfma(rows[2 * tn], I0, zero16());
        PIN(Xn);
        SB();
#pragma unroll
        for (int i = 0; i < 4; i++) e[i] = __builtin_amdgcn_exp2f(Xc[i]);
        if (t == 3) { dma_piece(tl + 2, 0); dma_piece(tl + 2, 1); }
        SB();
        Xn = mfma(rows[2 * tn + 1], I1, Xn);
        PIN(Xn);
        SB();
#pragma unroll
        for (int i = 4; i < 8; i++) e[i] = __builtin_amdgcn_exp2f(Xc[i]);
        if (t == 3) { dma_piece(tl + 2, 2); dma_piece(tl + 2, 3); }
        SB();
        Xn = mfma(extA, extB[tn], Xn);
        PIN(Xn);
        SB();
#pragma unroll
        for (int i = 8; i < 12; i++) e[i] = __builtin_amdgcn_exp2f(Xc[i]);
        if (t == 3) { dma_piece(tl + 2, 4); dma_piece(tl + 2, 5); }
        SB();
        Y[(t + 3) & 3] = mfma(S1, m1p, Y[(t + 3) & 3]);
        SB();
        if (t == 0) {
          S1 = *(const u32x4*)(lds + srd + 32);
          *(u16*)(lds + sp) = 0;
          *(u16*)(lds + sp + 80) = 0;
        }
        if (t == 2) {
          wait_vm0();
          rows_read();
          list_read(tl + 2);
          meta_use(tl + 1);
        }
#pragma unroll
        for (int i = 12; i < 16; i++) e[i] = __builtin_amdgcn_exp2f(Xc[i]);
        if (t == 3) { dma_piece(tl + 2, 6); dma_piece(tl + 2, 7); s_write(); }
        SB();
        f32x2 dd[8];
#pragma unroll
        for (int i = 0; i < 8; i++) { dd[i][0] = e[2 * i]; dd[i][1] = e[2 * i + 1]; dd[i] += 1.0f; }
        SB();
#pragma unroll
        for (int i = 0; i < 8; i++) { dd[i][0] = __builtin_amdgcn_rcpf(dd[i][0]); dd[i][1] = __builtin_amdgcn_rcpf(dd[i][1]); }
        SB();
        u32x4 m0;
#pragma unroll
        for (int i = 0; i < 4; i++) { f32x2 x = {Xc[2 * i], Xc[2 * i + 1]}; dd[i] *= x; }
#pragma unroll
        for (int i = 0; i < 4; i++) m0[i] = __builtin_bit_cast(unsigned, __builtin_convertvector(dd[i], bf16x2));
        SB();
#pragma unroll
        for (int i = 4; i < 8; i++) { f32x2 x = {Xc[2 * i], Xc[2 * i + 1]}; dd[i] *= x; }
        SB();
        Y[t] = mfma(S0, m0, Y[t]);
        SB();
#pragma unroll
        for (int i = 0; i < 4; i++) m1p[i] = __builtin_bit_cast(unsigned, __builtin_convertvector(dd[4 + i], bf16x2));
        if (t == 2) rows_pin();
      }
    }
    SB();
    Y[3] = mfma(S1, m1p, Y[3]);
    if (nfull < nt) {
      const int c = n & 31;
      S0 = *(const u32x4*)(lds + srd);
      S1 = *(const u32x4*)(lds + srd + 32);
      *(u16*)(lds + sp) = 0;
      *(u16*)(lds + sp + 80) = 0;
#pragma unroll
      for (int t = 0; t < 4; t++) {
        f32x16& Xc = (t & 1) ? XB : XA;
        f32x16& Xn = (t & 1) ? XA : XB;
        if (t < 3) {
          Xn = mfma(rows[2 * t + 2], I0, zero16());
          Xn = mfma(rows[2 * t + 3], I1, Xn);
          Xn = mfma(extA, extB[t + 1], Xn);
        }
        unsigned m[8] = {0u, 0u, 0u, 0u, 0u, 0u, 0u, 0u};
        silu4(Xc, 0, m[0], m[1]);
        if (c > 8) silu4(Xc, 1, m[2], m[3]);
        if (c > 16) silu4(Xc, 2, m[4], m[5]);
        if (c > 24) silu4(Xc, 3, m[6], m[7]);
        const u32x4 m0 = {m[0], m[1], m[2], m[3]}, m1 = {m[4], m[5], m[6], m[7]};
        Y[t] = mfma(S0, m0, Y[t]);
        Y[t] = mfma(S1, m1, Y[t]);
      }
    }
    wh_loaded = true;
    wait_vm0();
    }
    if (!slow) break;
    lo += quota;
    if (lo >= maxtot) break;
    barrier();
    if (tid < 8) *(u32*)(lds + L_GC + tid * 4) = 0u;
    barrier();
  }
  STAMP(10);
  if (!wh_loaded) {
    const u32x4 fa = *(const u32x4*)(lds + L_FK + wv * 128 + (lane >> 4) * 32), fb = *(const u32x4*)(lds + L_FK + wv * 128 + (lane >> 4) * 32 + 16);
#pragma unroll
    for (int i = 0; i < 8; i++) dma16(wh, (i < 4 ? fa[i & 3] : fb[i & 3]) | kdma, lbase + rowbuf + piece_off(i));
    wait_vm0();
  }

  const int nrow = min(n0 + loc, NN - 1);
  const u32x4 res0 = *(const u32x4*)(h + (size_t)nrow * 128 + 32 * T + 16 * hh);
  const u32x4 res1 = *(const u32x4*)(h + (size_t)nrow * 128 + 32 * T + 16 * hh + 8);
  const bool pval = lane < 15 && 15 * gq + lane < 3 * nsz;
  const unsigned pofs = (unsigned)(3 * n0 + 15 * gq + lane);
  unsigned pin = 0;
  if (pval) pin = pos[pofs];
#pragma unroll
  for (int i = 0; i < 3; i++) {
    if (i < 2 || hh == 0) {
      const u32x2 mv = {pk2(Y[0][4 * i] * NLN2, Y[1][4 * i] * NLN2), pk2(Y[2][4 * i] * NLN2, Y[3][4 * i] * NLN2)};
      *(u32x2*)(lds + L_M + (5 * gq + 2 * i + hh) * MSTR + 8 * r) = mv;
    }
  }
  {
    float zz[9], wxv[4];
#pragma unroll
    for (int t = 0; t < 4; t++) wxv[t] = bf2f(((const u16*)(lds + L_WX))[32 * t + r]) * NLN2;
#pragma unroll
    for (int i = 0; i < 3; i++) {
#pragma unroll
      for (int cc = 1; cc < 4; cc++) {
        float z = Y[0][4 * i + cc] * wxv[0] + Y[1][4 * i + cc] * wxv[1] + Y[2][4 * i + cc] * wxv[2] + Y[3][4 * i + cc] * wxv[3];
        z += __builtin_bit_cast(float, __builtin_amdgcn_update_dpp(0, __builtin_bit_cast(int, z), 0x111, 0xf, 0xf, false));
        z += __builtin_bit_cast(float, __builtin_amdgcn_update_dpp(0, __builtin_bit_cast(int, z), 0x112, 0xf, 0xf, false));
        z += __builtin_bit_cast(float, __builtin_amdgcn_update_dpp(0, __builtin_bit_cast(int, z), 0x114, 0xf, 0xf, false));
        z += __builtin_bit_cast(float, __builtin_amdgcn_update_dpp(0, __builtin_bit_cast(int, z), 0x118, 0xf, 0xf, false));
        z += __builtin_bit_cast(float, __builtin_amdgcn_update_dpp(0, __builtin_bit_cast(int, z), 0x142, 0xa, 0xf, false));
        zz[3 * i + cc - 1] = z;
      }
    }
    const int zp = L_S + gq * 2560;
    if (r == 31) {
#pragma unroll
      for (int i = 0; i < 9; i++) *(u32*)(lds + zp + 12 * hh + 24 * (i / 3) + 4 * (i % 3)) = __float_as_uint(zz[i]);
    }
    asm volatile("" ::: "memory");
    const float z = __uint_as_float(*(const u32*)(lds + zp + 4 * (lane & 15)));
    if (pval) out[(size_t)NN * 128 + pofs] = (u16)pk2(bf2f(pin) + z, 0.f);
  }
  barrier();
  STAMP(11);
  {
    const unsigned wa = lbase + L_ROWS + 2080 * hh + 256 * q4 + 64 * (T ^ q4) + 32 * (p4 & 1) + 16 * blk + 8 * (p4 >> 1);
    const int ma = L_M + min(loc, 39) * MSTR + 16 * hh;
    f32x16 P = zero16(), P2 = zero16();
#pragma unroll
    for (int ks = 0; ks < 8; ks++) {
      P = mfma(tr_read2(wa + ks * ROWBUF + 4352, wa + ks * ROWBUF + 4352 + 1040), *(const u32x4*)(lds + (ha ^ (32 * ks))), P);
      P2 = mfma(tr_read2(wa + ks * ROWBUF, wa + ks * ROWBUF + 1040), *(const u32x4*)(lds + ma + 32 * ks), P2);
    }
    if (valid) {
      u32x4 ov[2];
#pragma unroll
      for (int i = 0; i < 8; i++) {
        const unsigned rs = i < 4 ? res0[i & 3] : res1[i & 3];
        const float x0 = P[2 * i] + P2[2 * i], x1 = P[2 * i + 1] + P2[2 * i + 1];
        const float s0 = x0 * __builtin_amdgcn_rcpf(1.f + __builtin_amdgcn_exp2f(x0 * NLOG2E));
        const float s1 = x1 * __builtin_amdgcn_rcpf(1.f + __builtin_amdgcn_exp2f(x1 * NLOG2E));
        ov[i >> 2][i & 3] = pk2(bf2f(rs) + s0, bfhi(rs) + s1);
      }
      u32x4* op = (u32x4*)(out + (size_t)(n0 + loc) * 128 + 32 * T + 16 * hh);
      op[0] = ov[0];
      op[1] = ov[1];
    }
  }
  STAMP(12);
}

extern "C" void kernel_launch(void* const* d_in, const int* in_sizes, int n_in, void* d_out, int out_size, void* d_ws,
                              size_t ws_size, hipStream_t stream) {
  egnn_fused<<<NB, 512, 0, stream>>>((const u16*)d_in[0], (const u16*)d_in[1], (const u32x2*)d_in[2], (const int*)d_in[3],
                                     (const int*)d_in[4], (const u16*)d_in[5], (const u16*)d_in[6], (const u16*)d_in[7],
                                     (char*)d_ws, (u16*)d_out);
}
```

```cpp
#include <hip/hip_runtime.h>
#include <hip/hip_bf16.h>
#include <stdint.h>

typedef __bf16 bf16x8 __attribute__((ext_vector_type(8)));
typedef __bf16 bf16x2 __attribute__((ext_vector_type(2)));
typedef short s16x4 __attribute__((ext_vector_type(4)));
typedef float f32x2 __attribute__((ext_vector_type(2)));
typedef float f32x4 __attribute__((ext_vector_type(4), may_alias));
typedef float f32x16 __attribute__((ext_vector_type(16)));
typedef unsigned u32x2 __attribute__((ext_vector_type(2), may_alias));
typedef unsigned u32x3 __attribute__((ext_vector_type(3)));
typedef unsigned u32x4 __attribute__((ext_vector_type(4), may_alias));
typedef unsigned u32x2a4 __attribute__((ext_vector_type(2), aligned(4), may_alias));
typedef unsigned short u16 __attribute__((may_alias));
typedef unsigned u32 __attribute__((may_alias));
#define LDSP(T) __attribute__((address_space(3))) T*
#define DI __device__ __forceinline__
#define SB() __builtin_amdgcn_sched_barrier(0)
#define PIN(x) asm volatile("" : "+v"(x))

constexpr int NN = 10000;
constexpr int CH = 1250, CHS = 1280;
constexpr int NB = 256;
constexpr float NLOG2E = -1.4426950408889634f;
constexpr float NLN2 = -0.6931471805599453f;

constexpr unsigned WS_BT = 0;
constexpr unsigned WS_R = 2560000;
constexpr unsigned WS_TAB = WS_R + NB * CHS * 12;
constexpr unsigned WS_EP = WS_TAB + NB * NB * 8;
constexpr unsigned WS_END = WS_EP + 256;

constexpr int CAPW = 224;
constexpr int ROWBUF = 8496;
constexpr int L_ROWS = 0;
constexpr int L_R1 = 8 * ROWBUF;
constexpr int L_R2 = L_R1 + 8 * CAPW * 16;
constexpr int L_S = L_R2 + 8 * CAPW * 4;
constexpr int L_M = L_S + 8 * 2560;
constexpr int MSTR = 272;
constexpr int L_H = 135168;
constexpr int L_A = L_H + 10240;
constexpr int L_HIST = L_A + 8 * 2048;
constexpr int L_OH = L_HIST + 1024;
constexpr int L_GC = L_OH + 128;
constexpr int L_DUM = L_GC + 64;
constexpr int L_WX = L_DUM + 128;
constexpr int L_END = L_WX + 256;
constexpr int L_W = L_R1;
constexpr int L_ST = L_A;
constexpr int L_OFF = L_A + 15360;
constexpr int L_PF = 61440;
constexpr int MAPW = 256;
constexpr int L_MAP = L_M;
constexpr int L_FK = L_HIST;
static_assert(L_W + 65536 <= L_H && L_H % 256 == 0 && L_M + 40 * MSTR <= L_H && L_END <= 163840, "lds map");

DI constexpr int piece_off(int i) { return 1040 * (i & 3) + 4352 * (i >> 2); }
DI float bf2f(unsigned u) { return __uint_as_float(u << 16); }
DI float bfhi(unsigned u) { return __uint_as_float(u & 0xffff0000u); }
DI unsigned pk2(float a, float b) {
  f32x2 v = {a, b};
  return __builtin_bit_cast(unsigned, __builtin_convertvector(v, bf16x2));
}
DI f32x16 mfma(u32x4 a, u32x4 b, f32x16 c) {
  return __builtin_amdgcn_mfma_f32_32x32x16_bf16(__builtin_bit_cast(bf16x8, a), __builtin_bit_cast(bf16x8, b), c, 0, 0, 0);
}
DI f32x16 zero16() {
  f32x16 z;
#pragma unroll
  for (int i = 0; i < 16; i++) z[i] = 0.f;
  return z;
}
DI u32x4 tr_read2(unsigned a0, unsigned a1) {
  const u32x2 lo = __builtin_bit_cast(u32x2, __builtin_amdgcn_ds_read_tr16_b64_v4i16((LDSP(s16x4))(size_t)a0));
  const u32x2 hi = __builtin_bit_cast(u32x2, __builtin_amdgcn_ds_read_tr16_b64_v4i16((LDSP(s16x4))(size_t)a1));
  const u32x4 v = {lo[0], lo[1], hi[0], hi[1]};
  return v;
}
DI int bucket_start(int b) { return (b * 625 + 15) >> 4; }
DI int wave_incl_scan(int x) {
  x += __builtin_amdgcn_update_dpp(0, x, 0x111, 0xf, 0xf, false);
  x += __builtin_amdgcn_update_dpp(0, x, 0x112, 0xf, 0xf, false);
  x += __builtin_amdgcn_update_dpp(0, x, 0x114, 0xf, 0xf, false);
  x += __builtin_amdgcn_update_dpp(0, x, 0x118, 0xf, 0xf, false);
  x += __builtin_amdgcn_update_dpp(0, x, 0x142, 0xa, 0xf, false);
  x += __builtin_amdgcn_update_dpp(0, x, 0x143, 0xc, 0xf, false);
  return x;
}
DI void dma16(const void* sbase, unsigned voff, unsigned lds_base) {
  asm volatile("s_mov_b32 m0, %2\n\ts_nop 0\n\tglobal_load_lds_dwordx4 %0, %1" ::"v"(voff), "s"(sbase), "s"(lds_base) : "memory");
}
DI void dma16_dev(const void* sbase, unsigned voff, unsigned lds_base) {
  asm volatile("s_mov_b32 m0, %2\n\ts_nop 0\n\tglobal_load_lds_dwordx4 %0, %1 sc1" ::"v"(voff), "s"(sbase), "s"(lds_base) : "memory");
}
DI void bdma16_dev(u32x4 srd, unsigned voff, unsigned lds_base) {
  asm volatile("s_mov_b32 m0, %2\n\ts_nop 0\n\tbuffer_load_dwordx4 %0, %1, 0 offen sc1 lds" ::"v"(voff), "s"(srd), "s"(lds_base) : "memory");
}
DI void dma4_dev(const void* sbase, unsigned voff, unsigned lds_base) {
  asm volatile("s_mov_b32 m0, %2\n\ts_nop 0\n\tglobal_load_lds_dword %0, %1 sc1" ::"v"(voff), "s"(sbase), "s"(lds_base) : "memory");
}
DI void wait_vm0() { asm volatile("s_waitcnt vmcnt(0)" ::: "memory"); }
DI void barrier() { asm volatile("s_waitcnt lgkmcnt(0)\n\ts_barrier" ::: "memory"); }

#define STAMP(i)

DI void silu4(const f32x16& x, int i, unsigned& m0, unsigned& m1) {
  f32x2 a = {x[4 * i], x[4 * i + 1]}, b = {x[4 * i + 2], x[4 * i + 3]};
  f32x2 ea = {__builtin_amdgcn_exp2f(a[0]), __builtin_amdgcn_exp2f(a[1])};
  f32x2 eb = {__builtin_amdgcn_exp2f(b[0]), __builtin_amdgcn_exp2f(b[1])};
  ea += 1.0f;
  eb += 1.0f;
  f32x2 ra = {__builtin_amdgcn_rcpf(ea[0]), __builtin_amdgcn_rcpf(ea[1])};
  f32x2 rb = {__builtin_amdgcn_rcpf(eb[0]), __builtin_amdgcn_rcpf(eb[1])};
  a *= ra;
  b *= rb;
  m0 = __builtin_bit_cast(unsigned, __builtin_convertvector(a, bf16x2));
  m1 = __builtin_bit_cast(unsigned, __builtin_convertvector(b, bf16x2));
}

__global__ __launch_bounds__(512, 2) void egnn_fused(const u16* __restrict__ h, const u16* __restrict__ pos,
                                                     const u32x2* __restrict__ eattr, const int* __restrict__ src,
                                                     const int* __restrict__ dst, const u16* __restrict__ we,
                                                     const u16* __restrict__ wx, const u16* __restrict__ wh,
                                                     char* ws, u16* __restrict__ out) {
  __shared__ __attribute__((aligned(256))) char lds[L_END];
  const unsigned lbase = (unsigned)(size_t)lds;
  const int tid = threadIdx.x, lane = tid & 63;
  const int wv = __builtin_amdgcn_readfirstlane(tid >> 6);
  const int w = blockIdx.x;
  const int b = (w & 7) * 32 + (w >> 3);
  const int n0 = bucket_start(b), nsz = bucket_start(b + 1) - n0;
  const int r = lane & 31, hh = lane >> 5;
  const int blk = (lane >> 4) & 1, q4 = (lane & 15) >> 2, p4 = lane & 3;
  const __amdgpu_buffer_rsrc_t wsr = __builtin_amdgcn_make_buffer_rsrc(ws, 0, WS_END, 0x00020000);
  STAMP(0);

  int d[3], s[3];
  u32x2 a[3];
  unsigned tagv;
  const bool v2 = tid < CH - 1024;
  {
    const unsigned e0 = (unsigned)(w * CH + tid) * 4, e2 = v2 ? e0 + 4096 : e0;
    asm volatile("global_load_dword %0, %10, %14\n\tglobal_load_dword %1, %10, %14 offset:2048\n\tglobal_load_dword %2, %11, %14\n\t"
                 "global_load_dword %3, %10, %15\n\tglobal_load_dword %4, %10, %15 offset:2048\n\tglobal_load_dword %5, %11, %15\n\t"
                 "global_load_dwordx2 %6, %12, %16\n\tglobal_load_dwordx2 %7, %13, %16\n\tglobal_load_dwordx2 %8, %18, %16\n\t"
                 "global_load_dword %9, %19, %17 sc1"
                 : "=&v"(d[0]), "=&v"(d[1]), "=&v"(d[2]), "=&v"(s[0]), "=&v"(s[1]), "=&v"(s[2]), "=&v"(a[0]), "=&v"(a[1]), "=&v"(a[2]), "=&v"(tagv)
                 : "v"(e0), "v"(e2), "v"(2 * e0), "v"(2 * e0 + 4096), "s"(dst), "s"(src), "s"(eattr), "s"(ws), "v"(2 * e2), "v"(WS_EP)
                 : "memory");
  }
  if (tid < 256) *(u32*)(lds + L_HIST + tid * 4) = 0u;
  else {
    const int t = tid - 256;
    if (t < 32) {
      const int j = (t >> 2) - 4 * ((t >> 1) & 1) - 2 * (t & 1);
      *(u32*)(lds + L_OH + t * 4) = j == 0 ? 0x3F80u : j == 1 ? 0x3F800000u : 0u;
      if (t < 16) *(u32*)(lds + L_GC + t * 4) = 0u;
    }
  }
  barrier();
  {
    const int q = lane >> 4, c = lane & 15;
    const unsigned wlb = (unsigned)(q * 256 + 16 * (c ^ (q << 2)));
    const int row = 4 * wv + q;
#pragma unroll
    for (int i = 0; i < 4; i++) dma16(we, (wlb ^ (16 * i)) + (32 + 4 * wv + i) * 1024, lbase + L_W + (32 + 4 * wv + i) * 1024);
    dma16(h, (unsigned)(min(n0 + row, NN - 1) * 256 + 16 * (c ^ (row & 15))), lbase + L_H + wv * 1024);
    if (wv < 2) dma16(h, (unsigned)(min(n0 + 32 + row, NN - 1) * 256 + 16 * (c ^ (row & 15))), lbase + L_H + (8 + wv) * 1024);
    if (wv == 3) dma16(we, 65536 + lane * 16, lbase + 65536);
    if (wv == 4 && lane < 16) dma16(we, 66560 + lane * 16, lbase + 66560);
    if (wv == 5 && lane < 16) dma16(wx, lane * 16, lbase + L_WX);
#pragma unroll
    for (int i = 0; i < 4; i++) dma16(we, (wlb ^ (16 * i)) + (4 * wv + i) * 1024, lbase + L_W + (4 * wv + i) * 1024);
  }
  asm volatile("s_waitcnt vmcnt(9)"
               : "+v"(d[0]), "+v"(d[1]), "+v"(d[2]), "+v"(s[0]), "+v"(s[1]), "+v"(s[2]), "+v"(a[0]), "+v"(a[1]), "+v"(a[2]), "+v"(tagv)::"memory");
  STAMP(1);
  int key[3];
  unsigned rank[3];
#pragma unroll
  for (int k = 0; k < 3; k++) {
    key[k] = (int)((unsigned)__umul24((unsigned)d[k], 429497u) >> 24);
    rank[k] = 0;
    if (k < 2 || v2) rank[k] = atomicAdd((unsigned*)(lds + L_HIST) + key[k], 1u);
  }
#pragma unroll
  for (int k = 0; k < 3; k++) {
    const int lc = d[k] - bucket_start(key[k]);
    const int g = (lc * 205) >> 10;
    s[k] = (s[k] << 8) | (lc + 3 * g);
    PIN(s[k]);
  }
  const unsigned tag = (unsigned)__builtin_amdgcn_readfirstlane((int)tagv) + 1u;
  const int T = wv & 3, nb = wv >> 2;
  const int loc = 5 * (4 * nb + (r >> 3)) + (r & 7);
  const bool valid = (r & 7) < 5 && loc < nsz;
  const int ha = L_H + min(loc, 39) * 256 + 16 * (hh ^ (min(loc, 39) & 15));
  const unsigned trx0 = lbase + L_W + (unsigned)((8 * hh + q4) * 256), trs0 = (unsigned)((q4 << 2) | (2 * hh));
  asm volatile("s_waitcnt vmcnt(4)" ::: "memory");
  barrier();
  STAMP(2);
  u32x4 off4, hf[8];
  unsigned oend;
  {
    const u32x4 cnt = *(const u32x4*)(lds + L_HIST + lane * 16);
    const unsigned ch = (unsigned)(8 * (T & 1) + 4 * (p4 & 1) + 2 * blk + (T >> 1));
    const unsigned b0 = trx0 + 32768 + 16 * (ch ^ trs0) + 8 * (p4 >> 1);
    const unsigned b1 = trx0 + 32768 + 1024 + 16 * (ch ^ trs0 ^ 1) + 8 * (p4 >> 1);
    u32x4 wf[8];
#pragma unroll
    for (int ks = 0; ks < 8; ks++) {
      hf[ks] = *(const u32x4*)(lds + (ha ^ (32 * ks)));
      wf[ks] = tr_read2(b0 + ks * 4096, b1 + ks * 4096);
    }
    SB();
    const unsigned tot = cnt[0] + cnt[1] + cnt[2] + cnt[3];
    oend = (unsigned)wave_incl_scan((int)tot);
    off4[0] = oend - tot;
    off4[1] = off4[0] + cnt[0];
    off4[2] = off4[1] + cnt[1];
    off4[3] = off4[2] + cnt[2];
    *(u32x4*)(lds + L_OFF + lane * 16) = off4;
#pragma unroll
    for (int k = 0; k < 3; k++) rank[k] += *(const u32*)(lds + L_OFF + key[k] * 4);
    f32x16 acc0 = zero16(), acc1 = zero16();
#pragma unroll
    for (int ks = 0; ks < 8; ks += 2) {
      acc0 = mfma(wf[ks], hf[ks], acc0);
      acc1 = mfma(wf[ks + 1], hf[ks + 1], acc1);
    }
#pragma unroll
    for (int k = 0; k < 3; k++) {
      if (k < 2 || v2) {
        u32* rp = (u32*)(lds + L_ST + rank[k] * 12);
        rp[0] = a[k][0];
        rp[1] = a[k][1];
        rp[2] = (unsigned)s[k];
      }
    }
    u32x4 bt0, bt1;
#pragma unroll
    for (int i = 0; i < 4; i++) {
      bt0[i] = pk2((acc0[2 * i] + acc1[2 * i]) * NLOG2E, (acc0[2 * i + 1] + acc1[2 * i + 1]) * NLOG2E);
      bt1[i] = pk2((acc0[8 + 2 * i] + acc1[8 + 2 * i]) * NLOG2E, (acc0[9 + 2 * i] + acc1[9 + 2 * i]) * NLOG2E);
    }
    if ((r & 7) < 5) {
      const int bo = L_ROWS + loc * 256 + 16 * ((4 * T + 2 * hh) ^ (loc & 15));
      *(u32x4*)(lds + bo) = bt0;
      *(u32x4*)(lds + (bo ^ 16)) = bt1;
    }
  }
  wait_vm0();
  barrier();
  STAMP(4);
  auto stage_pos = [&]() {
#pragma unroll
    for (int i = 0; i < 7; i++) dma16(pos, (unsigned)((8 * i + wv) * 1024 + lane * 16), lbase + L_ROWS + (8 * i + wv) * 1024);
    if (wv < 2 || (wv == 2 && lane < 38)) dma16(pos, (unsigned)((56 + wv) * 1024 + lane * 16), lbase + L_ROWS + (56 + wv) * 1024);
  };
  u32x2 at[4];
  {
    const u32x4 v0 = *(const u32x4*)(lds + L_ST + tid * 16);
    const u32x4 v1 = *(const u32x4*)(lds + L_ST + 8192 + min(tid, 447) * 16);
    u32x4 v2_ = *(const u32x4*)(lds + L_ROWS + tid * 16);
    u32x4 v3 = *(const u32x4*)(lds + L_ROWS + 8192 + (tid & 127) * 16);
    const unsigned chA = (unsigned)(4 * T + 2 * blk + (p4 >> 1));
    const unsigned a0 = trx0 + 16 * (chA ^ trs0) + 8 * (p4 & 1), a1 = trx0 + 1024 + 16 * (chA ^ trs0 ^ 1) + 8 * (p4 & 1);
    u32x4 wf[8];
#pragma unroll
    for (int ks = 0; ks < 8; ks++) wf[ks] = tr_read2(a0 + ks * 4096, a1 + ks * 4096);
    SB();
    __builtin_amdgcn_raw_buffer_store_b128(v0, wsr, WS_R + (unsigned)(w * CHS * 12 + tid * 16), 0, 16);
    if (tid < 448) __builtin_amdgcn_raw_buffer_store_b128(v1, wsr, WS_R + (unsigned)(w * CHS * 12 + 8192 + tid * 16), 0, 16);
    const int row = tid >> 4;
    __builtin_amdgcn_raw_buffer_store_b128(v2_, wsr, WS_BT + (unsigned)((n0 + row) * 256 + 16 * ((tid ^ row) & 15)), 0, 16);
    if (tid < 128 && 32 + row < nsz) __builtin_amdgcn_raw_buffer_store_b128(v3, wsr, WS_BT + (unsigned)((n0 + 32 + row) * 256 + 16 * ((tid ^ row) & 15)), 0, 16);
    asm volatile("" : "+v"(v2_), "+v"(v3)::"memory");
    stage_pos();
    f32x16 acc0 = zero16(), acc1 = zero16();
#pragma unroll
    for (int ks = 0; ks < 8; ks += 2) {
      acc0 = mfma(hf[ks], wf[ks], acc0);
      acc1 = mfma(hf[ks + 1], wf[ks + 1], acc1);
    }
#pragma unroll
    for (int i = 0; i < 4; i++) {
      at[i][0] = pk2((acc0[4 * i] + acc1[4 * i]) * NLOG2E, (acc0[4 * i + 1] + acc1[4 * i + 1]) * NLOG2E);
      at[i][1] = pk2((acc0[4 * i + 2] + acc1[4 * i + 2]) * NLOG2E, (acc0[4 * i + 3] + acc1[4 * i + 3]) * NLOG2E);
      PIN(at[i]);
    }
  }
  STAMP(3);
  wait_vm0();
  barrier();
  STAMP(5);
  if (wv == 0) {
    const u32x4 g0 = {tag, off4[0] | (off4[1] << 16), tag, off4[1] | (off4[2] << 16)};
    const u32x4 g1 = {tag, off4[2] | (off4[3] << 16), tag, off4[3] | (oend << 16)};
    const unsigned off = WS_TAB + (unsigned)(w * NB + 4 * lane) * 8;
    __builtin_amdgcn_raw_buffer_store_b128(g0, wsr, off, 0, 16);
    __builtin_amdgcn_raw_buffer_store_b128(g1, wsr, off + 16, 0, 16);
  }
#pragma unroll
  for (int i = 0; i < 4; i++) *(u32x2*)(lds + L_A + (4 * nb + i) * 2048 + T * 512 + lane * 8) = at[i];
  {
    const u32x4 z = {0u, 0u, 0u, 0u};
    *(u32x4*)(lds + L_S + tid * 16) = z;
    *(u32x4*)(lds + L_S + 8192 + tid * 16) = z;
    if (tid < 256) *(u32x4*)(lds + L_S + 16384 + tid * 16) = z;
  }
  if (tid >= 256) {
    const int t = tid - 256, v = t >> 5, q = (t >> 3) & 3, i = t & 7;
    *(u32*)(lds + L_FK + t * 4) = (unsigned)(i < 4 ? 128 + 32 * q + 4 * v + i : 16 * v + 4 * (i - 4) + q) << 8;
  }
  u32x4 extB[4];
#pragma unroll
  for (int t = 0; t < 4; t++) {
    const u16* wp = (const u16*)(lds + 65536) + 32 * t + r;
    const float w0 = bf2f(wp[(1 + 2 * hh) * 128]) * NLOG2E, w1 = bf2f(wp[(2 + 2 * hh) * 128]) * NLOG2E, w2 = bf2f(wp[0]) * NLOG2E;
    extB[t][0] = 0u;
    extB[t][1] = 0u;
    extB[t][2] = pk2(w0, w1);
    extB[t][3] = hh ? 0u : pk2(w2, 0.f);
  }
  u32x4 I0, I1;
  {
    const unsigned one = (r & 1) ? 0x3F800000u : 0x3F80u;
#pragma unroll
    for (int jj = 0; jj < 4; jj++) {
      const bool m = ((r & 7) >> 1) == jj;
      I0[jj] = (m && (r >> 3) == hh) ? one : 0u;
      I1[jj] = (m && (r >> 3) == 2 + hh) ? one : 0u;
    }
  }
  const int rowbuf = L_ROWS + wv * ROWBUF;
  unsigned kdma = 16 * ((lane & 15) ^ (4 * (lane >> 4)));
  const int rd0 = rowbuf + 1040 * (r & 3) + 4352 * ((r >> 2) & 1) + (r >> 3) * 256;
  int rdA = rd0 + 64 * ((2 * hh) ^ (r >> 3)), rdB = rd0 + 64 * ((2 * hh + 1) ^ (r >> 3));
  int swr0 = L_S + 32 * (r >> 4) + 16 * ((r >> 2) & 1) + 8 * ((r >> 3) & 1) + 2 * (r & 3) + 160 * hh;
  int srd0 = L_S + r * 80 + 16 * hh;
  const char* btab = ws + WS_BT;
  f32x16 Y[4];
#pragma unroll
  for (int t = 0; t < 4; t++) {
    Y[t] = zero16();
    PIN(Y[t]);
    PIN(extB[t]);
  }
  PIN(I0); PIN(I1); PIN(kdma); PIN(rdA); PIN(rdB); PIN(swr0); PIN(srd0);
  STAMP(6);

  bool wh_loaded = false, slow = false;
  int lo = 0, quota = MAPW, gq = wv;
  while (true) {
    wh_loaded = false;
    int tq = tid;
    PIN(tq);
    const int lq = tq & 63;
    int kc, kfirst, kpre, wtot;
    {
      const unsigned tabme = WS_TAB + (unsigned)((tq >> 1) * NB + b) * 8;
      u32x2 gq2 = {tag + 1u, 0u};
      bool pf = slow;
#pragma clang loop unroll(disable)
      for (int spin = 0; spin < (1 << 22); spin++) {
        if (gq2[0] != tag) asm volatile("global_load_dwordx2 %0, %1, %2 sc1\n\ts_waitcnt vmcnt(0)" : "=&v"(gq2) : "v"(tabme), "s"(ws) : "memory");
        const unsigned long long okm = __ballot(gq2[0] == tag);
        if (!pf && ((okm >> (2 * (w >> 3))) & 1)) {
          pf = true;
          const int pw = 32 * wv + (w >> 3);
          const unsigned pb = (unsigned)bucket_start((pw & 7) * 32 + (pw >> 3)) * 256 + lq * 16;
          asm volatile("s_mov_b32 m0, %4\n\ts_nop 0\n\t"
                       "global_load_lds_dwordx4 %0, %3 sc1\n\tglobal_load_lds_dwordx4 %0, %3 offset:1024 sc1\n\t"
                       "global_load_lds_dwordx4 %0, %3 offset:2048 sc1\n\tglobal_load_lds_dwordx4 %0, %3 offset:3072 sc1\n\t"
                       "global_load_lds_dwordx4 %1, %3 sc1\n\tglobal_load_lds_dwordx4 %1, %3 offset:1024 sc1\n\t"
                       "global_load_lds_dwordx4 %1, %3 offset:2048 sc1\n\tglobal_load_lds_dwordx4 %1, %3 offset:3072 sc1\n\t"
                       "global_load_lds_dwordx4 %2, %3 sc1\n\tglobal_load_lds_dwordx4 %2, %3 offset:1024 sc1"
                       ::"v"(pb), "v"(pb + 4096), "v"(pb + 8192), "s"(btab), "s"(lbase + L_PF) : "memory");
        }
        if (okm == ~0ull) break;
        __builtin_amdgcn_s_sleep(1);
      }
      const int o0 = min((int)(gq2[1] & 0xffffu), CH), o1 = min((int)(gq2[1] >> 16), CH);
      kc = max(o1 - o0, 0);
      kfirst = (tq >> 1) * CHS + o0;
      const int inc = wave_incl_scan((tq & 1) ? 0 : kc);
      kpre = inc - kc;
      wtot = __builtin_amdgcn_readlane(inc, 63);
      if (!slow && lq == 0) atomicMax((unsigned*)(lds + L_GC + 32), (unsigned)wtot);
    }
    STAMP(7);
    {
      const int map = L_MAP + wv * MAPW * 4;
      const int nw = min(wtot - lo, quota);
      const int jhi = min(kc, lo + quota - kpre);
#pragma clang loop vectorize(disable) unroll(disable)
      for (int j = max(0, lo - kpre) + ((tq ^ max(0, lo - kpre)) & 1); j < jhi; j += 2) *(u32*)(lds + map + (kpre + j - lo) * 4) = (unsigned)(kfirst + j) * 12u;
      for (int p0 = 0; p0 < nw; p0 += 192) {
        u32x3 q[3];
        unsigned ix[3];
#pragma unroll
        for (int k = 0; k < 3; k++) {
          const int p = p0 + 64 * k + lq;
          const unsigned mi = *(const u32*)(lds + map + min(p, MAPW - 1) * 4);
          ix[k] = p < nw ? mi : 0u;
        }
        asm volatile("global_load_dwordx3 %0, %3, %6 sc1\n\tglobal_load_dwordx3 %1, %4, %6 sc1\n\tglobal_load_dwordx3 %2, %5, %6 sc1\n\ts_waitcnt vmcnt(0)"
                     : "=&v"(q[0]), "=&v"(q[1]), "=&v"(q[2]) : "v"(ix[0]), "v"(ix[1]), "v"(ix[2]), "s"(ws + WS_R) : "memory");
        unsigned rk[3];
        u32x2 pd[3], ps[3];
        int dn[3], sn[3];
#pragma unroll
        for (int k = 0; k < 3; k++) {
          const unsigned g = (q[k][2] >> 3) & 7u;
          rk[k] = CAPW;
          if (p0 + 64 * k + lq < nw) rk[k] = atomicAdd((unsigned*)(lds + L_GC) + g, 1u);
          dn[k] = n0 + (int)(q[k][2] & 63u) - 3 * (int)g;
          sn[k] = min((int)(q[k][2] >> 8), NN - 1);
          const int od = L_ROWS + ((6 * dn[k]) & ~3), os = L_ROWS + ((6 * sn[k]) & ~3);
          pd[k][0] = *(const u32*)(lds + od); pd[k][1] = *(const u32*)(lds + od + 4);
          ps[k][0] = *(const u32*)(lds + os); ps[k][1] = *(const u32*)(lds + os + 4);
        }
#pragma unroll
        for (int k = 0; k < 3; k++) {
          const unsigned long long ud = (((unsigned long long)pd[k][1] << 32) | pd[k][0]) >> ((dn[k] & 1) * 16);
          const unsigned long long us = (((unsigned long long)ps[k][1] << 32) | ps[k][0]) >> ((sn[k] & 1) * 16);
          const unsigned dlo = (unsigned)ud, dhi = (unsigned)(ud >> 32), slo = (unsigned)us, shi = (unsigned)(us >> 32);
          const unsigned dxy = pk2(bf2f(dlo) - bf2f(slo), bfhi(dlo) - bfhi(slo));
          const unsigned dz_ = pk2(bf2f(dhi) - bf2f(shi), 0.f);
          const float dx = bf2f(dxy), dy = bfhi(dxy), dz = bf2f(dz_);
          const unsigned sxy = pk2(dx * dx, dy * dy), sz = pk2(dz * dz, 0.f);
          const unsigned d2 = pk2((bf2f(sxy) + bfhi(sxy)) + bf2f(sz), 0.f) & 0xffffu;
          const u32x4 rec = {q[k][0], d2 | (dxy << 16), q[k][1], (dxy >> 16) | (dz_ << 16)};
          if (rk[k] < CAPW) {
            const unsigned e = ((q[k][2] >> 3) & 7u) * CAPW + rk[k];
            *(u32x4*)(lds + L_R1 + e * 16) = rec;
            *(u32*)(lds + L_R2 + e * 4) = q[k][2];
          }
        }
      }
    }
    barrier();
    STAMP(8);
    if (w == 0 && tid == 0) __builtin_amdgcn_raw_buffer_store_b32(tag, wsr, WS_EP, 0, 16);
    const int cl = *(const u32*)(lds + L_GC + (lane & 7) * 4);
    const int maxtot = __builtin_amdgcn_readfirstlane(*(const u32*)(lds + L_GC + 32));
    if (!slow) {
      const int kk = (cl << 3) | (lane & 7);
      int rk = 0;
      rk += __builtin_amdgcn_update_dpp(0, kk, 0x121, 0xf, 0xf, false) > kk;
      rk += __builtin_amdgcn_update_dpp(0, kk, 0x122, 0xf, 0xf, false) > kk;
      rk += __builtin_amdgcn_update_dpp(0, kk, 0x123, 0xf, 0xf, false) > kk;
      rk += __builtin_amdgcn_update_dpp(0, kk, 0x124, 0xf, 0xf, false) > kk;
      rk += __builtin_amdgcn_update_dpp(0, kk, 0x125, 0xf, 0xf, false) > kk;
      rk += __builtin_amdgcn_update_dpp(0, kk, 0x126, 0xf, 0xf, false) > kk;
      rk += __builtin_amdgcn_update_dpp(0, kk, 0x127, 0xf, 0xf, false) > kk;
      const int cmax = __any(cl > CAPW) ? CAPW + 1 : 0;
      if (maxtot > MAPW || cmax > CAPW) {
        slow = true;
        quota = CAPW / 8;
        barrier();
        if (tid < 8) *(u32*)(lds + L_GC + tid * 4) = 0u;
        barrier();
        continue;
      }
      gq = __builtin_ctzll(__ballot(rk == (wv < 4 ? wv : 11 - wv)));
    }
    const int n = __builtin_amdgcn_readlane(cl, gq);
    const int swr = swr0 + gq * 2560, srd = srd0 + gq * 2560;
    const int l1 = L_R1 + gq * CAPW * 16, l2 = L_R2 + gq * CAPW * 4;
    if (n > 0) {
    const int nt = (n + 31) >> 5;
    if (lane < 32 * nt - n) {
      const u32x4 z = {0u, 0u, 0u, 0u};
      *(u32*)(lds + l2 + (n + lane) * 4) = 0u;
      *(u32x4*)(lds + l1 + (n + lane) * 16) = z;
    }
#pragma unroll
    for (int t = 0; t < 4; t++) {
      const u32x2 v = *(const u32x2*)(lds + L_A + gq * 2048 + t * 512 + lane * 8);
      extB[t][0] = v[0];
      extB[t][1] = v[1];
    }

    u32x4 rows[8], extA, S0, S1, ra, rb, m1p = {0u, 0u, 0u, 0u};
    u32x2 r1;
    unsigned r2;
    int sp;
    f32x16 XA, XB;
    auto list_read = [&](int tl) {
      const int list = tl < nt ? l2 + tl * 128 : L_FK + wv * 128;
      ra = *(const u32x4*)(lds + list + (lane >> 4) * 32);
      rb = *(const u32x4*)(lds + list + (lane >> 4) * 32 + 16);
    };
    auto dma_piece = [&](int tl, int i) {
      const unsigned v = i < 4 ? ra[i & 3] : rb[i & 3];
      const unsigned long long bp = (unsigned long long)(tl < nt ? btab : (const char*)wh);
      const u32x4 srd = {(unsigned)bp, (unsigned)(bp >> 32) & 0xffffu, tl < nt ? (unsigned)(NN * 256) : 65536u, 0x00020000u};
      bdma16_dev(srd, (v & 0xffffff00u) | kdma, lbase + rowbuf + piece_off(i));
    };
    auto meta_read = [&](int tl) {
      r1 = *(const u32x2*)(lds + l1 + (tl * 32 + r) * 16 + hh * 8);
      r2 = *(const u32*)(lds + l2 + (tl * 32 + r) * 4);
    };
    auto meta_use = [&](int tl) {
      const unsigned dl = r2 & 7u;
      const u32x2 oh = *(const u32x2*)(lds + L_OH + dl * 16 + hh * 8);
      extA[0] = oh[0]; extA[1] = oh[1]; extA[2] = r1[0]; extA[3] = r1[1];
      sp = (tl * 32 + r < n) ? swr + (int)dl * 320 : L_DUM;
    };
    auto s_write = [&]() {
      *(u16*)(lds + sp) = (u16)(hh ? extA[3] : 0x3F80u);
      *(u16*)(lds + sp + 80) = (u16)(extA[3] >> 16);
    };
    auto rows_read = [&]() {
#pragma unroll
      for (int j = 0; j < 8; j++) rows[j] = *(const u32x4*)(lds + (j < 4 ? rdA : rdB) + 16 * (j & 3));
    };
    auto rows_pin = [&]() {
      asm volatile("" : "+v"(rows[0]), "+v"(rows[1]), "+v"(rows[2]), "+v"(rows[3]), "+v"(rows[4]), "+v"(rows[5]), "+v"(rows[6]), "+v"(rows[7]));
    };

    list_read(0);
    meta_read(0);
#pragma unroll
    for (int i = 0; i < 8; i++) dma_piece(0, i);
    list_read(1);
    meta_use(0);
    s_write();
    S1 = m1p;
    wait_vm0();
    rows_read();
    rows_pin();
#pragma unroll
    for (int i = 0; i < 8; i++) dma_piece(1, i);
    XA = mfma(rows[0], I0, zero16());
    XA = mfma(rows[1], I1, XA);
    XA = mfma(extA, extB[0], XA);
    STAMP(9);

    const int nfull = n >> 5;
    for (int tl = 0; tl < nfull; tl++) {
#pragma unroll
      for (int t = 0; t < 4; t++) {
        f32x16& Xc = (t & 1) ? XB : XA;
        f32x16& Xn = (t & 1) ? XA : XB;
        const int tn = (t + 1) & 3;
        float e[16];
        SB();
        if (t == 0) {
          S0 = *(const u32x4*)(lds + srd);
        }
        if (t == 2) meta_read(tl + 1);
        Xn = mfma(rows[2 * tn], I0, zero16());
        PIN(Xn);
        SB();
#pragma unroll
        for (int i = 0; i < 4; i++) e[i] = __builtin_amdgcn_exp2f(Xc[i]);
        if (t == 3) { dma_piece(tl + 2, 0); dma_piece(tl + 2, 1); }
        SB();
        Xn = mfma(rows[2 * tn + 1], I1, Xn);
        PIN(Xn);
        SB();
#pragma unroll
        for (int i = 4; i < 8; i++) e[i] = __builtin_amdgcn_exp2f(Xc[i]);
        if (t == 3) { dma_piece(tl + 2, 2); dma_piece(tl + 2, 3); }
        SB();
        Xn = mfma(extA, extB[tn], Xn);
        PIN(Xn);
        SB();
#pragma unroll
        for (int i = 8; i < 12; i++) e[i] = __builtin_amdgcn_exp2f(Xc[i]);
        if (t == 3) { dma_piece(tl + 2, 4); dma_piece(tl + 2, 5); }
        SB();
        Y[(t + 3) & 3] = mfma(S1, m1p, Y[(t + 3) & 3]);
        SB();
        if (t == 0) {
          S1 = *(const u32x4*)(lds + srd + 32);
          *(u16*)(lds + sp) = 0;
          *(u16*)(lds + sp + 80) = 0;
        }
        if (t == 2) {
          wait_vm0();
          rows_read();
          list_read(tl + 2);
          meta_use(tl + 1);
        }
#pragma unroll
        for (int i = 12; i < 16; i++) e[i] = __builtin_amdgcn_exp2f(Xc[i]);
        if (t == 3) { dma_piece(tl + 2, 6); dma_piece(tl + 2, 7); s_write(); }
        SB();
        f32x2 dd[8];
#pragma unroll
        for (int i = 0; i < 8; i++) { dd[i][0] = e[2 * i]; dd[i][1] = e[2 * i + 1]; dd[i] += 1.0f; }
        SB();
#pragma unroll
        for (int i = 0; i < 8; i++) { dd[i][0] = __builtin_amdgcn_rcpf(dd[i][0]); dd[i][1] = __builtin_amdgcn_rcpf(dd[i][1]); }
        SB();
        u32x4 m0;
#pragma unroll
        for (int i = 0; i < 4; i++) { f32x2 x = {Xc[2 * i], Xc[2 * i + 1]}; dd[i] *= x; }
#pragma unroll
        for (int i = 0; i < 4; i++) m0[i] = __builtin_bit_cast(unsigned, __builtin_convertvector(dd[i], bf16x2));
        SB();
#pragma unroll
        for (int i = 4; i < 8; i++) { f32x2 x = {Xc[2 * i], Xc[2 * i + 1]}; dd[i] *= x; }
        SB();
        Y[t] = mfma(S0, m0, Y[t]);
        SB();
#pragma unroll
        for (int i = 0; i < 4; i++) m1p[i] = __builtin_bit_cast(unsigned, __builtin_convertvector(dd[4 + i], bf16x2));
        if (t == 2) rows_pin();
      }
    }
    SB();
    Y[3] = mfma(S1, m1p, Y[3]);
    if (nfull < nt) {
      const int c = n & 31;
      S0 = *(const u32x4*)(lds + srd);
      S1 = *(const u32x4*)(lds + srd + 32);
      *(u16*)(lds + sp) = 0;
      *(u16*)(lds + sp + 80) = 0;
#pragma unroll
      for (int t = 0; t < 4; t++) {
        f32x16& Xc = (t & 1) ? XB : XA;
        f32x16& Xn = (t & 1) ? XA : XB;
        if (t < 3) {
          Xn = mfma(rows[2 * t + 2], I0, zero16());
          Xn = mfma(rows[2 * t + 3], I1, Xn);
          Xn = mfma(extA, extB[t + 1], Xn);
        }
        unsigned m[8] = {0u, 0u, 0u, 0u, 0u, 0u, 0u, 0u};
        silu4(Xc, 0, m[0], m[1]);
        if (c > 8) silu4(Xc, 1, m[2], m[3]);
        if (c > 16) silu4(Xc, 2, m[4], m[5]);
        if (c > 24) silu4(Xc, 3, m[6], m[7]);
        const u32x4 m0 = {m[0], m[1], m[2], m[3]}, m1 = {m[4], m[5], m[6], m[7]};
        Y[t] = mfma(S0, m0, Y[t]);
        Y[t] = mfma(S1, m1, Y[t]);
      }
    }
    wh_loaded = true;
    wait_vm0();
    }
    if (!slow) break;
    lo += quota;
    if (lo >= maxtot) break;
    barrier();
    if (tid < 8) *(u32*)(lds + L_GC + tid * 4) = 0u;
    stage_pos();
    wait_vm0();
    barrier();
  }
  STAMP(10);
  if (!wh_loaded) {
    wait_vm0();
    const u32x4 fa = *(const u32x4*)(lds + L_FK + wv * 128 + (lane >> 4) * 32), fb = *(const u32x4*)(lds + L_FK + wv * 128 + (lane >> 4) * 32 + 16);
#pragma unroll
    for (int i = 0; i < 8; i++) dma16(wh, (i < 4 ? fa[i & 3] : fb[i & 3]) | kdma, lbase + rowbuf + piece_off(i));
    wait_vm0();
  }

  const int nrow = min(n0 + loc, NN - 1);
  const u32x4 res0 = *(const u32x4*)(h + (size_t)nrow * 128 + 32 * T + 16 * hh);
  const u32x4 res1 = *(const u32x4*)(h + (size_t)nrow * 128 + 32 * T + 16 * hh + 8);
  const bool pval = lane < 15 && 15 * gq + lane < 3 * nsz;
  const unsigned pofs = (unsigned)(3 * n0 + 15 * gq + lane);
  unsigned pin = 0;
  if (pval) pin = pos[pofs];
#pragma unroll
  for (int i = 0; i < 3; i++) {
    if (i < 2 || hh == 0) {
      const u32x2 mv = {pk2(Y[0][4 * i] * NLN2, Y[1][4 * i] * NLN2), pk2(Y[2][4 * i] * NLN2, Y[3][4 * i] * NLN2)};
      *(u32x2*)(lds + L_M + (5 * gq + 2 * i + hh) * MSTR + 8 * r) = mv;
    }
  }
  barrier();
  STAMP(11);
  {
    const unsigned wa = lbase + L_ROWS + 2080 * hh + 256 * q4 + 64 * (T ^ q4) + 32 * (p4 & 1) + 16 * blk + 8 * (p4 >> 1);
    const int ma = L_M + min(loc, 39) * MSTR + 16 * hh;
    f32x16 P = zero16(), P2 = zero16();
#pragma unroll
    for (int ks = 0; ks < 8; ks++) {
      P = mfma(tr_read2(wa + ks * ROWBUF + 4352, wa + ks * ROWBUF + 4352 + 1040), *(const u32x4*)(lds + (ha ^ (32 * ks))), P);
      P2 = mfma(tr_read2(wa + ks * ROWBUF, wa + ks * ROWBUF + 1040), *(const u32x4*)(lds + ma + 32 * ks), P2);
    }
    if (valid) {
      u32x4 ov[2];
#pragma unroll
      for (int i = 0; i < 8; i++) {
        const unsigned rs = i < 4 ? res0[i & 3] : res1[i & 3];
        const float x0 = P[2 * i] + P2[2 * i], x1 = P[2 * i + 1] + P2[2 * i + 1];
        const float s0 = x0 * __builtin_amdgcn_rcpf(1.f + __builtin_amdgcn_exp2f(x0 * NLOG2E));
        const float s1 = x1 * __builtin_amdgcn_rcpf(1.f + __builtin_amdgcn_exp2f(x1 * NLOG2E));
        ov[i >> 2][i & 3] = pk2(bf2f(rs) + s0, bfhi(rs) + s1);
      }
      u32x4* op = (u32x4*)(out + (size_t)(n0 + loc) * 128 + 32 * T + 16 * hh);
      op[0] = ov[0];
      op[1] = ov[1];
    }
  }
  {
    float zz[9], wxv[4];
#pragma unroll
    for (int t = 0; t < 4; t++) wxv[t] = bf2f(((const u16*)(lds + L_WX))[32 * t + r]) * NLN2;
#pragma unroll
    for (int i = 0; i < 3; i++) {
#pragma unroll
      for (int cc = 1; cc < 4; cc++) {
        float z = Y[0][4 * i + cc] * wxv[0] + Y[1][4 * i + cc] * wxv[1] + Y[2][4 * i + cc] * wxv[2] + Y[3][4 * i + cc] * wxv[3];
        z += __builtin_bit_cast(float, __builtin_amdgcn_update_dpp(0, __builtin_bit_cast(int, z), 0x111, 0xf, 0xf, false));
        z += __builtin_bit_cast(float, __builtin_amdgcn_update_dpp(0, __builtin_bit_cast(int, z), 0x112, 0xf, 0xf, false));
        z += __builtin_bit_cast(float, __builtin_amdgcn_update_dpp(0, __builtin_bit_cast(int, z), 0x114, 0xf, 0xf, false));
        z += __builtin_bit_cast(float, __builtin_amdgcn_update_dpp(0, __builtin_bit_cast(int, z), 0x118, 0xf, 0xf, false));
        z += __builtin_bit_cast(float, __builtin_amdgcn_update_dpp(0, __builtin_bit_cast(int, z), 0x142, 0xa, 0xf, false));
        zz[3 * i + cc - 1] = z;
      }
    }
    const int zp = L_S + gq * 2560;
    if (r == 31) {
#pragma unroll
      for (int i = 0; i < 9; i++) *(u32*)(lds + zp + 12 * hh + 24 * (i / 3) + 4 * (i % 3)) = __float_as_uint(zz[i]);
    }
    asm volatile("" ::: "memory");
    const float z = __uint_as_float(*(const u32*)(lds + zp + 4 * (lane & 15)));
    if (pval) out[(size_t)NN * 128 + pofs] = (u16)pk2(bf2f(pin) + z, 0.f);
  }
  STAMP(12);
}

extern "C" void kernel_launch(void* const* d_in, const int* in_sizes, int n_in, void* d_out, int out_size, void* d_ws,
                              size_t ws_size, hipStream_t stream) {
  egnn_fused<<<NB, 512, 0, stream>>>((const u16*)d_in[0], (const u16*)d_in[1], (const u32x2*)d_in[2], (const int*)d_in[3],
                                     (const int*)d_in[4], (const u16*)d_in[5], (const u16*)d_in[6], (const u16*)d_in[7],
                                     (char*)d_ws, (u16*)d_out);
}
```

```cpp
#include <hip/hip_runtime.h>
#include <hip/hip_bf16.h>
#include <stdint.h>

typedef __bf16 bf16x8 __attribute__((ext_vector_type(8)));
typedef __bf16 bf16x2 __attribute__((ext_vector_type(2)));
typedef short s16x4 __attribute__((ext_vector_type(4)));
typedef float f32x2 __attribute__((ext_vector_type(2)));
typedef float f32x4 __attribute__((ext_vector_type(4), may_alias));
typedef float f32x16 __attribute__((ext_vector_type(16)));
typedef unsigned u32x2 __attribute__((ext_vector_type(2), may_alias));
typedef unsigned u32x3 __attribute__((ext_vector_type(3)));
typedef unsigned u32x4 __attribute__((ext_vector_type(4), may_alias));
typedef unsigned u32x2a4 __attribute__((ext_vector_type(2), aligned(4), may_alias));
typedef unsigned short u16 __attribute__((may_alias));
typedef unsigned u32 __attribute__((may_alias));
#define LDSP(T) __attribute__((address_space(3))) T*
#define DI __device__ __forceinline__
#define SB() __builtin_amdgcn_sched_barrier(0)
#define PIN(x) asm volatile("" : "+v"(x))

#define O_PRE 0
#define O_STAG 1
#define O_HALF 1
#define O_PRIO 7
constexpr int NN = 10000;
constexpr int CH = 1250, CHS = 1280;
constexpr int NB = 256;
constexpr float NLOG2E = -1.4426950408889634f;
constexpr float NLN2 = -0.6931471805599453f;

constexpr unsigned WS_BT = 0;
constexpr unsigned WS_R = 2560000;
constexpr unsigned WS_TAB = WS_R + NB * CHS * 16;
constexpr unsigned WS_EP = WS_TAB + NB * NB * 8;
constexpr unsigned WS_END = WS_EP + 256;

constexpr int CAPW = 224;
constexpr int ROWBUF = 8496;
constexpr int L_ROWS = 0;
constexpr int L_R1 = 8 * ROWBUF;
constexpr int L_R2 = L_R1 + 8 * CAPW * 16;
constexpr int L_S = L_R2 + 8 * CAPW * 4;
constexpr int L_M = L_S + 8 * 2560;
constexpr int MSTR = 272;
constexpr int L_H = 135168;
constexpr int L_A = L_H + 10240;
constexpr int L_HIST = L_A + 8 * 2048;
constexpr int L_OH = L_HIST + 1024;
constexpr int L_GC = L_OH + 128;
constexpr int L_DUM = L_GC + 64;
constexpr int L_WX = L_DUM + 128;
constexpr int L_END = L_WX + 256;
constexpr int L_W = L_R1;
constexpr int L_EXT = L_W + 65536;
constexpr int L_ST = 47104;
constexpr int L_BS = L_A;
constexpr int L_OFF = L_A + 10240;
constexpr int MAPW = 256;
constexpr int L_MAP = L_M;
constexpr int L_FK = L_HIST;
static_assert(L_ST + 20480 <= L_R1 && L_BS % 256 == 0 && L_W % 32 == 0 && L_W + 65536 <= L_EXT && L_EXT + 1280 <= L_H && L_H % 256 == 0 && L_M + 40 * MSTR <= L_H && L_END <= 163840, "lds map");

DI constexpr int piece_off(int i) { return 1040 * (i & 3) + 4352 * (i >> 2); }
DI float bf2f(unsigned u) { return __uint_as_float(u << 16); }
DI float bfhi(unsigned u) { return __uint_as_float(u & 0xffff0000u); }
DI unsigned pk2(float a, float b) {
  f32x2 v = {a, b};
  return __builtin_bit_cast(unsigned, __builtin_convertvector(v, bf16x2));
}
DI f32x16 mfma(u32x4 a, u32x4 b, f32x16 c) {
  return __builtin_amdgcn_mfma_f32_32x32x16_bf16(__builtin_bit_cast(bf16x8, a), __builtin_bit_cast(bf16x8, b), c, 0, 0, 0);
}
DI f32x16 zero16() {
  f32x16 z;
#pragma unroll
  for (int i = 0; i < 16; i++) z[i] = 0.f;
  return z;
}
DI u32x4 tr_read2(unsigned a0, unsigned a1) {
  const u32x2 lo = __builtin_bit_cast(u32x2, __builtin_amdgcn_ds_read_tr16_b64_v4i16((LDSP(s16x4))(size_t)a0));
  const u32x2 hi = __builtin_bit_cast(u32x2, __builtin_amdgcn_ds_read_tr16_b64_v4i16((LDSP(s16x4))(size_t)a1));
  const u32x4 v = {lo[0], lo[1], hi[0], hi[1]};
  return v;
}
DI int bucket_start(int b) { return (b * 625 + 15) >> 4; }
DI int wave_incl_scan(int x) {
  x += __builtin_amdgcn_update_dpp(0, x, 0x111, 0xf, 0xf, false);
  x += __builtin_amdgcn_update_dpp(0, x, 0x112, 0xf, 0xf, false);
  x += __builtin_amdgcn_update_dpp(0, x, 0x114, 0xf, 0xf, false);
  x += __builtin_amdgcn_update_dpp(0, x, 0x118, 0xf, 0xf, false);
  x += __builtin_amdgcn_update_dpp(0, x, 0x142, 0xa, 0xf, false);
  x += __builtin_amdgcn_update_dpp(0, x, 0x143, 0xc, 0xf, false);
  return x;
}
DI void dma16(const void* sbase, unsigned voff, unsigned lds_base) {
  asm volatile("s_mov_b32 m0, %2\n\ts_nop 0\n\tglobal_load_lds_dwordx4 %0, %1" ::"v"(voff), "s"(sbase), "s"(lds_base) : "memory");
}
DI void dma16_dev(const void* sbase, unsigned voff, unsigned lds_base) {
  asm volatile("s_mov_b32 m0, %2\n\ts_nop 0\n\tglobal_load_lds_dwordx4 %0, %1 sc1" ::"v"(voff), "s"(sbase), "s"(lds_base) : "memory");
}
DI void bdma16_dev(u32x4 srd, unsigned voff, unsigned lds_base) {
  asm volatile("s_mov_b32 m0, %2\n\ts_nop 0\n\tbuffer_load_dwordx4 %0, %1, 0 offen sc1 lds" ::"v"(voff), "s"(srd), "s"(lds_base) : "memory");
}
DI void dma4_dev(const void* sbase, unsigned voff, unsigned lds_base) {
  asm volatile("s_mov_b32 m0, %2\n\ts_nop 0\n\tglobal_load_lds_dword %0, %1 sc1" ::"v"(voff), "s"(sbase), "s"(lds_base) : "memory");
}
DI void wait_vm0() { asm volatile("s_waitcnt vmcnt(0)" ::: "memory"); }
DI void barrier() { asm volatile("s_waitcnt lgkmcnt(0)\n\ts_barrier" ::: "memory"); }

#define STAMP(i)
#define STAMP_OUT()

__global__ __launch_bounds__(512, 2) void egnn_fused(const u16* __restrict__ h, const u16* __restrict__ pos,
                                                     const u32x2* __restrict__ eattr, const int* __restrict__ src,
                                                     const int* __restrict__ dst, const u16* __restrict__ we,
                                                     const u16* __restrict__ wx, const u16* __restrict__ wh,
                                                     char* ws, u16* __restrict__ out) {
  __shared__ __attribute__((aligned(256))) char lds[L_END];
  const unsigned lbase = (unsigned)(size_t)lds;
  const int tid = threadIdx.x, lane = tid & 63;
  const int wv = __builtin_amdgcn_readfirstlane(tid >> 6);
  const int w = blockIdx.x;
  const int b = (w & 7) * 32 + (w >> 3);
  const int n0 = bucket_start(b), nsz = bucket_start(b + 1) - n0;
  const int r = lane & 31, hh = lane >> 5;
  const int blk = (lane >> 4) & 1, q4 = (lane & 15) >> 2, p4 = lane & 3;
  const __amdgpu_buffer_rsrc_t wsr = __builtin_amdgcn_make_buffer_rsrc(ws, 0, WS_END, 0x00020000);
  STAMP(0);

  auto pos_early = [&]() {
#pragma unroll
    for (int i = 0; i < 5; i++) dma16(pos, (unsigned)((8 * i + wv) * 1024 + lane * 16), lbase + L_ROWS + (8 * i + wv) * 1024);
    if (wv < 6) dma16(pos, (unsigned)((40 + wv) * 1024 + lane * 16), lbase + L_ROWS + (40 + wv) * 1024);
  };
  auto pos_late = [&]() {
    dma16(pos, (unsigned)((46 + wv) * 1024 + lane * 16), lbase + L_ROWS + (46 + wv) * 1024);
    if (wv < 4 || (wv == 4 && lane < 38)) dma16(pos, (unsigned)((54 + wv) * 1024 + lane * 16), lbase + L_ROWS + (54 + wv) * 1024);
  };

  int d[3], s[3];
  u32x2 a[3];
  unsigned tagv;
  const bool v2 = tid < CH - 1024;
  {
    const unsigned e0 = (unsigned)(w * CH + tid) * 4, e2 = v2 ? e0 + 4096 : e0;
    asm volatile("global_load_dword %0, %10, %14\n\tglobal_load_dword %1, %10, %14 offset:2048\n\tglobal_load_dword %2, %11, %14\n\t"
                 "global_load_dword %3, %10, %15\n\tglobal_load_dword %4, %10, %15 offset:2048\n\tglobal_load_dword %5, %11, %15\n\t"
                 "global_load_dwordx2 %6, %12, %16\n\tglobal_load_dwordx2 %7, %13, %16\n\tglobal_load_dwordx2 %8, %18, %16\n\t"
                 "global_load_dword %9, %19, %17 sc1"
                 : "=&v"(d[0]), "=&v"(d[1]), "=&v"(d[2]), "=&v"(s[0]), "=&v"(s[1]), "=&v"(s[2]), "=&v"(a[0]), "=&v"(a[1]), "=&v"(a[2]), "=&v"(tagv)
                 : "v"(e0), "v"(e2), "v"(2 * e0), "v"(2 * e0 + 4096), "s"(dst), "s"(src), "s"(eattr), "s"(ws), "v"(2 * e2), "v"(WS_EP)
                 : "memory");
  }
  if (tid < 256) *(u32*)(lds + L_HIST + tid * 4) = 0u;
  else {
    const int t = tid - 256;
    if (t < 32) {
      const int j = (t >> 2) - 4 * ((t >> 1) & 1) - 2 * (t & 1);
      *(u32*)(lds + L_OH + t * 4) = j == 0 ? 0x3F80u : j == 1 ? 0x3F800000u : 0u;
      if (t < 16) *(u32*)(lds + L_GC + t * 4) = 0u;
    }
  }
  barrier();
  {
    const int q = lane >> 4, c = lane & 15;
    const unsigned wlb = (unsigned)(q * 256 + 16 * (c ^ (q << 2)));
    const int row = 4 * wv + q;
#pragma unroll
    for (int i = 0; i < 4; i++) dma16(we, (wlb ^ (16 * i)) + (32 + 4 * wv + i) * 1024, lbase + L_W + (32 + 4 * wv + i) * 1024);
    dma16(h, (unsigned)(min(n0 + row, NN - 1) * 256 + 16 * (c ^ (row & 15))), lbase + L_H + wv * 1024);
    if (wv < 2) dma16(h, (unsigned)(min(n0 + 32 + row, NN - 1) * 256 + 16 * (c ^ (row & 15))), lbase + L_H + (8 + wv) * 1024);
    if (wv == 3) dma16(we, 65536 + lane * 16, lbase + L_EXT);
    if (wv == 4 && lane < 16) dma16(we, 66560 + lane * 16, lbase + L_EXT + 1024);
    if (wv == 5 && lane < 16) dma16(wx, lane * 16, lbase + L_WX);
#pragma unroll
    for (int i = 0; i < 4; i++) dma16(we, (wlb ^ (16 * i)) + (4 * wv + i) * 1024, lbase + L_W + (4 * wv + i) * 1024);
    pos_early();
  }
  const int T = wv & 3, nb = wv >> 2;
  const int loc = 5 * (4 * nb + (r >> 3)) + (r & 7);
  const bool valid = (r & 7) < 5 && loc < nsz;
  int ha = L_H + min(loc, 39) * 256 + 16 * (hh ^ (min(loc, 39) & 15));
  const unsigned trx0 = lbase + L_W + (unsigned)((8 * hh + q4) * 256), trs0 = (unsigned)((q4 << 2) | (2 * hh));
  unsigned b0 = trx0 + 32768 + 16 * ((unsigned)(8 * (T & 1) + 4 * (p4 & 1) + 2 * blk + (T >> 1)) ^ trs0) + 8 * (p4 >> 1);
  unsigned a0 = trx0 + 16 * ((unsigned)(4 * T + 2 * blk + (p4 >> 1)) ^ trs0) + 8 * (p4 & 1);
  int bo = L_BS + loc * 256 + 16 * ((4 * T + 2 * hh) ^ (loc & 15));
  unsigned gbo = WS_BT + (unsigned)((n0 + (tid >> 4)) * 256 + 16 * ((tid ^ (tid >> 4)) & 15));
  PIN(b0); PIN(a0); PIN(bo); PIN(gbo); PIN(ha);
  u32x4 I0, I1;
  {
    const unsigned one = (r & 1) ? 0x3F800000u : 0x3F80u;
#pragma unroll
    for (int jj = 0; jj < 4; jj++) {
      const bool m = ((r & 7) >> 1) == jj;
      I0[jj] = (m && (r >> 3) == hh) ? one : 0u;
      I1[jj] = (m && (r >> 3) == 2 + hh) ? one : 0u;
    }
  }
  const int rowbuf = L_ROWS + wv * ROWBUF;
  unsigned kdma = 16 * ((lane & 15) ^ (4 * (lane >> 4)));
  const int rd0 = rowbuf + 1040 * (r & 3) + 4352 * ((r >> 2) & 1) + (r >> 3) * 256;
  int rdA = rd0 + 64 * ((2 * hh) ^ (r >> 3)), rdB = rd0 + 64 * ((2 * hh + 1) ^ (r >> 3));
  int swr0 = L_S + 32 * (r >> 4) + 16 * ((r >> 2) & 1) + 8 * ((r >> 3) & 1) + 2 * (r & 3) + 160 * hh;
  int srd0 = L_S + r * 80 + 16 * hh;
  const char* btab = ws + WS_BT;
  f32x16 Y[4];
#pragma unroll
  for (int t = 0; t < 4; t++) {
    Y[t] = zero16();
    PIN(Y[t]);
  }
  PIN(I0); PIN(I1); PIN(kdma); PIN(rdA); PIN(rdB); PIN(swr0); PIN(srd0);
  asm volatile("s_waitcnt vmcnt(14)"
               : "+v"(d[0]), "+v"(d[1]), "+v"(d[2]), "+v"(s[0]), "+v"(s[1]), "+v"(s[2]), "+v"(a[0]), "+v"(a[1]), "+v"(a[2]), "+v"(tagv)::"memory");
  STAMP(1);
  int key[3];
  unsigned rank[3];
#pragma unroll
  for (int k = 0; k < 3; k++) {
    key[k] = (int)((unsigned)__umul24((unsigned)d[k], 429497u) >> 24);
    rank[k] = 0;
    if (k < 2 || v2) rank[k] = atomicAdd((unsigned*)(lds + L_HIST) + key[k], 1u);
  }
  const unsigned tag = (unsigned)__builtin_amdgcn_readfirstlane((int)tagv) + 1u;
  asm volatile("s_waitcnt vmcnt(9)" ::: "memory");
  barrier();
  STAMP(2);
  u32x4 off4, hf[8], rec[3];
  unsigned oend;
  {
    const u32x4 cnt = *(const u32x4*)(lds + L_HIST + lane * 16);
    const unsigned b1 = (b0 ^ 16) + 1024;
    u32x4 wf[8];
#pragma unroll
    for (int ks = 0; ks < 8; ks++) {
      hf[ks] = *(const u32x4*)(lds + (ha ^ (32 * ks)));
      wf[ks] = tr_read2(b0 + ks * 4096, b1 + ks * 4096);
    }
    SB();
    const unsigned tot = cnt[0] + cnt[1] + cnt[2] + cnt[3];
    oend = (unsigned)wave_incl_scan((int)tot);
    off4[0] = oend - tot;
    off4[1] = off4[0] + cnt[0];
    off4[2] = off4[1] + cnt[1];
    off4[3] = off4[2] + cnt[2];
    *(u32x4*)(lds + L_OFF + lane * 16) = off4;
#pragma unroll
    for (int k = 0; k < 3; k++) rank[k] += *(const u32*)(lds + L_OFF + key[k] * 4);
    f32x16 acc = zero16();
#pragma unroll
    for (int ks = 0; ks < 8; ks++) acc = mfma(wf[ks], hf[ks], acc);
    SB();
#pragma unroll
    for (int k = 0; k < 3; k++) {
      const int lc = d[k] - bucket_start(key[k]);
      const int g = (lc * 205) >> 10;
      rec[k][0] = a[k][0];
      rec[k][1] = (a[k][1] & 0xffffu) | (tag << 16);
      rec[k][2] = (a[k][1] >> 16) | (tag << 16);
      rec[k][3] = ((unsigned)s[k] << 8) | (unsigned)(lc + 3 * g) | ((tag >> 16) << 22);
      PIN(rec[k]);
    }
    SB();
    u32x4 bt0, bt1;
#pragma unroll
    for (int i = 0; i < 4; i++) {
      bt0[i] = pk2(acc[2 * i] * NLOG2E, acc[2 * i + 1] * NLOG2E);
      bt1[i] = pk2(acc[8 + 2 * i] * NLOG2E, acc[9 + 2 * i] * NLOG2E);
    }
    if ((r & 7) < 5) {
      *(u32x4*)(lds + bo) = bt0;
      *(u32x4*)(lds + (bo ^ 16)) = bt1;
    }
#pragma unroll
    for (int k = 0; k < 3; k++)
      if (k < 2 || v2) *(u32x4*)(lds + L_ST + rank[k] * 16) = rec[k];
  }
  asm volatile("s_waitcnt vmcnt(5)" ::: "memory");
  barrier();
  STAMP(4);
  u32x2 at[4];
  {
    u32x4 vb0 = *(const u32x4*)(lds + L_BS + tid * 16);
    u32x4 vb1 = *(const u32x4*)(lds + L_BS + 8192 + (tid & 127) * 16);
    u32x4 v0 = *(const u32x4*)(lds + L_ST + tid * 16);
    u32x4 v1 = *(const u32x4*)(lds + L_ST + 8192 + tid * 16);
    u32x4 v2_ = *(const u32x4*)(lds + L_ST + 16384 + (tid & 255) * 16);
    const unsigned a1 = (a0 ^ 16) + 1024;
    u32x4 wf[8];
#pragma unroll
    for (int ks = 0; ks < 8; ks++) wf[ks] = tr_read2(a0 + ks * 4096, a1 + ks * 4096);
    SB();
    __builtin_amdgcn_raw_buffer_store_b128(vb0, wsr, gbo, 0, 16);
    if (tid < 16 * (nsz - 32)) __builtin_amdgcn_raw_buffer_store_b128(vb1, wsr, gbo + 8192, 0, 16);
    asm volatile("" ::: "memory");
    if (tid < 256) __builtin_amdgcn_raw_buffer_store_b128(v2_, wsr, WS_R + (unsigned)(w * CHS + 1024 + tid) * 16, 0, 16);
    __builtin_amdgcn_raw_buffer_store_b128(v0, wsr, WS_R + (unsigned)(w * CHS + tid) * 16, 0, 16);
    __builtin_amdgcn_raw_buffer_store_b128(v1, wsr, WS_R + (unsigned)(w * CHS + 512 + tid) * 16, 0, 16);
    asm volatile("" ::: "memory");
    f32x16 acc = zero16();
#pragma unroll
    for (int ks = 0; ks < 8; ks++) acc = mfma(hf[ks], wf[ks], acc);
#pragma unroll
    for (int i = 0; i < 4; i++) {
      at[i][0] = pk2(acc[4 * i] * NLOG2E, acc[4 * i + 1] * NLOG2E);
      at[i][1] = pk2(acc[4 * i + 2] * NLOG2E, acc[4 * i + 3] * NLOG2E);
      PIN(at[i]);
    }
  }
  asm volatile("s_waitcnt vmcnt(2)" ::: "memory");
  barrier();
  STAMP(5);
  if (wv == 0) {
    const u32x4 g0 = {tag, off4[0] | (off4[1] << 16), tag, off4[1] | (off4[2] << 16)};
    const u32x4 g1 = {tag, off4[2] | (off4[3] << 16), tag, off4[3] | (oend << 16)};
    const unsigned off = WS_TAB + (unsigned)(w * NB + 4 * lane) * 8;
    __builtin_amdgcn_raw_buffer_store_b128(g0, wsr, off, 0, 16);
    __builtin_amdgcn_raw_buffer_store_b128(g1, wsr, off + 16, 0, 16);
  }

  asm volatile("" ::: "memory");
  pos_late();
#pragma unroll
  for (int i = 0; i < 4; i++) *(u32x2*)(lds + L_A + (4 * nb + i) * 2048 + T * 512 + lane * 8) = at[i];
  {
    const u32x4 z = {0u, 0u, 0u, 0u};
    *(u32x4*)(lds + L_S + tid * 16) = z;
    *(u32x4*)(lds + L_S + 8192 + tid * 16) = z;
    if (tid < 256) {
      *(u32x4*)(lds + L_S + 16384 + tid * 16) = z;
    } else {
      const int t = tid - 256, v = t >> 5, q = (t >> 3) & 3, i = t & 7;
      *(u32*)(lds + L_FK + t * 4) = (unsigned)(i < 4 ? 128 + 32 * q + 4 * v + i : 16 * v + 4 * (i - 4) + q) << 8;
    }
  }
  u32x4 extB[4];
#pragma unroll
  for (int t = 0; t < 4; t++) {
    const u16* wp = (const u16*)(lds + L_EXT) + 32 * t + r;
    const float w0 = bf2f(wp[(1 + 2 * hh) * 128]) * NLOG2E, w1 = bf2f(wp[(2 + 2 * hh) * 128]) * NLOG2E, w2 = bf2f(wp[0]) * NLOG2E;
    extB[t][0] = 0u;
    extB[t][1] = 0u;
    extB[t][2] = pk2(w0, w1);
    extB[t][3] = hh ? 0u : pk2(w2, 0.f);
    PIN(extB[t]);
  }
  wait_vm0();
  barrier();
  STAMP(6);

  bool wh_loaded = false, slow = false;
  int lo = 0, quota = MAPW, gq = wv;
  unsigned pin = 0;
  while (true) {
    wh_loaded = false;
    int tq = tid;
    PIN(tq);
    const int lq = tq & 63;
    int kc, kfirst, kpre, wtot;
    {
      const unsigned tabq = WS_TAB + (unsigned)((tq >> 1) * NB + b) * 8;
      u32x2 gq2 = {tag + 1u, 0u};
#pragma clang loop unroll(disable)
      for (int spin = 0; spin < (1 << 22); spin++) {
        if (gq2[0] != tag) asm volatile("global_load_dwordx2 %0, %1, %2 sc1\n\ts_waitcnt vmcnt(0)" : "=&v"(gq2) : "v"(tabq), "s"(ws) : "memory");
        if (__all(gq2[0] == tag)) break;
        __builtin_amdgcn_s_sleep(1);
      }
      const int o0 = min((int)(gq2[1] & 0xffffu), CH), o1 = min((int)(gq2[1] >> 16), CH);
      kc = max(o1 - o0, 0);
      kfirst = (tq >> 1) * CHS + o0;
      const int inc = wave_incl_scan((tq & 1) ? 0 : kc);
      kpre = inc - kc;
      wtot = __builtin_amdgcn_readlane(inc, 63);
      if (!slow && lq == 0) atomicMax((unsigned*)(lds + L_GC + 32), (unsigned)wtot);
    }
    STAMP(7);
    u32x3 q[4];
    u32x2 pd[4], ps[4];
    int ra1[4], par[4];
    bool four;
    {
      const int map = L_MAP + wv * MAPW * 4;
      const int nw = min(wtot - lo, quota);
      const int jhi = min(kc, lo + quota - kpre);
#pragma clang loop vectorize(disable) unroll(disable)
      for (int j = max(0, lo - kpre) + ((tq ^ max(0, lo - kpre)) & 1); j < jhi; j += 2) *(u32*)(lds + map + (kpre + j - lo) * 4) = (unsigned)(kfirst + j) * 16u;
      unsigned ix[4];
#pragma unroll
      for (int k = 0; k < 4; k++) {
        const unsigned mi = *(const u32*)(lds + map + (64 * k + lq) * 4);
        ix[k] = 64 * k + lq < nw ? mi : 0u;
      }
      STAMP(23);
      u32x4 q4_[4];
#pragma clang loop unroll(disable)
      for (int spin = 0; spin < (1 << 22); spin++) {
        asm volatile("global_load_dwordx4 %0, %4, %8 sc1\n\tglobal_load_dwordx4 %1, %5, %8 sc1\n\tglobal_load_dwordx4 %2, %6, %8 sc1\n\t"
                     "global_load_dwordx4 %3, %7, %8 sc1\n\ts_waitcnt vmcnt(0)"
                     : "=&v"(q4_[0]), "=&v"(q4_[1]), "=&v"(q4_[2]), "=&v"(q4_[3]) : "v"(ix[0]), "v"(ix[1]), "v"(ix[2]), "v"(ix[3]), "s"(ws + WS_R) : "memory");
        bool ok = true;
#pragma unroll
        for (int k = 0; k < 4; k++) {
          const unsigned bad = (((q4_[k][1] ^ (tag << 16)) | (q4_[k][2] ^ (tag << 16))) >> 16) | ((q4_[k][3] ^ ((tag >> 16) << 22)) >> 22);
          ok = ok && (bad == 0 || 64 * k + lq >= nw);
        }
        if (__all(ok)) break;
      }
#pragma unroll
      for (int k = 0; k < 4; k++) {
        q[k][0] = q4_[k][0];
        q[k][1] = (q4_[k][1] & 0xffffu) | (q4_[k][2] << 16);
        q[k][2] = q4_[k][3] & 0x3fffffu;
      }
      STAMP(24);
      unsigned rk[4];
#pragma unroll
      for (int k = 0; k < 4; k++) {
        rk[k] = CAPW;
        if (64 * k + lq < nw) rk[k] = atomicAdd((unsigned*)(lds + L_GC) + ((q[k][2] >> 3) & 7u), 1u);
        const unsigned g = (q[k][2] >> 3) & 7u;
        const int dn = n0 + (int)(q[k][2] & 63u) - 3 * (int)g, sn = min((int)(q[k][2] >> 8), NN - 1);
        const int od = L_ROWS + ((6 * dn) & ~3), os = L_ROWS + ((6 * sn) & ~3);
        pd[k][0] = *(const u32*)(lds + od); pd[k][1] = *(const u32*)(lds + od + 4);
        ps[k][0] = *(const u32*)(lds + os); ps[k][1] = *(const u32*)(lds + os + 4);
        par[k] = (dn & 1) * 16;
      }
#pragma unroll
      for (int k = 0; k < 4; k++) {
        const bool ok = rk[k] < CAPW;
        const int e = (int)(((q[k][2] >> 3) & 7u) * CAPW + rk[k]);
        *(u32*)(lds + (ok ? L_R2 + e * 4 : L_DUM + 96)) = q[k][2];
        ra1[k] = ok ? L_R1 + e * 16 : L_DUM + 96;
      }
      four = nw > 192;
    }
    barrier();
    STAMP(8);
    const int cl = *(const u32*)(lds + L_GC + (lane & 7) * 4);
    const int maxtot = __builtin_amdgcn_readfirstlane(*(const u32*)(lds + L_GC + 32));
    if (!slow) {
      const int kk = (cl << 3) | (lane & 7);
      int rk = 0;
      rk += __builtin_amdgcn_update_dpp(0, kk, 0x121, 0xf, 0xf, false) > kk;
      rk += __builtin_amdgcn_update_dpp(0, kk, 0x122, 0xf, 0xf, false) > kk;
      rk += __builtin_amdgcn_update_dpp(0, kk, 0x123, 0xf, 0xf, false) > kk;
      rk += __builtin_amdgcn_update_dpp(0, kk, 0x124, 0xf, 0xf, false) > kk;
      rk += __builtin_amdgcn_update_dpp(0, kk, 0x125, 0xf, 0xf, false) > kk;
      rk += __builtin_amdgcn_update_dpp(0, kk, 0x126, 0xf, 0xf, false) > kk;
      rk += __builtin_amdgcn_update_dpp(0, kk, 0x127, 0xf, 0xf, false) > kk;
      const int cmax = __any(cl > CAPW) ? CAPW + 1 : 0;
      if (maxtot > MAPW || cmax > CAPW) {
        slow = true;
        quota = CAPW / 8;
        barrier();
        if (tid < 8) *(u32*)(lds + L_GC + tid * 4) = 0u;
        barrier();
        continue;
      }
      gq = __builtin_ctzll(__ballot(rk == (wv < 4 ? wv : 11 - wv)));
    }
    const int n = __builtin_amdgcn_readlane(cl, gq);
    const int swr = swr0 + gq * 2560, srd = srd0 + gq * 2560;
    const int l1 = L_R1 + gq * CAPW * 16, l2 = L_R2 + gq * CAPW * 4;
    const int nt = (n + 31) >> 5;
    u32x4 rows[8], extA, S0, S1, ra, rb, m1p = {0u, 0u, 0u, 0u};
    u32x2 r1;
    unsigned r2;
    int sp;
    f32x16 XA, XB;
    auto list_read = [&](int tl) {
      const int list = tl < nt ? l2 + tl * 128 : L_FK + wv * 128;
      ra = *(const u32x4*)(lds + list + (lane >> 4) * 32);
      rb = *(const u32x4*)(lds + list + (lane >> 4) * 32 + 16);
    };
    auto dma_piece = [&](int tl, int i) {
      const unsigned v = i < 4 ? ra[i & 3] : rb[i & 3];
      const unsigned long long bp = (unsigned long long)(tl < nt ? btab : (const char*)wh);
      const u32x4 srd = {(unsigned)bp, (unsigned)(bp >> 32) & 0xffffu, tl < nt ? (unsigned)(NN * 256) : 65536u, 0x00020000u};
      bdma16_dev(srd, (v & 0xffffff00u) | kdma, lbase + rowbuf + piece_off(i));
    };
    auto meta_read = [&](int tl) {
      r1 = *(const u32x2*)(lds + l1 + (tl * 32 + r) * 16 + hh * 8);
      r2 = *(const u32*)(lds + l2 + (tl * 32 + r) * 4);
    };
    auto meta_use = [&](int tl) {
      const unsigned dl = r2 & 7u;
      const u32x2 oh = *(const u32x2*)(lds + L_OH + dl * 16 + hh * 8);
      extA[0] = oh[0]; extA[1] = oh[1]; extA[2] = r1[0]; extA[3] = r1[1];
      sp = (tl * 32 + r < n) ? swr + (int)dl * 320 : L_DUM;
    };
    auto s_write = [&]() {
      *(u16*)(lds + sp) = (u16)(hh ? extA[3] : 0x3F80u);
      *(u16*)(lds + sp + 80) = (u16)(extA[3] >> 16);
    };
    auto rows_read = [&]() {
#pragma unroll
      for (int j = 0; j < 8; j++) rows[j] = *(const u32x4*)(lds + (j < 4 ? rdA : rdB) + 16 * (j & 3));
    };
    auto rows_pin = [&]() {
      asm volatile("" : "+v"(rows[0]), "+v"(rows[1]), "+v"(rows[2]), "+v"(rows[3]), "+v"(rows[4]), "+v"(rows[5]), "+v"(rows[6]), "+v"(rows[7]));
    };

    list_read(0);
    if (n > 0 && (!O_STAG || wv < 4)) {
#pragma unroll
      for (int i = 0; i < 8; i++) dma_piece(0, i);
    }
    STAMP(18);
    auto finish = [&](int k) {
      const unsigned long long ud = (((unsigned long long)pd[k][1] << 32) | pd[k][0]) >> par[k];
      const unsigned long long us = (((unsigned long long)ps[k][1] << 32) | ps[k][0]) >> ((q[k][2] >> 4) & 16u);
      const unsigned dlo = (unsigned)ud, dhi = (unsigned)(ud >> 32), slo = (unsigned)us, shi = (unsigned)(us >> 32);
      const unsigned dxy = pk2(bf2f(dlo) - bf2f(slo), bfhi(dlo) - bfhi(slo));
      const unsigned dz_ = pk2(bf2f(dhi) - bf2f(shi), 0.f);
      const float dx = bf2f(dxy), dy = bfhi(dxy), dz = bf2f(dz_);
      const unsigned sxy = pk2(dx * dx, dy * dy), sz = pk2(dz * dz, 0.f);
      const unsigned d2 = pk2((bf2f(sxy) + bfhi(sxy)) + bf2f(sz), 0.f) & 0xffffu;
      const u32x4 rec = {q[k][0], d2 | (dxy << 16), q[k][1], (dxy >> 16) | (dz_ << 16)};
      *(u32x4*)(lds + ra1[k]) = rec;
    };
    finish(0);
    finish(1);
    finish(2);
    if (four) finish(3);
    if (lane < 32 * nt - n) {
      const u32x4 z = {0u, 0u, 0u, 0u};
      *(u32x4*)(lds + l1 + (n + lane) * 16) = z;
    }
    if (w == 0 && tid == 0) __builtin_amdgcn_raw_buffer_store_b32(tag, wsr, WS_EP, 0, 16);
    pin = pos[min(3 * n0 + 15 * gq + (lq & 15), 3 * NN - 1)];
#pragma unroll
    for (int t = 0; t < 4; t++) {
      const u32x2 v = *(const u32x2*)(lds + L_A + gq * 2048 + t * 512 + lq * 8);
      extB[t][0] = v[0];
      extB[t][1] = v[1];
    }
    STAMP(19);
    barrier();
    STAMP(20);
    if (O_PRIO == 1 && wv >= 4) __builtin_amdgcn_s_setprio(1);
    if (O_PRIO == 2 && wv < 4) __builtin_amdgcn_s_setprio(1);
    if (n > 0) {
    if (O_STAG && wv >= 4) {
#pragma unroll
      for (int i = 0; i < 8; i++) dma_piece(0, i);
    }
    meta_read(0);
    list_read(1);
    meta_use(0);
    s_write();
    S1 = m1p;
    STAMP(21);
    wait_vm0();
    STAMP(22);
    PIN(pin);
    rows_read();
    rows_pin();
    XA = mfma(rows[0], I0, zero16());
    XA = mfma(rows[1], I1, XA);
    XA = mfma(extA, extB[0], XA);
    STAMP(9);

    const int nfull = O_HALF ? (n + 15) >> 5 : nt;
    for (int tl = 0; tl < nfull; tl++) {
#pragma unroll
      for (int t = 0; t < 4; t++) {
        f32x16& Xc = (t & 1) ? XB : XA;
        f32x16& Xn = (t & 1) ? XA : XB;
        const int tn = (t + 1) & 3;
        auto gap = [&](int k) {
          SB();
          if (t == 0 && k < 8 && tl == 0) dma_piece(1, k);
          if (t == 0 && k == 0) S0 = *(const u32x4*)(lds + srd);
          if (t == 0 && k == 3) S1 = *(const u32x4*)(lds + srd + 32);
          if (t == 0 && k == 4) {
            *(u16*)(lds + sp) = 0;
            *(u16*)(lds + sp + 80) = 0;
          }
          if (t == 2 && k == 0) meta_read(tl + 1);
          if (t == 2 && k == 2) meta_use(tl + 1);
          if (t == 2 && k == 3) wait_vm0();
          if (t == 2 && k >= 3 && k < 7) {
            rows[2 * k - 6] = *(const u32x4*)(lds + (k < 5 ? rdA : rdB) + 16 * ((2 * k - 6) & 3));
            rows[2 * k - 5] = *(const u32x4*)(lds + (k < 5 ? rdA : rdB) + 16 * ((2 * k - 5) & 3));
          }
          if (t == 2 && k == 7) list_read(tl + 2);
          if (t == 3 && k < 8) dma_piece(tl + 2, k);
          if (t == 3 && k == 8) s_write();
          SB();
        };
        f32x2 dd[8];
        if (O_PRIO == 4 && t == 0) __builtin_amdgcn_s_setprio(1);
        if (O_PRIO == 4 && t == 2) __builtin_amdgcn_s_setprio(0);
        if (O_PRIO == 7 && t == 3 && wv >= 4) __builtin_amdgcn_s_setprio(1);
        if (O_PRIO == 7 && t == 2 && wv >= 4) __builtin_amdgcn_s_setprio(0);
        SB();
        Xn = mfma(rows[2 * tn], I0, zero16());
        PIN(Xn);
        SB();
#pragma unroll
        for (int g = 0; g < 4; g++) {
#pragma unroll
          for (int i = 2 * g; i < 2 * g + 2; i++) {
            dd[i][0] = __builtin_amdgcn_exp2f(Xc[2 * i]);
            dd[i][1] = __builtin_amdgcn_exp2f(Xc[2 * i + 1]);
            PIN(dd[i]);
          }
          SB();
          if (g == 0) { Xn = mfma(rows[2 * tn + 1], I1, Xn); PIN(Xn); }
          if (g == 1) { Xn = mfma(extA, extB[tn], Xn); PIN(Xn); }
          if (g == 2) { Y[(t + 3) & 3] = mfma(S1, m1p, Y[(t + 3) & 3]); PIN(Y[(t + 3) & 3]); }
          gap(g);
        }
#pragma unroll
        for (int i = 0; i < 8; i++) { dd[i] += 1.0f; PIN(dd[i]); }
        gap(4);
#pragma unroll
        for (int g = 0; g < 4; g++) {
#pragma unroll
          for (int i = 2 * g; i < 2 * g + 2; i++) {
            dd[i][0] = __builtin_amdgcn_rcpf(dd[i][0]);
            dd[i][1] = __builtin_amdgcn_rcpf(dd[i][1]);
            PIN(dd[i]);
          }
          gap(5 + g);
        }
        u32x4 m0;
#pragma unroll
        for (int i = 0; i < 4; i++) { f32x2 x = {Xc[2 * i], Xc[2 * i + 1]}; dd[i] *= x; }
#pragma unroll
        for (int i = 0; i < 4; i++) m0[i] = __builtin_bit_cast(unsigned, __builtin_convertvector(dd[i], bf16x2));
        SB();
#pragma unroll
        for (int i = 4; i < 8; i++) { f32x2 x = {Xc[2 * i], Xc[2 * i + 1]}; dd[i] *= x; PIN(dd[i]); }
        SB();
        Y[t] = mfma(S0, m0, Y[t]);
        PIN(Y[t]);
        SB();
#pragma unroll
        for (int i = 0; i < 4; i++) m1p[i] = __builtin_bit_cast(unsigned, __builtin_convertvector(dd[4 + i], bf16x2));
        PIN(m1p);
        if (t == 2) rows_pin();
      }
    }
    SB();
    Y[3] = mfma(S1, m1p, Y[3]);
    if (nfull < nt) {
      S0 = *(const u32x4*)(lds + srd);
      *(u16*)(lds + sp) = 0;
      *(u16*)(lds + sp + 80) = 0;
#pragma unroll
      for (int t = 0; t < 4; t++) {
        f32x16& Xc = (t & 1) ? XB : XA;
        f32x16& Xn = (t & 1) ? XA : XB;
        f32x2 dd[4];
        if (t < 2 && nfull == 0) {
#pragma unroll
          for (int i = 0; i < 4; i++) dma_piece(nt, 4 * t + i);
        }
        SB();
        if (t < 3) { Xn = mfma(rows[2 * t + 2], I0, zero16()); PIN(Xn); }
        SB();
#pragma unroll
        for (int i = 0; i < 2; i++) { dd[i][0] = __builtin_amdgcn_exp2f(Xc[2 * i]); dd[i][1] = __builtin_amdgcn_exp2f(Xc[2 * i + 1]); }
        SB();
        if (t < 3) { Xn = mfma(rows[2 * t + 3], I1, Xn); PIN(Xn); }
        SB();
#pragma unroll
        for (int i = 2; i < 4; i++) { dd[i][0] = __builtin_amdgcn_exp2f(Xc[2 * i]); dd[i][1] = __builtin_amdgcn_exp2f(Xc[2 * i + 1]); }
        SB();
        if (t < 3) { Xn = mfma(extA, extB[t + 1], Xn); PIN(Xn); }
        SB();
#pragma unroll
        for (int i = 0; i < 4; i++) dd[i] += 1.0f;
        SB();
#pragma unroll
        for (int i = 0; i < 4; i++) { dd[i][0] = __builtin_amdgcn_rcpf(dd[i][0]); dd[i][1] = __builtin_amdgcn_rcpf(dd[i][1]); }
        SB();
        u32x4 m0;
#pragma unroll
        for (int i = 0; i < 4; i++) { f32x2 x = {Xc[2 * i], Xc[2 * i + 1]}; dd[i] *= x; }
#pragma unroll
        for (int i = 0; i < 4; i++) m0[i] = __builtin_bit_cast(unsigned, __builtin_convertvector(dd[i], bf16x2));
        SB();
        Y[t] = mfma(S0, m0, Y[t]);
      }
    }
    wh_loaded = true;
    wait_vm0();
    }
    if (!slow) break;
    lo += quota;
    if (lo >= maxtot) break;
    barrier();
    if (tid < 8) *(u32*)(lds + L_GC + tid * 4) = 0u;
    pos_early();
    pos_late();
    wait_vm0();
    barrier();
  }
  STAMP(10);
  if (!wh_loaded) {
    wait_vm0();
    PIN(pin);
    const u32x4 fa = *(const u32x4*)(lds + L_FK + wv * 128 + (lane >> 4) * 32), fb = *(const u32x4*)(lds + L_FK + wv * 128 + (lane >> 4) * 32 + 16);
#pragma unroll
    for (int i = 0; i < 8; i++) dma16(wh, (i < 4 ? fa[i & 3] : fb[i & 3]) | kdma, lbase + rowbuf + piece_off(i));
    wait_vm0();
  }

#pragma unroll
  for (int i = 0; i < 3; i++) {
    if (i < 2 || hh == 0) {
      const u32x2 mv = {pk2(Y[0][4 * i] * NLN2, Y[1][4 * i] * NLN2), pk2(Y[2][4 * i] * NLN2, Y[3][4 * i] * NLN2)};
      *(u32x2*)(lds + L_M + (5 * gq + 2 * i + hh) * MSTR + 8 * r) = mv;
    }
  }
  u32x4 hq[8], res[2];
  unsigned wxr[4];
#pragma unroll
  for (int ks = 0; ks < 8; ks++) hq[ks] = *(const u32x4*)(lds + (ha ^ (32 * ks)));
  {
    const int ra_ = (ha ^ (16 * hh)) ^ (64 * T + 32 * hh);
    res[0] = *(const u32x4*)(lds + ra_);
    res[1] = *(const u32x4*)(lds + (ra_ ^ 16));
  }
#pragma unroll
  for (int t = 0; t < 4; t++) wxr[t] = ((const u16*)(lds + L_WX))[32 * t + r];
  unsigned wa = lbase + L_ROWS + 2080 * hh + 256 * q4 + 64 * (T ^ q4) + 32 * (p4 & 1) + 16 * blk + 8 * (p4 >> 1);
  int ma = L_M + min(loc, 39) * MSTR + 16 * hh;
  PIN(wa);
  PIN(ma);
  STAMP(13);
  barrier();
  STAMP(11);
  {
    u32x4 w1[8], w2[8], mq[8];
#pragma unroll
    for (int ks = 0; ks < 8; ks++) w1[ks] = tr_read2(wa + ks * ROWBUF + 4352, wa + ks * ROWBUF + 4352 + 1040);
#pragma unroll
    for (int ks = 0; ks < 8; ks++) {
      w2[ks] = tr_read2(wa + ks * ROWBUF, wa + ks * ROWBUF + 1040);
      mq[ks] = *(const u32x4*)(lds + ma + 32 * ks);
    }
    SB();
    f32x16 P = zero16();
#pragma unroll
    for (int ks = 0; ks < 8; ks++) P = mfma(w1[ks], hq[ks], P);
#pragma unroll
    for (int ks = 0; ks < 8; ks++) P = mfma(w2[ks], mq[ks], P);
    PIN(P);
    SB();
    float zz[9];
    {
      float wxv[4];
#pragma unroll
      for (int t = 0; t < 4; t++) wxv[t] = bf2f(wxr[t]) * NLN2;
#pragma unroll
      for (int i = 0; i < 9; i++) {
        const int j = 4 * (i / 3) + 1 + i % 3;
        zz[i] = Y[0][j] * wxv[0];
        PIN(zz[i]);
        zz[i] = __builtin_fmaf(Y[1][j], wxv[1], zz[i]);
        PIN(zz[i]);
        zz[i] = __builtin_fmaf(Y[2][j], wxv[2], zz[i]);
        PIN(zz[i]);
        zz[i] = __builtin_fmaf(Y[3][j], wxv[3], zz[i]);
        PIN(zz[i]);
      }
    }
    SB();
    f32x2 x[8], e[8];
#pragma unroll
    for (int i = 0; i < 8; i++) {
      x[i][0] = P[2 * i];
      x[i][1] = P[2 * i + 1];
      e[i] = x[i] * NLOG2E;
    }
    SB();
#pragma unroll
    for (int i = 0; i < 8; i++) { e[i][0] = __builtin_amdgcn_exp2f(e[i][0]); e[i][1] = __builtin_amdgcn_exp2f(e[i][1]); }
    SB();
#pragma unroll
    for (int i = 0; i < 8; i++) e[i] += 1.0f;
    SB();
#pragma unroll
    for (int i = 0; i < 8; i++) { e[i][0] = __builtin_amdgcn_rcpf(e[i][0]); e[i][1] = __builtin_amdgcn_rcpf(e[i][1]); }
    SB();
    u32x4 ov[2];
#pragma unroll
    for (int i = 0; i < 8; i++) {
      const unsigned rs = res[i >> 2][i & 3];
      const f32x2 rv = {bf2f(rs), bfhi(rs)};
      const f32x2 o = __builtin_elementwise_fma(x[i], e[i], rv);
      ov[i >> 2][i & 3] = pk2(o[0], o[1]);
    }
    if (valid) {
      u32x4* op = (u32x4*)((char*)out + (unsigned)(n0 + loc) * 256 + 64 * T + 32 * hh);
      op[0] = ov[0];
      op[1] = ov[1];
    }
    STAMP(17);
#define DPP9(ctl)                                                                                                         \
  "v_add_f32_dpp %0, %0, %0 " ctl "\n\tv_add_f32_dpp %1, %1, %1 " ctl "\n\tv_add_f32_dpp %2, %2, %2 " ctl "\n\t"          \
  "v_add_f32_dpp %3, %3, %3 " ctl "\n\tv_add_f32_dpp %4, %4, %4 " ctl "\n\tv_add_f32_dpp %5, %5, %5 " ctl "\n\t"          \
  "v_add_f32_dpp %6, %6, %6 " ctl "\n\tv_add_f32_dpp %7, %7, %7 " ctl "\n\tv_add_f32_dpp %8, %8, %8 " ctl "\n\t"
    asm volatile("s_nop 1\n\t" DPP9("row_shr:1 row_mask:0xf bank_mask:0xf bound_ctrl:1") DPP9("row_shr:2 row_mask:0xf bank_mask:0xf bound_ctrl:1")
                 DPP9("row_shr:4 row_mask:0xf bank_mask:0xf bound_ctrl:1") DPP9("row_shr:8 row_mask:0xf bank_mask:0xf bound_ctrl:1")
                 DPP9("row_bcast:15 row_mask:0xa bank_mask:0xf") "s_nop 0"
                 : "+v"(zz[0]), "+v"(zz[1]), "+v"(zz[2]), "+v"(zz[3]), "+v"(zz[4]), "+v"(zz[5]), "+v"(zz[6]), "+v"(zz[7]), "+v"(zz[8]));
    const int zp = L_S + gq * 2560;
    if (r == 31) {
#pragma unroll
      for (int i = 0; i < 9; i++) *(u32*)(lds + zp + 12 * hh + 24 * (i / 3) + 4 * (i % 3)) = __float_as_uint(zz[i]);
    }
    asm volatile("" ::: "memory");
    const float z = __uint_as_float(*(const u32*)(lds + zp + 4 * (lane & 15)));
    if (lane < 15 && 15 * gq + lane < 3 * nsz) out[(size_t)NN * 128 + (unsigned)(3 * n0 + 15 * gq + lane)] = (u16)pk2(bf2f(pin) + z, 0.f);
  }
  STAMP(12);
  STAMP_OUT();
}

extern "C" void kernel_launch(void* const* d_in, const int* in_sizes, int n_in, void* d_out, int out_size, void* d_ws,
                              size_t ws_size, hipStream_t stream) {
  egnn_fused<<<NB, 512, 0, stream>>>((const u16*)d_in[0], (const u16*)d_in[1], (const u32x2*)d_in[2], (const int*)d_in[3],
                                     (const int*)d_in[4], (const u16*)d_in[5], (const u16*)d_in[6], (const u16*)d_in[7],
                                     (char*)d_ws, (u16*)d_out);
}
```

```cpp
#include <hip/hip_runtime.h>
#include <hip/hip_bf16.h>
#include <stdint.h>

typedef __bf16 bf16x8 __attribute__((ext_vector_type(8)));
typedef __bf16 bf16x2 __attribute__((ext_vector_type(2)));
typedef short s16x4 __attribute__((ext_vector_type(4)));
typedef float f32x2 __attribute__((ext_vector_type(2)));
typedef float f32x4 __attribute__((ext_vector_type(4), may_alias));
typedef float f32x16 __attribute__((ext_vector_type(16)));
typedef unsigned u32x2 __attribute__((ext_vector_type(2), may_alias));
typedef unsigned u32x3 __attribute__((ext_vector_type(3)));
typedef unsigned u32x4 __attribute__((ext_vector_type(4), may_alias));
typedef unsigned u32x2a4 __attribute__((ext_vector_type(2), aligned(4), may_alias));
typedef unsigned short u16 __attribute__((may_alias));
typedef unsigned u32 __attribute__((may_alias));
#define LDSP(T) __attribute__((address_space(3))) T*
#define DI __device__ __forceinline__
#define SB() __builtin_amdgcn_sched_barrier(0)
#define PIN(x) asm volatile("" : "+v"(x))

#define O_PRE 0
#define O_STAG 1
#define O_HALF 1
#define O_TOUCH 0
#define O_PRIO 7
constexpr int NN = 10000;
constexpr int CH = 1250, CHS = 1280;
constexpr int NB = 256;
constexpr float NLOG2E = -1.4426950408889634f;
constexpr float NLN2 = -0.6931471805599453f;

constexpr unsigned WS_BT = 4096;
constexpr unsigned WS_R = WS_BT + (NN + 256) * 256;
constexpr unsigned WS_TAB = WS_R + NB * CHS * 16;
constexpr unsigned WS_EP = WS_TAB + NB * NB * 8;
constexpr unsigned WS_END = WS_EP + 256;

constexpr int CAPW = 224;
constexpr int ROWBUF = 8496;
constexpr int L_ROWS = 0;
constexpr int L_R1 = 8 * ROWBUF;
constexpr int L_R2 = L_R1 + 8 * CAPW * 16;
constexpr int L_S = L_R2 + 8 * CAPW * 4;
constexpr int L_M = L_S + 8 * 2560;
constexpr int MSTR = 272;
constexpr int L_H = 135168;
constexpr int L_A = L_H + 10240;
constexpr int L_HIST = L_A + 8 * 2048;
constexpr int L_OH = L_HIST + 1024;
constexpr int L_GC = L_OH + 128;
constexpr int L_DUM = L_GC + 64;
constexpr int L_WX = L_DUM + 128;
constexpr int L_END = L_WX + 256;
constexpr int L_W = L_R1;
constexpr int L_EXT = L_W + 65536;
constexpr int L_ST = 47104;
constexpr int L_BS = L_A;
constexpr int L_OFF = L_A + 10496;
constexpr int MAPW = 256;
constexpr int L_MAP = L_M;
constexpr int L_FK = L_HIST;
static_assert(L_ST + 20480 <= L_R1 && L_BS % 256 == 0 && L_W % 32 == 0 && L_W + 65536 <= L_EXT && L_EXT + 1280 <= L_H && L_H % 256 == 0 && L_M + 40 * MSTR <= L_H && L_END <= 163840, "lds map");

DI constexpr int piece_off(int i) { return 1040 * (i & 3) + 4352 * (i >> 2); }
DI float bf2f(unsigned u) { return __uint_as_float(u << 16); }
DI float bfhi(unsigned u) { return __uint_as_float(u & 0xffff0000u); }
DI unsigned pk2(float a, float b) {
  f32x2 v = {a, b};
  return __builtin_bit_cast(unsigned, __builtin_convertvector(v, bf16x2));
}
DI f32x16 mfma(u32x4 a, u32x4 b, f32x16 c) {
  return __builtin_amdgcn_mfma_f32_32x32x16_bf16(__builtin_bit_cast(bf16x8, a), __builtin_bit_cast(bf16x8, b), c, 0, 0, 0);
}
DI f32x16 zero16() {
  f32x16 z;
#pragma unroll
  for (int i = 0; i < 16; i++) z[i] = 0.f;
  return z;
}
DI u32x4 tr_read2(unsigned a0, unsigned a1) {
  const u32x2 lo = __builtin_bit_cast(u32x2, __builtin_amdgcn_ds_read_tr16_b64_v4i16((LDSP(s16x4))(size_t)a0));
  const u32x2 hi = __builtin_bit_cast(u32x2, __builtin_amdgcn_ds_read_tr16_b64_v4i16((LDSP(s16x4))(size_t)a1));
  const u32x4 v = {lo[0], lo[1], hi[0], hi[1]};
  return v;
}
DI int bucket_start(int b) { return (b * 625 + 15) >> 4; }
DI int wave_incl_scan(int x) {
  x += __builtin_amdgcn_update_dpp(0, x, 0x111, 0xf, 0xf, false);
  x += __builtin_amdgcn_update_dpp(0, x, 0x112, 0xf, 0xf, false);
  x += __builtin_amdgcn_update_dpp(0, x, 0x114, 0xf, 0xf, false);
  x += __builtin_amdgcn_update_dpp(0, x, 0x118, 0xf, 0xf, false);
  x += __builtin_amdgcn_update_dpp(0, x, 0x142, 0xa, 0xf, false);
  x += __builtin_amdgcn_update_dpp(0, x, 0x143, 0xc, 0xf, false);
  return x;
}
DI void dma16(const void* sbase, unsigned voff, unsigned lds_base) {
  asm volatile("s_mov_b32 m0, %2\n\ts_nop 0\n\tglobal_load_lds_dwordx4 %0, %1" ::"v"(voff), "s"(sbase), "s"(lds_base) : "memory");
}
DI void dma16_dev(const void* sbase, unsigned voff, unsigned lds_base) {
  asm volatile("s_mov_b32 m0, %2\n\ts_nop 0\n\tglobal_load_lds_dwordx4 %0, %1 sc1" ::"v"(voff), "s"(sbase), "s"(lds_base) : "memory");
}
template <int I>
DI void row_dma(u32x4 srd, unsigned key, unsigned lanec, unsigned lds_base, unsigned& t) {
  if (I & 3)
    asm volatile("v_and_or_b32 %0, %1, %2, %3\n\tbuffer_load_dwordx4 %0, %4, %5 offen offset:%6 sc1 lds"
                 : "+v"(t) : "v"(key), "s"(0xffffff00u), "v"(lanec), "s"(srd), "s"(3120 - 1040 * (I & 3)), "i"(1040 * (I & 3)) : "memory");
  else
    asm volatile("s_mov_b32 m0, %6\n\tv_and_or_b32 %0, %1, %2, %3\n\tbuffer_load_dwordx4 %0, %4, %5 offen sc1 lds"
                 : "+v"(t) : "v"(key), "s"(0xffffff00u), "v"(lanec), "s"(srd), "s"(3120), "s"(lds_base + 4352 * (I >> 2)) : "memory");
}
DI void dma4_dev(const void* sbase, unsigned voff, unsigned lds_base) {
  asm volatile("s_mov_b32 m0, %2\n\ts_nop 0\n\tglobal_load_lds_dword %0, %1 sc1" ::"v"(voff), "s"(sbase), "s"(lds_base) : "memory");
}
DI void wait_vm0() { asm volatile("s_waitcnt vmcnt(0)" ::: "memory"); }
DI void barrier() { asm volatile("s_waitcnt lgkmcnt(0)\n\ts_barrier" ::: "memory"); }

#define STAMP(i)
#define STAMP_OUT()

__global__ __launch_bounds__(512, 2) void egnn_fused(const u16* __restrict__ h, const u16* __restrict__ pos,
                                                     const u32x2* __restrict__ eattr, const int* __restrict__ src,
                                                     const int* __restrict__ dst, const u16* __restrict__ we,
                                                     const u16* __restrict__ wx, const u16* __restrict__ wh,
                                                     char* ws, u16* __restrict__ out) {
  __shared__ __attribute__((aligned(256))) char lds[L_END];
  const unsigned lbase = (unsigned)(size_t)lds;
  const int tid = threadIdx.x, lane = tid & 63;
  const int wv = __builtin_amdgcn_readfirstlane(tid >> 6);
  const int w = blockIdx.x;
  const int b = (w & 7) * 32 + (w >> 3);
  const int n0 = bucket_start(b), nsz = bucket_start(b + 1) - n0;
  const int r = lane & 31, hh = lane >> 5;
  const int blk = (lane >> 4) & 1, q4 = (lane & 15) >> 2, p4 = lane & 3;
  const __amdgpu_buffer_rsrc_t wsr = __builtin_amdgcn_make_buffer_rsrc(ws, 0, WS_END, 0x00020000);
  STAMP(0);

  auto pos_early = [&]() {
#pragma unroll
    for (int i = 0; i < 5; i++) dma16(pos, (unsigned)((8 * i + wv) * 1024 + lane * 16), lbase + L_ROWS + (8 * i + wv) * 1024);
    if (wv < 6) dma16(pos, (unsigned)((40 + wv) * 1024 + lane * 16), lbase + L_ROWS + (40 + wv) * 1024);
  };
  auto pos_late = [&]() {
    dma16(pos, (unsigned)((46 + wv) * 1024 + lane * 16), lbase + L_ROWS + (46 + wv) * 1024);
    if (wv < 4 || (wv == 4 && lane < 38)) dma16(pos, (unsigned)((54 + wv) * 1024 + lane * 16), lbase + L_ROWS + (54 + wv) * 1024);
  };

  int d[3], s[3];
  u32x2 a[3];
  unsigned tagv;
  const bool v2 = tid < CH - 1024;
  if (O_TOUCH) {
    const char* pp[14] = {(const char*)(dst + w * CH), (const char*)we, (const char*)(h + n0 * 128), (const char*)(src + w * CH),
                          (const char*)(eattr + w * CH), ws + WS_EP, (const char*)pos, (const char*)wx, (const char*)wh,
                          ws + WS_BT + n0 * 256, ws + WS_R + w * CHS * 16, ws + WS_TAB + w * NB * 8, (const char*)(out + n0 * 128),
                          (const char*)(out + NN * 128)};
    unsigned lo = 0, hi = 0;
#pragma unroll
    for (int i = 0; i < 14; i++) {
      const unsigned long long a_ = (unsigned long long)pp[i];
      asm volatile("v_writelane_b32 %0, %2, %4\n\tv_writelane_b32 %1, %3, %4" : "+v"(lo), "+v"(hi) : "s"((unsigned)a_), "s"((unsigned)(a_ >> 32)), "i"(i));
    }
    if (lane < (O_TOUCH == 1 ? 9 : 14)) {
      const unsigned long long va = ((unsigned long long)hi << 32) | lo;
      asm volatile("s_mov_b32 m0, %1\n\ts_nop 0\n\tglobal_load_lds_dword %0, off" ::"v"(va), "s"(lbase + L_DUM) : "memory");
    }
  }
  const unsigned wlb = (unsigned)((lane >> 4) * 256 + 16 * ((lane & 15) ^ ((lane >> 4) << 2)));
  {
    const unsigned e0 = (unsigned)(w * CH + tid) * 4, e2 = v2 ? e0 + 4096 : e0;
    asm volatile("global_load_dword %0, %3, %5\n\tglobal_load_dword %1, %3, %5 offset:2048\n\tglobal_load_dword %2, %4, %5"
                 : "=&v"(d[0]), "=&v"(d[1]), "=&v"(d[2]) : "v"(e0), "v"(e2), "s"(dst) : "memory");
    const int row = 4 * wv + (lane >> 4), c = lane & 15;
#pragma unroll
    for (int i = 0; i < 4; i++) dma16(we, (wlb ^ (16 * i)) + (32 + 4 * wv + i) * 1024, lbase + L_W + (32 + 4 * wv + i) * 1024);
    dma16(h, (unsigned)(min(n0 + row, NN - 1) * 256 + 16 * (c ^ (row & 15))), lbase + L_H + wv * 1024);
    if (wv < 2) dma16(h, (unsigned)(min(n0 + 32 + row, NN - 1) * 256 + 16 * (c ^ (row & 15))), lbase + L_H + (8 + wv) * 1024);
    asm volatile("global_load_dword %0, %7, %12\n\tglobal_load_dword %1, %7, %12 offset:2048\n\tglobal_load_dword %2, %8, %12\n\t"
                 "global_load_dwordx2 %3, %9, %13\n\tglobal_load_dwordx2 %4, %10, %13\n\tglobal_load_dwordx2 %5, %11, %13\n\t"
                 "global_load_dword %6, %15, %14 sc1"
                 : "=&v"(s[0]), "=&v"(s[1]), "=&v"(s[2]), "=&v"(a[0]), "=&v"(a[1]), "=&v"(a[2]), "=&v"(tagv)
                 : "v"(e0), "v"(e2), "v"(2 * e0), "v"(2 * e0 + 4096), "v"(2 * e2), "s"(src), "s"(eattr), "s"(ws), "v"(WS_EP)
                 : "memory");
  }
  if (tid < 256) *(u32*)(lds + L_HIST + tid * 4) = 0u;
  else {
    const int t = tid - 256;
    if (t < 32) {
      const int j = (t >> 2) - 4 * ((t >> 1) & 1) - 2 * (t & 1);
      *(u32*)(lds + L_OH + t * 4) = j == 0 ? 0x3F80u : j == 1 ? 0x3F800000u : 0u;
      if (t < 16) *(u32*)(lds + L_GC + t * 4) = 0u;
    }
  }
  barrier();
  {
    if (wv == 3) dma16(we, 65536 + lane * 16, lbase + L_EXT);
    if (wv == 4 && lane < 16) dma16(we, 66560 + lane * 16, lbase + L_EXT + 1024);
    if (wv == 5 && lane < 16) dma16(wx, lane * 16, lbase + L_WX);
    if (wv == 6 && lane < 16) dma16(wh, (unsigned)(w * 256 + lane * 16), lbase + L_BS + 10240);
#pragma unroll
    for (int i = 0; i < 4; i++) dma16(we, (wlb ^ (16 * i)) + (4 * wv + i) * 1024, lbase + L_W + (4 * wv + i) * 1024);
    pos_early();
  }
  const int T = wv & 3, nb = wv >> 2;
  const int loc = 5 * (4 * nb + (r >> 3)) + (r & 7);
  const bool valid = (r & 7) < 5 && loc < nsz;
  int ha = L_H + min(loc, 39) * 256 + 16 * (hh ^ (min(loc, 39) & 15));
  const unsigned trx0 = lbase + L_W + (unsigned)((8 * hh + q4) * 256), trs0 = (unsigned)((q4 << 2) | (2 * hh));
  unsigned b0 = trx0 + 32768 + 16 * ((unsigned)(8 * (T & 1) + 4 * (p4 & 1) + 2 * blk + (T >> 1)) ^ trs0) + 8 * (p4 >> 1);
  unsigned a0 = trx0 + 16 * ((unsigned)(4 * T + 2 * blk + (p4 >> 1)) ^ trs0) + 8 * (p4 & 1);
  int bo = L_BS + loc * 256 + 16 * ((4 * T + 2 * hh) ^ (loc & 15));
  unsigned gbo = WS_BT + (unsigned)((n0 + (tid >> 4)) * 256 + 16 * ((tid ^ (tid >> 4)) & 15));
  PIN(b0); PIN(a0); PIN(bo); PIN(gbo); PIN(ha);
  u32x4 I0, I1;
  {
    const unsigned one = (r & 1) ? 0x3F800000u : 0x3F80u;
#pragma unroll
    for (int jj = 0; jj < 4; jj++) {
      const bool m = ((r & 7) >> 1) == jj;
      I0[jj] = (m && (r >> 3) == hh) ? one : 0u;
      I1[jj] = (m && (r >> 3) == 2 + hh) ? one : 0u;
    }
  }
  const int rowbuf = L_ROWS + wv * ROWBUF;
  unsigned kdma = 16 * ((lane & 15) ^ (4 * (lane >> 4)));
  const int rd0 = rowbuf + 1040 * (r & 3) + 4352 * ((r >> 2) & 1) + (r >> 3) * 256;
  int rdA = rd0 + 64 * ((2 * hh) ^ (r >> 3)), rdB = rd0 + 64 * ((2 * hh + 1) ^ (r >> 3));
  int swr0 = L_S + 32 * (r >> 4) + 16 * ((r >> 2) & 1) + 8 * ((r >> 3) & 1) + 2 * (r & 3) + 160 * hh;
  int srd0 = L_S + r * 80 + 16 * hh;
  const unsigned long long bbase = (unsigned long long)(ws + WS_BT - 3120);
  const u32x4 bsrd = {(unsigned)bbase, (unsigned)(bbase >> 32) & 0xffffu, (unsigned)((NN + 256) * 256 + 3120), 0x00020000u};
  f32x16 Y[4];
#pragma unroll
  for (int t = 0; t < 4; t++) {
    Y[t] = zero16();
    PIN(Y[t]);
  }
  PIN(I0); PIN(I1); PIN(kdma); PIN(rdA); PIN(rdB); PIN(swr0); PIN(srd0);
  asm volatile("s_waitcnt vmcnt(21)" : "+v"(d[0]), "+v"(d[1]), "+v"(d[2])::"memory");
  STAMP(1);
  int key[3];
  unsigned rank[3];
#pragma unroll
  for (int k = 0; k < 3; k++) {
    key[k] = (int)((unsigned)__umul24((unsigned)d[k], 429497u) >> 24);
    rank[k] = 0;
    if (k < 2 || v2) rank[k] = atomicAdd((unsigned*)(lds + L_HIST) + key[k], 1u);
  }
  asm volatile("s_waitcnt vmcnt(16)" ::: "memory");
  barrier();
  STAMP(2);
  u32x4 off4, hf[8], rec[3];
  unsigned oend, tag;
  {
    const u32x4 cnt = *(const u32x4*)(lds + L_HIST + lane * 16);
    const unsigned b1 = (b0 ^ 16) + 1024;
    u32x4 wf[8];
#pragma unroll
    for (int ks = 0; ks < 8; ks++) {
      hf[ks] = *(const u32x4*)(lds + (ha ^ (32 * ks)));
      wf[ks] = tr_read2(b0 + ks * 4096, b1 + ks * 4096);
    }
    SB();
    const unsigned tot = cnt[0] + cnt[1] + cnt[2] + cnt[3];
    oend = (unsigned)wave_incl_scan((int)tot);
    off4[0] = oend - tot;
    off4[1] = off4[0] + cnt[0];
    off4[2] = off4[1] + cnt[1];
    off4[3] = off4[2] + cnt[2];
    *(u32x4*)(lds + L_OFF + lane * 16) = off4;
#pragma unroll
    for (int k = 0; k < 3; k++) rank[k] += *(const u32*)(lds + L_OFF + key[k] * 4);
    f32x16 acc = zero16();
#pragma unroll
    for (int ks = 0; ks < 8; ks++) acc = mfma(wf[ks], hf[ks], acc);
    asm volatile("s_waitcnt vmcnt(9)" : "+v"(s[0]), "+v"(s[1]), "+v"(s[2]), "+v"(a[0]), "+v"(a[1]), "+v"(a[2]), "+v"(tagv)::"memory");
    tag = (unsigned)__builtin_amdgcn_readfirstlane((int)tagv) + 1u;
    SB();
#pragma unroll
    for (int k = 0; k < 3; k++) {
      const int lc = d[k] - bucket_start(key[k]);
      const int g = (lc * 205) >> 10;
      rec[k][0] = a[k][0];
      rec[k][1] = (a[k][1] & 0xffffu) | (tag << 16);
      rec[k][2] = (a[k][1] >> 16) | (tag << 16);
      rec[k][3] = ((unsigned)s[k] << 8) | (unsigned)(lc + 3 * g) | ((tag >> 16) << 22);
      PIN(rec[k]);
    }
    SB();
    u32x4 bt0, bt1;
#pragma unroll
    for (int i = 0; i < 4; i++) {
      bt0[i] = pk2(acc[2 * i] * NLOG2E, acc[2 * i + 1] * NLOG2E);
      bt1[i] = pk2(acc[8 + 2 * i] * NLOG2E, acc[9 + 2 * i] * NLOG2E);
    }
    if ((r & 7) < 5) {
      *(u32x4*)(lds + bo) = bt0;
      *(u32x4*)(lds + (bo ^ 16)) = bt1;
    }
#pragma unroll
    for (int k = 0; k < 3; k++)
      if (k < 2 || v2) *(u32x4*)(lds + L_ST + rank[k] * 16) = rec[k];
  }
  asm volatile("s_waitcnt vmcnt(5)" ::: "memory");
  barrier();
  STAMP(4);
  u32x2 at[4];
  {
    u32x4 vb0 = *(const u32x4*)(lds + L_BS + tid * 16);
    u32x4 vb1 = *(const u32x4*)(lds + L_BS + 8192 + min(tid, 143) * 16);
    u32x4 v0 = *(const u32x4*)(lds + L_ST + tid * 16);
    u32x4 v1 = *(const u32x4*)(lds + L_ST + 8192 + tid * 16);
    u32x4 v2_ = *(const u32x4*)(lds + L_ST + 16384 + (tid & 255) * 16);
    const unsigned a1 = (a0 ^ 16) + 1024;
    u32x4 wf[8];
#pragma unroll
    for (int ks = 0; ks < 8; ks++) wf[ks] = tr_read2(a0 + ks * 4096, a1 + ks * 4096);
    SB();
    __builtin_amdgcn_raw_buffer_store_b128(vb0, wsr, gbo, 0, 16);
    if (tid < 16 * (nsz - 32) || (tid >> 4) == 8)
      __builtin_amdgcn_raw_buffer_store_b128(vb1, wsr, tid < 128 ? gbo + 8192 : WS_BT + (unsigned)((NN + w) * 256 + (tid & 15) * 16), 0, 16);
    asm volatile("" ::: "memory");
    if (tid < 256) __builtin_amdgcn_raw_buffer_store_b128(v2_, wsr, WS_R + (unsigned)(w * CHS + 1024 + tid) * 16, 0, 16);
    __builtin_amdgcn_raw_buffer_store_b128(v0, wsr, WS_R + (unsigned)(w * CHS + tid) * 16, 0, 16);
    __builtin_amdgcn_raw_buffer_store_b128(v1, wsr, WS_R + (unsigned)(w * CHS + 512 + tid) * 16, 0, 16);
    asm volatile("" ::: "memory");
    f32x16 acc = zero16();
#pragma unroll
    for (int ks = 0; ks < 8; ks++) acc = mfma(hf[ks], wf[ks], acc);
#pragma unroll
    for (int i = 0; i < 4; i++) {
      at[i][0] = pk2(acc[4 * i] * NLOG2E, acc[4 * i + 1] * NLOG2E);
      at[i][1] = pk2(acc[4 * i + 2] * NLOG2E, acc[4 * i + 3] * NLOG2E);
      PIN(at[i]);
    }
  }
  asm volatile("s_waitcnt vmcnt(2)" ::: "memory");
  barrier();
  STAMP(5);
  if (wv == 0) {
    const u32x4 g0 = {tag, off4[0] | (off4[1] << 16), tag, off4[1] | (off4[2] << 16)};
    const u32x4 g1 = {tag, off4[2] | (off4[3] << 16), tag, off4[3] | (oend << 16)};
    const unsigned off = WS_TAB + (unsigned)(w * NB + 4 * lane) * 8;
    __builtin_amdgcn_raw_buffer_store_b128(g0, wsr, off, 0, 16);
    __builtin_amdgcn_raw_buffer_store_b128(g1, wsr, off + 16, 0, 16);
  }

  asm volatile("" ::: "memory");
  pos_late();
#pragma unroll
  for (int i = 0; i < 4; i++) *(u32x2*)(lds + L_A + (4 * nb + i) * 2048 + T * 512 + lane * 8) = at[i];
  {
    const u32x4 z = {0u, 0u, 0u, 0u};
    *(u32x4*)(lds + L_S + tid * 16) = z;
    *(u32x4*)(lds + L_S + 8192 + tid * 16) = z;
    if (tid < 256) {
      *(u32x4*)(lds + L_S + 16384 + tid * 16) = z;
    } else {
      const int t = tid - 256, v = t >> 5, q = (t >> 3) & 3, i = t & 7;
      *(u32*)(lds + L_FK + t * 4) = (unsigned)(NN + (i < 4 ? 128 + 32 * q + 4 * v + i : 16 * v + 4 * (i - 4) + q)) << 8;
    }
  }
  u32x4 extB[4];
#pragma unroll
  for (int t = 0; t < 4; t++) {
    const u16* wp = (const u16*)(lds + L_EXT) + 32 * t + r;
    const float w0 = bf2f(wp[(1 + 2 * hh) * 128]) * NLOG2E, w1 = bf2f(wp[(2 + 2 * hh) * 128]) * NLOG2E, w2 = bf2f(wp[0]) * NLOG2E;
    extB[t][0] = 0u;
    extB[t][1] = 0u;
    extB[t][2] = pk2(w0, w1);
    extB[t][3] = hh ? 0u : pk2(w2, 0.f);
    PIN(extB[t]);
  }
  wait_vm0();
  barrier();
  STAMP(6);

  bool wh_loaded = false, slow = false;
  int lo = 0, quota = MAPW, gq = wv;
  unsigned pin = 0, atmp = 0;
  while (true) {
    wh_loaded = false;
    int tq = tid;
    PIN(tq);
    const int lq = tq & 63;
    int kc, kfirst, kpre, wtot;
    {
      const unsigned tabq = WS_TAB + (unsigned)((tq >> 1) * NB + b) * 8;
      u32x2 gq2 = {tag + 1u, 0u};
#pragma clang loop unroll(disable)
      for (int spin = 0; spin < (1 << 22); spin++) {
        if (gq2[0] != tag) asm volatile("global_load_dwordx2 %0, %1, %2 sc1\n\ts_waitcnt vmcnt(0)" : "=&v"(gq2) : "v"(tabq), "s"(ws) : "memory");
        if (__all(gq2[0] == tag)) break;
        __builtin_amdgcn_s_sleep(1);
      }
      const int o0 = min((int)(gq2[1] & 0xffffu), CH), o1 = min((int)(gq2[1] >> 16), CH);
      kc = max(o1 - o0, 0);
      kfirst = (tq >> 1) * CHS + o0;
      const int inc = wave_incl_scan((tq & 1) ? 0 : kc);
      kpre = inc - kc;
      wtot = __builtin_amdgcn_readlane(inc, 63);
      if (!slow && lq == 0) atomicMax((unsigned*)(lds + L_GC + 32), (unsigned)wtot);
    }
    STAMP(7);
    u32x3 q[4];
    u32x2 pd[4], ps[4];
    int ra1[4], par[4];
    bool four;
    {
      const int map = L_MAP + wv * MAPW * 4;
      const int nw = min(wtot - lo, quota);
      const int jhi = min(kc, lo + quota - kpre);
#pragma clang loop vectorize(disable) unroll(disable)
      for (int j = max(0, lo - kpre) + ((tq ^ max(0, lo - kpre)) & 1); j < jhi; j += 2) *(u32*)(lds + map + (kpre + j - lo) * 4) = (unsigned)(kfirst + j) * 16u;
      unsigned ix[4];
#pragma unroll
      for (int k = 0; k < 4; k++) {
        const unsigned mi = *(const u32*)(lds + map + (64 * k + lq) * 4);
        ix[k] = 64 * k + lq < nw ? mi : 0u;
      }
      STAMP(23);
      u32x4 q4_[4];
#pragma clang loop unroll(disable)
      for (int spin = 0; spin < (1 << 22); spin++) {
        asm volatile("global_load_dwordx4 %0, %4, %8 sc1\n\tglobal_load_dwordx4 %1, %5, %8 sc1\n\tglobal_load_dwordx4 %2, %6, %8 sc1\n\t"
                     "global_load_dwordx4 %3, %7, %8 sc1\n\ts_waitcnt vmcnt(0)"
                     : "=&v"(q4_[0]), "=&v"(q4_[1]), "=&v"(q4_[2]), "=&v"(q4_[3]) : "v"(ix[0]), "v"(ix[1]), "v"(ix[2]), "v"(ix[3]), "s"(ws + WS_R) : "memory");
        bool ok = true;
#pragma unroll
        for (int k = 0; k < 4; k++) {
          const unsigned bad = (((q4_[k][1] ^ (tag << 16)) | (q4_[k][2] ^ (tag << 16))) >> 16) | ((q4_[k][3] ^ ((tag >> 16) << 22)) >> 22);
          ok = ok && (bad == 0 || 64 * k + lq >= nw);
        }
        if (__all(ok)) break;
      }
#pragma unroll
      for (int k = 0; k < 4; k++) {
        q[k][0] = q4_[k][0];
        q[k][1] = (q4_[k][1] & 0xffffu) | (q4_[k][2] << 16);
        q[k][2] = q4_[k][3] & 0x3fffffu;
      }
      STAMP(24);
      unsigned rk[4];
#pragma unroll
      for (int k = 0; k < 4; k++) {
        rk[k] = CAPW;
        if (64 * k + lq < nw) rk[k] = atomicAdd((unsigned*)(lds + L_GC) + ((q[k][2] >> 3) & 7u), 1u);
        const unsigned g = (q[k][2] >> 3) & 7u;
        const int dn = n0 + (int)(q[k][2] & 63u) - 3 * (int)g, sn = min((int)(q[k][2] >> 8), NN - 1);
        const int od = L_ROWS + ((6 * dn) & ~3), os = L_ROWS + ((6 * sn) & ~3);
        pd[k][0] = *(const u32*)(lds + od); pd[k][1] = *(const u32*)(lds + od + 4);
        ps[k][0] = *(const u32*)(lds + os); ps[k][1] = *(const u32*)(lds + os + 4);
        par[k] = (dn & 1) * 16;
      }
#pragma unroll
      for (int k = 0; k < 4; k++) {
        const bool ok = rk[k] < CAPW;
        const int e = (int)(((q[k][2] >> 3) & 7u) * CAPW + rk[k]);
        *(u32*)(lds + (ok ? L_R2 + e * 4 : L_DUM + 96)) = q[k][2];
        ra1[k] = ok ? L_R1 + e * 16 : L_DUM + 96;
      }
      four = nw > 192;
    }
    barrier();
    STAMP(8);
    const int cl = *(const u32*)(lds + L_GC + (lane & 7) * 4);
    const int maxtot = __builtin_amdgcn_readfirstlane(*(const u32*)(lds + L_GC + 32));
    if (!slow) {
      const int kk = (cl << 3) | (lane & 7);
      int rk = 0;
      rk += __builtin_amdgcn_update_dpp(0, kk, 0x121, 0xf, 0xf, false) > kk;
      rk += __builtin_amdgcn_update_dpp(0, kk, 0x122, 0xf, 0xf, false) > kk;
      rk += __builtin_amdgcn_update_dpp(0, kk, 0x123, 0xf, 0xf, false) > kk;
      rk += __builtin_amdgcn_update_dpp(0, kk, 0x124, 0xf, 0xf, false) > kk;
      rk += __builtin_amdgcn_update_dpp(0, kk, 0x125, 0xf, 0xf, false) > kk;
      rk += __builtin_amdgcn_update_dpp(0, kk, 0x126, 0xf, 0xf, false) > kk;
      rk += __builtin_amdgcn_update_dpp(0, kk, 0x127, 0xf, 0xf, false) > kk;
      const int cmax = __any(cl > CAPW) ? CAPW + 1 : 0;
      if (maxtot > MAPW || cmax > CAPW) {
        slow = true;
        quota = CAPW / 8;
        barrier();
        if (tid < 8) *(u32*)(lds + L_GC + tid * 4) = 0u;
        barrier();
        continue;
      }
      gq = __builtin_ctzll(__ballot(rk == (wv < 4 ? wv : 11 - wv)));
    }
    const int n = __builtin_amdgcn_readlane(cl, gq);
    const int swr = swr0 + gq * 2560, srd = srd0 + gq * 2560;
    const int l1 = L_R1 + gq * CAPW * 16, l2 = L_R2 + gq * CAPW * 4;
    const int nt = (n + 31) >> 5;
    u32x4 rows[8], extA, S0, S1, ra, rb, m1p = {0u, 0u, 0u, 0u};
    u32x2 r1;
    unsigned r2;
    int sp;
    f32x16 XA, XB;
    auto list_read = [&](int tl) {
      const int list = tl < nt ? l2 + tl * 128 : L_FK + wv * 128;
      ra = *(const u32x4*)(lds + list + (lane >> 4) * 32);
      rb = *(const u32x4*)(lds + list + (lane >> 4) * 32 + 16);
    };
    auto dma_piece = [&](int, int i) {
      if (i == 0) row_dma<0>(bsrd, ra[0], kdma, lbase + rowbuf, atmp);
      if (i == 1) row_dma<1>(bsrd, ra[1], kdma, lbase + rowbuf, atmp);
      if (i == 2) row_dma<2>(bsrd, ra[2], kdma, lbase + rowbuf, atmp);
      if (i == 3) row_dma<3>(bsrd, ra[3], kdma, lbase + rowbuf, atmp);
      if (i == 4) row_dma<4>(bsrd, rb[0], kdma, lbase + rowbuf, atmp);
      if (i == 5) row_dma<5>(bsrd, rb[1], kdma, lbase + rowbuf, atmp);
      if (i == 6) row_dma<6>(bsrd, rb[2], kdma, lbase + rowbuf, atmp);
      if (i == 7) row_dma<7>(bsrd, rb[3], kdma, lbase + rowbuf, atmp);
    };
    auto meta_read = [&](int tl) {
      r1 = *(const u32x2*)(lds + l1 + (tl * 32 + r) * 16 + hh * 8);
      r2 = *(const u32*)(lds + l2 + (tl * 32 + r) * 4);
    };
    auto meta_use = [&](int tl) {
      const unsigned dl = r2 & 7u;
      const u32x2 oh = *(const u32x2*)(lds + L_OH + dl * 16 + hh * 8);
      extA[0] = oh[0]; extA[1] = oh[1]; extA[2] = r1[0]; extA[3] = r1[1];
      sp = (tl * 32 + r < n) ? swr + (int)dl * 320 : L_DUM;
    };
    auto s_write = [&]() {
      *(u16*)(lds + sp) = (u16)(hh ? extA[3] : 0x3F80u);
      *(u16*)(lds + sp + 80) = (u16)(extA[3] >> 16);
    };
    auto rows_read = [&]() {
#pragma unroll
      for (int j = 0; j < 8; j++) rows[j] = *(const u32x4*)(lds + (j < 4 ? rdA : rdB) + 16 * (j & 3));
    };
    auto rows_pin = [&]() {
      asm volatile("" : "+v"(rows[0]), "+v"(rows[1]), "+v"(rows[2]), "+v"(rows[3]), "+v"(rows[4]), "+v"(rows[5]), "+v"(rows[6]), "+v"(rows[7]));
    };

    list_read(0);
    if (n > 0 && (!O_STAG || wv < 4)) {
#pragma unroll
      for (int i = 0; i < 8; i++) dma_piece(0, i);
    }
    STAMP(18);
    auto finish = [&](int k) {
      const unsigned long long ud = (((unsigned long long)pd[k][1] << 32) | pd[k][0]) >> par[k];
      const unsigned long long us = (((unsigned long long)ps[k][1] << 32) | ps[k][0]) >> ((q[k][2] >> 4) & 16u);
      const unsigned dlo = (unsigned)ud, dhi = (unsigned)(ud >> 32), slo = (unsigned)us, shi = (unsigned)(us >> 32);
      const unsigned dxy = pk2(bf2f(dlo) - bf2f(slo), bfhi(dlo) - bfhi(slo));
      const unsigned dz_ = pk2(bf2f(dhi) - bf2f(shi), 0.f);
      const float dx = bf2f(dxy), dy = bfhi(dxy), dz = bf2f(dz_);
      const unsigned sxy = pk2(dx * dx, dy * dy), sz = pk2(dz * dz, 0.f);
      const unsigned d2 = pk2((bf2f(sxy) + bfhi(sxy)) + bf2f(sz), 0.f) & 0xffffu;
      const u32x4 rec = {q[k][0], d2 | (dxy << 16), q[k][1], (dxy >> 16) | (dz_ << 16)};
      *(u32x4*)(lds + ra1[k]) = rec;
    };
    finish(0);
    finish(1);
    finish(2);
    if (four) finish(3);
    if (lane < 32 * nt - n) {
      const u32x4 z = {0u, 0u, 0u, 0u};
      *(u32x4*)(lds + l1 + (n + lane) * 16) = z;
    }
    if (w == 0 && tid == 0) __builtin_amdgcn_raw_buffer_store_b32(tag, wsr, WS_EP, 0, 16);
    pin = pos[min(3 * n0 + 15 * gq + (lq & 15), 3 * NN - 1)];
#pragma unroll
    for (int t = 0; t < 4; t++) {
      const u32x2 v = *(const u32x2*)(lds + L_A + gq * 2048 + t * 512 + lq * 8);
      extB[t][0] = v[0];
      extB[t][1] = v[1];
    }
    STAMP(19);
    barrier();
    STAMP(20);
    if (O_PRIO == 1 && wv >= 4) __builtin_amdgcn_s_setprio(1);
    if (O_PRIO == 2 && wv < 4) __builtin_amdgcn_s_setprio(1);
    if (n > 0) {
    if (O_STAG && wv >= 4) {
#pragma unroll
      for (int i = 0; i < 8; i++) dma_piece(0, i);
    }
    meta_read(0);
    list_read(1);
    meta_use(0);
    s_write();
    S1 = m1p;
    STAMP(21);
    wait_vm0();
    STAMP(22);
    PIN(pin);
    rows_read();
    rows_pin();
    XA = mfma(rows[0], I0, zero16());
    XA = mfma(rows[1], I1, XA);
    XA = mfma(extA, extB[0], XA);
    STAMP(9);

    const int nfull = O_HALF ? (n + 15) >> 5 : nt;
    for (int tl = 0; tl < nfull; tl++) {
#pragma unroll
      for (int t = 0; t < 4; t++) {
        f32x16& Xc = (t & 1) ? XB : XA;
        f32x16& Xn = (t & 1) ? XA : XB;
        const int tn = (t + 1) & 3;
        auto gap = [&](int k) {
          SB();
          if (t == 0 && k < 8 && tl == 0) dma_piece(1, k);
          if (t == 0 && k == 0) S0 = *(const u32x4*)(lds + srd);
          if (t == 0 && k == 3) S1 = *(const u32x4*)(lds + srd + 32);
          if (t == 0 && k == 4) {
            *(u16*)(lds + sp) = 0;
            *(u16*)(lds + sp + 80) = 0;
          }
          if (t == 2 && k == 0) meta_read(tl + 1);
          if (t == 2 && k == 2) meta_use(tl + 1);
          if (t == 2 && k == 3) wait_vm0();
          if (t == 2 && k >= 3 && k < 7) {
            rows[2 * k - 6] = *(const u32x4*)(lds + (k < 5 ? rdA : rdB) + 16 * ((2 * k - 6) & 3));
            rows[2 * k - 5] = *(const u32x4*)(lds + (k < 5 ? rdA : rdB) + 16 * ((2 * k - 5) & 3));
          }
          if (t == 2 && k == 7) list_read(tl + 2);
          if (t == 3 && k < 8) dma_piece(tl + 2, k);
          if (t == 3 && k == 8) s_write();
          SB();
        };
        f32x2 dd[8];
        if (O_PRIO == 4 && t == 0) __builtin_amdgcn_s_setprio(1);
        if (O_PRIO == 4 && t == 2) __builtin_amdgcn_s_setprio(0);
        if (O_PRIO == 7 && t == 3 && wv >= 4) __builtin_amdgcn_s_setprio(1);
        if (O_PRIO == 7 && t == 2 && wv >= 4) __builtin_amdgcn_s_setprio(0);
        SB();
        Xn = mfma(rows[2 * tn], I0, zero16());
        PIN(Xn);
        SB();
#pragma unroll
        for (int g = 0; g < 4; g++) {
#pragma unroll
          for (int i = 2 * g; i < 2 * g + 2; i++) {
            dd[i][0] = __builtin_amdgcn_exp2f(Xc[2 * i]);
            dd[i][1] = __builtin_amdgcn_exp2f(Xc[2 * i + 1]);
            PIN(dd[i]);
          }
          SB();
          if (g == 0) { Xn = mfma(rows[2 * tn + 1], I1, Xn); PIN(Xn); }
          if (g == 1) { Xn = mfma(extA, extB[tn], Xn); PIN(Xn); }
          if (g == 2) { Y[(t + 3) & 3] = mfma(S1, m1p, Y[(t + 3) & 3]); PIN(Y[(t + 3) & 3]); }
          gap(g);
        }
#pragma unroll
        for (int i = 0; i < 8; i++) { dd[i] += 1.0f; if (i < 7) PIN(dd[i]); }
        gap(4);
#pragma unroll
        for (int g = 0; g < 4; g++) {
#pragma unroll
          for (int i = 2 * g; i < 2 * g + 2; i++) {
            dd[i][0] = __builtin_amdgcn_rcpf(dd[i][0]);
            dd[i][1] = __builtin_amdgcn_rcpf(dd[i][1]);
            PIN(dd[i]);
          }
          gap(5 + g);
        }
        u32x4 m0;
#pragma unroll
        for (int i = 0; i < 4; i++) { f32x2 x = {Xc[2 * i], Xc[2 * i + 1]}; dd[i] *= x; }
#pragma unroll
        for (int i = 0; i < 4; i++) m0[i] = __builtin_bit_cast(unsigned, __builtin_convertvector(dd[i], bf16x2));
        SB();
#pragma unroll
        for (int i = 4; i < 8; i++) { f32x2 x = {Xc[2 * i], Xc[2 * i + 1]}; dd[i] *= x; if (i < 7) PIN(dd[i]); }
        SB();
        Y[t] = mfma(S0, m0, Y[t]);
        PIN(Y[t]);
        SB();
#pragma unroll
        for (int i = 0; i < 4; i++) m1p[i] = __builtin_bit_cast(unsigned, __builtin_convertvector(dd[4 + i], bf16x2));
        PIN(m1p);
        if (t == 2) rows_pin();
      }
    }
    SB();
    Y[3] = mfma(S1, m1p, Y[3]);
    if (nfull < nt) {
      S0 = *(const u32x4*)(lds + srd);
      *(u16*)(lds + sp) = 0;
      *(u16*)(lds + sp + 80) = 0;
#pragma unroll
      for (int t = 0; t < 4; t++) {
        f32x16& Xc = (t & 1) ? XB : XA;
        f32x16& Xn = (t & 1) ? XA : XB;
        f32x2 dd[4];
        if (t < 2 && nfull == 0) {
#pragma unroll
          for (int i = 0; i < 4; i++) dma_piece(nt, 4 * t + i);
        }
        SB();
        if (t < 3) { Xn = mfma(rows[2 * t + 2], I0, zero16()); PIN(Xn); }
        SB();
#pragma unroll
        for (int i = 0; i < 2; i++) { dd[i][0] = __builtin_amdgcn_exp2f(Xc[2 * i]); dd[i][1] = __builtin_amdgcn_exp2f(Xc[2 * i + 1]); }
        SB();
        if (t < 3) { Xn = mfma(rows[2 * t + 3], I1, Xn); PIN(Xn); }
        SB();
#pragma unroll
        for (int i = 2; i < 4; i++) { dd[i][0] = __builtin_amdgcn_exp2f(Xc[2 * i]); dd[i][1] = __builtin_amdgcn_exp2f(Xc[2 * i + 1]); }
        SB();
        if (t < 3) { Xn = mfma(extA, extB[t + 1], Xn); PIN(Xn); }
        SB();
#pragma unroll
        for (int i = 0; i < 4; i++) dd[i] += 1.0f;
        SB();
#pragma unroll
        for (int i = 0; i < 4; i++) { dd[i][0] = __builtin_amdgcn_rcpf(dd[i][0]); dd[i][1] = __builtin_amdgcn_rcpf(dd[i][1]); }
        SB();
        u32x4 m0;
#pragma unroll
        for (int i = 0; i < 4; i++) { f32x2 x = {Xc[2 * i], Xc[2 * i + 1]}; dd[i] *= x; }
#pragma unroll
        for (int i = 0; i < 4; i++) m0[i] = __builtin_bit_cast(unsigned, __builtin_convertvector(dd[i], bf16x2));
        SB();
        Y[t] = mfma(S0, m0, Y[t]);
        if (t < 3) PIN(Xn);
      }
    }
    wh_loaded = true;
    wait_vm0();
    }
    if (!slow) break;
    lo += quota;
    if (lo >= maxtot) break;
    barrier();
    if (tid < 8) *(u32*)(lds + L_GC + tid * 4) = 0u;
    pos_early();
    pos_late();
    wait_vm0();
    barrier();
  }
  STAMP(10);
  if (!wh_loaded) {
    wait_vm0();
    PIN(pin);
    const u32x4 fa = *(const u32x4*)(lds + L_FK + wv * 128 + (lane >> 4) * 32), fb = *(const u32x4*)(lds + L_FK + wv * 128 + (lane >> 4) * 32 + 16);
    row_dma<0>(bsrd, fa[0], kdma, lbase + rowbuf, atmp); row_dma<1>(bsrd, fa[1], kdma, lbase + rowbuf, atmp);
    row_dma<2>(bsrd, fa[2], kdma, lbase + rowbuf, atmp); row_dma<3>(bsrd, fa[3], kdma, lbase + rowbuf, atmp);
    row_dma<4>(bsrd, fb[0], kdma, lbase + rowbuf, atmp); row_dma<5>(bsrd, fb[1], kdma, lbase + rowbuf, atmp);
    row_dma<6>(bsrd, fb[2], kdma, lbase + rowbuf, atmp); row_dma<7>(bsrd, fb[3], kdma, lbase + rowbuf, atmp);
    wait_vm0();
  }

#pragma unroll
  for (int i = 0; i < 3; i++) {
    if (i < 2 || hh == 0) {
      const u32x2 mv = {pk2(Y[0][4 * i] * NLN2, Y[1][4 * i] * NLN2), pk2(Y[2][4 * i] * NLN2, Y[3][4 * i] * NLN2)};
      *(u32x2*)(lds + L_M + (5 * gq + 2 * i + hh) * MSTR + 8 * r) = mv;
    }
  }
  u32x4 hq[8], res[2];
  unsigned wxr[4];
#pragma unroll
  for (int ks = 0; ks < 8; ks++) hq[ks] = *(const u32x4*)(lds + (ha ^ (32 * ks)));
  {
    const int ra_ = (ha ^ (16 * hh)) ^ (64 * T + 32 * hh);
    res[0] = *(const u32x4*)(lds + ra_);
    res[1] = *(const u32x4*)(lds + (ra_ ^ 16));
  }
#pragma unroll
  for (int t = 0; t < 4; t++) wxr[t] = ((const u16*)(lds + L_WX))[32 * t + r];
  unsigned wa = lbase + L_ROWS + 2080 * hh + 256 * q4 + 64 * (T ^ q4) + 32 * (p4 & 1) + 16 * blk + 8 * (p4 >> 1);
  int ma = L_M + min(loc, 39) * MSTR + 16 * hh;
  PIN(wa);
  PIN(ma);
  STAMP(13);
  barrier();
  STAMP(11);
  {
    u32x4 w1[8], w2[8], mq[8];
#pragma unroll
    for (int ks = 0; ks < 8; ks++) w1[ks] = tr_read2(wa + ks * ROWBUF + 4352, wa + ks * ROWBUF + 4352 + 1040);
#pragma unroll
    for (int ks = 0; ks < 8; ks++) {
      w2[ks] = tr_read2(wa + ks * ROWBUF, wa + ks * ROWBUF + 1040);
      mq[ks] = *(const u32x4*)(lds + ma + 32 * ks);
    }
    SB();
    f32x16 P = zero16();
#pragma unroll
    for (int ks = 0; ks < 8; ks++) P = mfma(w1[ks], hq[ks], P);
#pragma unroll
    for (int ks = 0; ks < 8; ks++) P = mfma(w2[ks], mq[ks], P);
    PIN(P);
    SB();
    float zz[9];
    {
      float wxv[4];
#pragma unroll
      for (int t = 0; t < 4; t++) wxv[t] = bf2f(wxr[t]) * NLN2;
#pragma unroll
      for (int i = 0; i < 9; i++) {
        const int j = 4 * (i / 3) + 1 + i % 3;
        zz[i] = Y[0][j] * wxv[0];
        PIN(zz[i]);
        zz[i] = __builtin_fmaf(Y[1][j], wxv[1], zz[i]);
        PIN(zz[i]);
        zz[i] = __builtin_fmaf(Y[2][j], wxv[2], zz[i]);
        PIN(zz[i]);
        zz[i] = __builtin_fmaf(Y[3][j], wxv[3], zz[i]);
        PIN(zz[i]);
      }
    }
    SB();
    f32x2 x[8], e[8];
#pragma unroll
    for (int i = 0; i < 8; i++) {
      x[i][0] = P[2 * i];
      x[i][1] = P[2 * i + 1];
      e[i] = x[i] * NLOG2E;
    }
    SB();
#pragma unroll
    for (int i = 0; i < 8; i++) { e[i][0] = __builtin_amdgcn_exp2f(e[i][0]); e[i][1] = __builtin_amdgcn_exp2f(e[i][1]); }
    SB();
#pragma unroll
    for (int i = 0; i < 8; i++) e[i] += 1.0f;
    SB();
#pragma unroll
    for (int i = 0; i < 8; i++) { e[i][0] = __builtin_amdgcn_rcpf(e[i][0]); e[i][1] = __builtin_amdgcn_rcpf(e[i][1]); }
    SB();
    u32x4 ov[2];
#pragma unroll
    for (int i = 0; i < 8; i++) {
      const unsigned rs = res[i >> 2][i & 3];
      const f32x2 rv = {bf2f(rs), bfhi(rs)};
      const f32x2 o = __builtin_elementwise_fma(x[i], e[i], rv);
      ov[i >> 2][i & 3] = pk2(o[0], o[1]);
    }
    if (valid) {
      u32x4* op = (u32x4*)((char*)out + (unsigned)(n0 + loc) * 256 + 64 * T + 32 * hh);
      op[0] = ov[0];
      op[1] = ov[1];
    }
    STAMP(17);
#define DPP9(ctl)                                                                                                         \
  "v_add_f32_dpp %0, %0, %0 " ctl "\n\tv_add_f32_dpp %1, %1, %1 " ctl "\n\tv_add_f32_dpp %2, %2, %2 " ctl "\n\t"          \
  "v_add_f32_dpp %3, %3, %3 " ctl "\n\tv_add_f32_dpp %4, %4, %4 " ctl "\n\tv_add_f32_dpp %5, %5, %5 " ctl "\n\t"          \
  "v_add_f32_dpp %6, %6, %6 " ctl "\n\tv_add_f32_dpp %7, %7, %7 " ctl "\n\tv_add_f32_dpp %8, %8, %8 " ctl "\n\t"
    asm volatile("s_nop 1\n\t" DPP9("row_shr:1 row_mask:0xf bank_mask:0xf bound_ctrl:1") DPP9("row_shr:2 row_mask:0xf bank_mask:0xf bound_ctrl:1")
                 DPP9("row_shr:4 row_mask:0xf bank_mask:0xf bound_ctrl:1") DPP9("row_shr:8 row_mask:0xf bank_mask:0xf bound_ctrl:1")
                 DPP9("row_bcast:15 row_mask:0xa bank_mask:0xf") "s_nop 0"
                 : "+v"(zz[0]), "+v"(zz[1]), "+v"(zz[2]), "+v"(zz[3]), "+v"(zz[4]), "+v"(zz[5]), "+v"(zz[6]), "+v"(zz[7]), "+v"(zz[8]));
    const int zp = L_S + gq * 2560;
    if (r == 31) {
#pragma unroll
      for (int i = 0; i < 9; i++) *(u32*)(lds + zp + 12 * hh + 24 * (i / 3) + 4 * (i % 3)) = __float_as_uint(zz[i]);
    }
    asm volatile("" ::: "memory");
    const float z = __uint_as_float(*(const u32*)(lds + zp + 4 * (lane & 15)));
    if (lane < 15 && 15 * gq + lane < 3 * nsz) out[(size_t)NN * 128 + (unsigned)(3 * n0 + 15 * gq + lane)] = (u16)pk2(bf2f(pin) + z, 0.f);
  }
  STAMP(12);
  STAMP_OUT();
}

extern "C" void kernel_launch(void* const* d_in, const int* in_sizes, int n_in, void* d_out, int out_size, void* d_ws,
                              size_t ws_size, hipStream_t stream) {
  egnn_fused<<<NB, 512, 0, stream>>>((const u16*)d_in[0], (const u16*)d_in[1], (const u32x2*)d_in[2], (const int*)d_in[3],
                                     (const int*)d_in[4], (const u16*)d_in[5], (const u16*)d_in[6], (const u16*)d_in[7],
                                     (char*)d_ws, (u16*)d_out);
}
```

```cpp
#include <hip/hip_runtime.h>
#include <hip/hip_bf16.h>
#include <stdint.h>

typedef __bf16 bf16x8 __attribute__((ext_vector_type(8)));
typedef __bf16 bf16x2 __attribute__((ext_vector_type(2)));
typedef short s16x4 __attribute__((ext_vector_type(4)));
typedef float f32x2 __attribute__((ext_vector_type(2)));
typedef float f32x4 __attribute__((ext_vector_type(4), may_alias));
typedef float f32x16 __attribute__((ext_vector_type(16)));
typedef unsigned u32x2 __attribute__((ext_vector_type(2), may_alias));
typedef unsigned u32x3 __attribute__((ext_vector_type(3)));
typedef unsigned u32x4 __attribute__((ext_vector_type(4), may_alias));
typedef unsigned u32x2a4 __attribute__((ext_vector_type(2), aligned(4), may_alias));
typedef unsigned short u16 __attribute__((may_alias));
typedef unsigned u32 __attribute__((may_alias));
#define LDSP(T) __attribute__((address_space(3))) T*
#define DI __device__ __forceinline__
#define SB() __builtin_amdgcn_sched_barrier(0)
#define PIN(x) asm volatile("" : "+v"(x))

#define O_PRE 0
#define O_STAG 1
#define O_HALF 1
#define O_SLICE 1
#define O_POS2 0
#define O_PRIO 7
constexpr int NN = 10000;
constexpr int CH = 1250, CHS = 1280;
constexpr int NB = 256;
constexpr float NLOG2E = -1.4426950408889634f;
constexpr float NLN2 = -0.6931471805599453f;

constexpr unsigned WS_BT = 4096;
constexpr unsigned WS_R = WS_BT + (NN + 256) * 256;
constexpr unsigned WS_TAB = WS_R + NB * CHS * 16;
constexpr unsigned WS_FB = WS_TAB + NB * NB * 8;
constexpr unsigned WS_EP = WS_FB + NB * 4;
constexpr unsigned WS_END = WS_EP + 256;

constexpr int CAPW = 224;
constexpr int ROWBUF = 8496;
constexpr int L_ROWS = 0;
constexpr int L_R1 = 8 * ROWBUF;
constexpr int L_R2 = L_R1 + 8 * CAPW * 16;
constexpr int L_S = L_R2 + 8 * CAPW * 4;
constexpr int L_M = L_S + 8 * 2560;
constexpr int MSTR = 272;
constexpr int L_H = 135168;
constexpr int L_A = L_H + 10240;
constexpr int L_HIST = L_A + 8 * 2048;
constexpr int L_OH = L_HIST + 1024;
constexpr int L_GC = L_OH + 128;
constexpr int L_DUM = L_GC + 64;
constexpr int L_WX = L_DUM + 128;
constexpr int L_END = L_WX + 256;
constexpr int L_W = L_R1;
constexpr int L_EXT = L_W + 65536;
constexpr int L_ST = 47104;
constexpr int L_BS = L_A;
constexpr int L_OFF = L_A + 10496;
constexpr int MAPW = 256;
constexpr int L_MAP = L_M;
constexpr int L_FK = L_HIST;
static_assert(L_ST + 20480 <= L_R1 && L_BS % 256 == 0 && L_W % 32 == 0 && L_W + 65536 <= L_EXT && L_EXT + 1280 <= L_H && L_H % 256 == 0 && L_M + 40 * MSTR <= L_H && L_END <= 163840, "lds map");

DI constexpr int piece_off(int i) { return 1040 * (i & 3) + 4352 * (i >> 2); }
DI float bf2f(unsigned u) { return __uint_as_float(u << 16); }
DI float bfhi(unsigned u) { return __uint_as_float(u & 0xffff0000u); }
DI unsigned pk2(float a, float b) {
  f32x2 v = {a, b};
  return __builtin_bit_cast(unsigned, __builtin_convertvector(v, bf16x2));
}
DI f32x16 mfma(u32x4 a, u32x4 b, f32x16 c) {
  return __builtin_amdgcn_mfma_f32_32x32x16_bf16(__builtin_bit_cast(bf16x8, a), __builtin_bit_cast(bf16x8, b), c, 0, 0, 0);
}
DI f32x16 zero16() {
  f32x16 z;
#pragma unroll
  for (int i = 0; i < 16; i++) z[i] = 0.f;
  return z;
}
DI u32x4 tr_read2(unsigned a0, unsigned a1) {
  const u32x2 lo = __builtin_bit_cast(u32x2, __builtin_amdgcn_ds_read_tr16_b64_v4i16((LDSP(s16x4))(size_t)a0));
  const u32x2 hi = __builtin_bit_cast(u32x2, __builtin_amdgcn_ds_read_tr16_b64_v4i16((LDSP(s16x4))(size_t)a1));
  const u32x4 v = {lo[0], lo[1], hi[0], hi[1]};
  return v;
}
DI int bucket_start(int b) { return (b * 625 + 15) >> 4; }
DI int wave_incl_scan(int x) {
  x += __builtin_amdgcn_update_dpp(0, x, 0x111, 0xf, 0xf, false);
  x += __builtin_amdgcn_update_dpp(0, x, 0x112, 0xf, 0xf, false);
  x += __builtin_amdgcn_update_dpp(0, x, 0x114, 0xf, 0xf, false);
  x += __builtin_amdgcn_update_dpp(0, x, 0x118, 0xf, 0xf, false);
  x += __builtin_amdgcn_update_dpp(0, x, 0x142, 0xa, 0xf, false);
  x += __builtin_amdgcn_update_dpp(0, x, 0x143, 0xc, 0xf, false);
  return x;
}
DI void dma16(const void* sbase, unsigned voff, unsigned lds_base) {
  asm volatile("s_mov_b32 m0, %2\n\ts_nop 0\n\tglobal_load_lds_dwordx4 %0, %1" ::"v"(voff), "s"(sbase), "s"(lds_base) : "memory");
}
DI void dma16_dev(const void* sbase, unsigned voff, unsigned lds_base) {
  asm volatile("s_mov_b32 m0, %2\n\ts_nop 0\n\tglobal_load_lds_dwordx4 %0, %1 sc1" ::"v"(voff), "s"(sbase), "s"(lds_base) : "memory");
}
template <int I>
DI void row_dma(u32x4 srd, unsigned key, unsigned lanec, unsigned lds_base, unsigned& t) {
  if (I & 3)
    asm volatile("v_and_or_b32 %0, %1, %2, %3\n\tbuffer_load_dwordx4 %0, %4, %5 offen offset:%6 sc1 lds"
                 : "+v"(t) : "v"(key), "s"(0xffffff00u), "v"(lanec), "s"(srd), "s"(3120 - 1040 * (I & 3)), "i"(1040 * (I & 3)) : "memory");
  else
    asm volatile("s_mov_b32 m0, %6\n\tv_and_or_b32 %0, %1, %2, %3\n\tbuffer_load_dwordx4 %0, %4, %5 offen sc1 lds"
                 : "+v"(t) : "v"(key), "s"(0xffffff00u), "v"(lanec), "s"(srd), "s"(3120), "s"(lds_base + 4352 * (I >> 2)) : "memory");
}
DI void dma4_dev(const void* sbase, unsigned voff, unsigned lds_base) {
  asm volatile("s_mov_b32 m0, %2\n\ts_nop 0\n\tglobal_load_lds_dword %0, %1 sc1" ::"v"(voff), "s"(sbase), "s"(lds_base) : "memory");
}
DI void wait_vm0() { asm volatile("s_waitcnt vmcnt(0)" ::: "memory"); }
DI void barrier() { asm volatile("s_waitcnt lgkmcnt(0)\n\ts_barrier" ::: "memory"); }

#define STAMP(i)
#define STAMP_OUT()

__global__ __launch_bounds__(512, 2) void egnn_fused(const u16* __restrict__ h, const u16* __restrict__ pos,
                                                     const u32x2* __restrict__ eattr, const int* __restrict__ src,
                                                     const int* __restrict__ dst, const u16* __restrict__ we,
                                                     const u16* __restrict__ wx, const u16* __restrict__ wh,
                                                     char* ws, u16* __restrict__ out) {
  __shared__ __attribute__((aligned(256))) char lds[L_END];
  const unsigned lbase = (unsigned)(size_t)lds;
  const int tid = threadIdx.x, lane = tid & 63;
  const int wv = __builtin_amdgcn_readfirstlane(tid >> 6);
  const int w = blockIdx.x;
  const int b = (w & 7) * 32 + (w >> 3);
  const int n0 = bucket_start(b), nsz = bucket_start(b + 1) - n0;
  const int r = lane & 31, hh = lane >> 5;
  const int blk = (lane >> 4) & 1, q4 = (lane & 15) >> 2, p4 = lane & 3;
  const __amdgpu_buffer_rsrc_t wsr = __builtin_amdgcn_make_buffer_rsrc(ws, 0, WS_END, 0x00020000);
  STAMP(0);

  auto pos_tile = [&](int i) {
    const int j = i < 5 ? 8 * i + wv : i == 5 ? 46 + wv : i == 6 ? 40 + wv : 54 + wv;
    if (i < 6 || (i == 6 && wv < 6) || (i == 7 && (wv < 4 || (wv == 4 && lane < 38))))
      dma16(pos, (unsigned)(j * 1024 + lane * 16), lbase + L_ROWS + j * 1024);
  };

  int d[3], s[3];
  u32x2 a[3];
  unsigned tagv;
  const bool v2 = tid < CH - 1024;
  const unsigned wlb = (unsigned)((lane >> 4) * 256 + 16 * ((lane & 15) ^ ((lane >> 4) << 2)));
  if (O_SLICE && wv < 4) {
    const int j = (w >> 3) * 4 + wv;
    if (j < 64) dma16(we, (unsigned)(j * 1024 + lane * 16), lbase + L_ST + wv * 1024);
    else if (j < 122) dma16(pos, (unsigned)((j - 64) * 1024 + lane * 16), lbase + L_ST + wv * 1024);
  }
  {
    const unsigned e0 = (unsigned)(w * CH + tid) * 4, e2 = v2 ? e0 + 4096 : e0;
    asm volatile("global_load_dword %0, %10, %14\n\tglobal_load_dword %1, %10, %14 offset:2048\n\tglobal_load_dword %2, %11, %14\n\t"
                 "global_load_dword %3, %10, %15\n\tglobal_load_dword %4, %10, %15 offset:2048\n\tglobal_load_dword %5, %11, %15\n\t"
                 "global_load_dwordx2 %6, %12, %16\n\tglobal_load_dwordx2 %7, %13, %16\n\tglobal_load_dwordx2 %8, %18, %16\n\t"
                 "global_load_dword %9, %19, %17 sc1"
                 : "=&v"(d[0]), "=&v"(d[1]), "=&v"(d[2]), "=&v"(s[0]), "=&v"(s[1]), "=&v"(s[2]), "=&v"(a[0]), "=&v"(a[1]), "=&v"(a[2]), "=&v"(tagv)
                 : "v"(e0), "v"(e2), "v"(2 * e0), "v"(2 * e0 + 4096), "s"(dst), "s"(src), "s"(eattr), "s"(ws), "v"(2 * e2), "v"(WS_EP)
                 : "memory");
    const int row = 4 * wv + (lane >> 4), c = lane & 15;
#pragma unroll
    for (int i = 0; i < 4; i++) dma16(we, (wlb ^ (16 * i)) + (32 + 4 * wv + i) * 1024, lbase + L_W + (32 + 4 * wv + i) * 1024);
    dma16(h, (unsigned)(min(n0 + row, NN - 1) * 256 + 16 * (c ^ (row & 15))), lbase + L_H + wv * 1024);
    if (wv < 2) dma16(h, (unsigned)(min(n0 + 32 + row, NN - 1) * 256 + 16 * (c ^ (row & 15))), lbase + L_H + (8 + wv) * 1024);
  }
  if (tid < 256) *(u32*)(lds + L_HIST + tid * 4) = 0u;
  else {
    const int t = tid - 256;
    if (t < 32) {
      const int j = (t >> 2) - 4 * ((t >> 1) & 1) - 2 * (t & 1);
      *(u32*)(lds + L_OH + t * 4) = j == 0 ? 0x3F80u : j == 1 ? 0x3F800000u : 0u;
      if (t < 16) *(u32*)(lds + L_GC + t * 4) = 0u;
    }
  }
  barrier();
  {
    if (wv == 3) dma16(we, 65536 + lane * 16, lbase + L_EXT);
    if (wv == 4 && lane < 16) dma16(we, 66560 + lane * 16, lbase + L_EXT + 1024);
    if (wv == 5 && lane < 16) dma16(wx, lane * 16, lbase + L_WX);
    if (wv == 6 && lane < 16) dma16(wh, (unsigned)(w * 256 + lane * 16), lbase + L_BS + 10240);
  }
  const int T = wv & 3, nb = wv >> 2;
  const int loc = 5 * (4 * nb + (r >> 3)) + (r & 7);
  const bool valid = (r & 7) < 5 && loc < nsz;
  int ha = L_H + min(loc, 39) * 256 + 16 * (hh ^ (min(loc, 39) & 15));
  const unsigned trx0 = lbase + L_W + (unsigned)((8 * hh + q4) * 256), trs0 = (unsigned)((q4 << 2) | (2 * hh));
  unsigned b0 = trx0 + 32768 + 16 * ((unsigned)(8 * (T & 1) + 4 * (p4 & 1) + 2 * blk + (T >> 1)) ^ trs0) + 8 * (p4 >> 1);
  unsigned a0 = trx0 + 16 * ((unsigned)(4 * T + 2 * blk + (p4 >> 1)) ^ trs0) + 8 * (p4 & 1);
  int bo = L_BS + loc * 256 + 16 * ((4 * T + 2 * hh) ^ (loc & 15));
  unsigned gbo = WS_BT + (unsigned)((n0 + (tid >> 4)) * 256 + 16 * ((tid ^ (tid >> 4)) & 15));
  PIN(b0); PIN(a0); PIN(bo); PIN(gbo); PIN(ha);
  u32x4 I0, I1;
  {
    const unsigned one = (r & 1) ? 0x3F800000u : 0x3F80u;
#pragma unroll
    for (int jj = 0; jj < 4; jj++) {
      const bool m = ((r & 7) >> 1) == jj;
      I0[jj] = (m && (r >> 3) == hh) ? one : 0u;
      I1[jj] = (m && (r >> 3) == 2 + hh) ? one : 0u;
    }
  }
  const int rowbuf = L_ROWS + wv * ROWBUF;
  unsigned kdma = 16 * ((lane & 15) ^ (4 * (lane >> 4)));
  const int rd0 = rowbuf + 1040 * (r & 3) + 4352 * ((r >> 2) & 1) + (r >> 3) * 256;
  int rdA = rd0 + 64 * ((2 * hh) ^ (r >> 3)), rdB = rd0 + 64 * ((2 * hh + 1) ^ (r >> 3));
  int swr0 = L_S + 32 * (r >> 4) + 16 * ((r >> 2) & 1) + 8 * ((r >> 3) & 1) + 2 * (r & 3) + 160 * hh;
  int srd0 = L_S + r * 80 + 16 * hh;
  const unsigned long long bbase = (unsigned long long)(ws + WS_BT - 3120);
  const u32x4 bsrd = {(unsigned)bbase, (unsigned)(bbase >> 32) & 0xffffu, (unsigned)((NN + 256) * 256 + 3120), 0x00020000u};
  f32x16 Y[4];
#pragma unroll
  for (int t = 0; t < 4; t++) {
    Y[t] = zero16();
    PIN(Y[t]);
  }
  PIN(I0); PIN(I1); PIN(kdma); PIN(rdA); PIN(rdB); PIN(swr0); PIN(srd0);
  asm volatile("s_waitcnt vmcnt(12)" : "+v"(d[0]), "+v"(d[1]), "+v"(d[2])::"memory");
  STAMP(1);
  int key[3];
  unsigned rank[3];
#pragma unroll
  for (int k = 0; k < 3; k++) {
    key[k] = (int)((unsigned)__umul24((unsigned)d[k], 429497u) >> 24);
    rank[k] = 0;
    if (k < 2 || v2) rank[k] = atomicAdd((unsigned*)(lds + L_HIST) + key[k], 1u);
  }
#pragma unroll
  for (int i = 0; i < 4; i++) dma16(we, (wlb ^ (16 * i)) + (4 * wv + i) * 1024, lbase + L_W + (4 * wv + i) * 1024);
  asm volatile("s_waitcnt vmcnt(9)" : "+v"(s[0]), "+v"(s[1]), "+v"(s[2]), "+v"(a[0]), "+v"(a[1]), "+v"(a[2]), "+v"(tagv)::"memory");
  const unsigned tag = (unsigned)__builtin_amdgcn_readfirstlane((int)tagv) + 1u;
  u32x4 rec[3];
#pragma unroll
  for (int k = 0; k < 3; k++) {
    const int lc = d[k] - bucket_start(key[k]);
    const int g = (lc * 205) >> 10;
    rec[k][0] = a[k][0];
    rec[k][1] = (a[k][1] & 0xffffu) | (tag << 16);
    rec[k][2] = (a[k][1] >> 16) | (tag << 16);
    rec[k][3] = ((unsigned)s[k] << 8) | (unsigned)(lc + 3 * g) | ((tag >> 16) << 22);
    PIN(rec[k]);
  }
  barrier();
  STAMP(2);
  u32x4 off4;
  unsigned oend;
  {
    const u32x4 cnt = *(const u32x4*)(lds + L_HIST + lane * 16);
    const unsigned tot = cnt[0] + cnt[1] + cnt[2] + cnt[3];
    oend = (unsigned)wave_incl_scan((int)tot);
    off4[0] = oend - tot;
    off4[1] = off4[0] + cnt[0];
    off4[2] = off4[1] + cnt[1];
    off4[3] = off4[2] + cnt[2];
    *(u32x4*)(lds + L_OFF + lane * 16) = off4;
    if (wv == 7) {
      const u32x4 g0 = {tag, off4[0] | (off4[1] << 16), tag, off4[1] | (off4[2] << 16)};
      const u32x4 g1 = {tag, off4[2] | (off4[3] << 16), tag, off4[3] | (oend << 16)};
      const unsigned off = WS_TAB + (unsigned)(w * NB + 4 * lane) * 8;
      __builtin_amdgcn_raw_buffer_store_b128(g0, wsr, off, 0, 16);
      __builtin_amdgcn_raw_buffer_store_b128(g1, wsr, off + 16, 0, 16);
    }
#pragma unroll
    for (int k = 0; k < 3; k++) rank[k] += *(const u32*)(lds + L_OFF + key[k] * 4);
#pragma unroll
    for (int k = 0; k < 3; k++)
      if (k < 2 || v2) *(u32x4*)(lds + L_ST + rank[k] * 16) = rec[k];
  }
  if (wv == 7) asm volatile("s_waitcnt vmcnt(2)" ::: "memory");
  else wait_vm0();
  barrier();
  STAMP(4);
  u32x2 at[4], ent;
  {
    u32x4 v0 = *(const u32x4*)(lds + L_ST + tid * 16);
    u32x4 v1 = *(const u32x4*)(lds + L_ST + 8192 + tid * 16);
    u32x4 v2_ = *(const u32x4*)(lds + L_ST + 16384 + (tid & 255) * 16);
    const unsigned b1 = (b0 ^ 16) + 1024, a1 = (a0 ^ 16) + 1024;
    u32x4 hf[8], wf[8];
#pragma unroll
    for (int ks = 0; ks < 8; ks++) {
      hf[ks] = *(const u32x4*)(lds + (ha ^ (32 * ks)));
      wf[ks] = tr_read2(b0 + ks * 4096, b1 + ks * 4096);
    }
    SB();
    __builtin_amdgcn_raw_buffer_store_b128(v0, wsr, WS_R + (unsigned)(w * CHS + tid) * 16, 0, 16);
    __builtin_amdgcn_raw_buffer_store_b128(v1, wsr, WS_R + (unsigned)(w * CHS + 512 + tid) * 16, 0, 16);
    if (tid < 256) __builtin_amdgcn_raw_buffer_store_b128(v2_, wsr, WS_R + (unsigned)(w * CHS + 1024 + tid) * 16, 0, 16);
    f32x16 acc = zero16();
#pragma unroll
    for (int ks = 0; ks < 8; ks++) {
      acc = mfma(wf[ks], hf[ks], acc);
      PIN(acc);
      if (!O_POS2) pos_tile(ks);
    }
    SB();
#pragma unroll
    for (int ks = 0; ks < 8; ks++) wf[ks] = tr_read2(a0 + ks * 4096, a1 + ks * 4096);
    SB();
    u32x4 bt0, bt1;
#pragma unroll
    for (int i = 0; i < 4; i++) {
      bt0[i] = pk2(acc[2 * i] * NLOG2E, acc[2 * i + 1] * NLOG2E);
      bt1[i] = pk2(acc[8 + 2 * i] * NLOG2E, acc[9 + 2 * i] * NLOG2E);
    }
    if ((r & 7) < 5) {
      *(u32x4*)(lds + bo) = bt0;
      *(u32x4*)(lds + (bo ^ 16)) = bt1;
    }
    barrier();
    STAMP(3);
    const u32x4 vb0 = *(const u32x4*)(lds + L_BS + tid * 16);
    const u32x4 vb1 = *(const u32x4*)(lds + L_BS + 8192 + min(tid, 143) * 16);
    __builtin_amdgcn_raw_buffer_store_b128(vb0, wsr, gbo, 0, 16);
    if (tid < 16 * (nsz - 32) || (tid >> 4) == 8)
      __builtin_amdgcn_raw_buffer_store_b128(vb1, wsr, tid < 128 ? gbo + 8192 : WS_BT + (unsigned)((NN + w) * 256 + (tid & 15) * 16), 0, 16);
    if (O_POS2) asm volatile("global_load_dwordx2 %0, %1, %2 sc1" : "=&v"(ent) : "v"(WS_TAB + (unsigned)((tid >> 1) * NB + b) * 8), "s"(ws) : "memory");
    asm volatile("" ::: "memory");
    acc = zero16();
#pragma unroll
    for (int ks = 0; ks < 8; ks++) {
      acc = mfma(hf[ks], wf[ks], acc);
      PIN(acc);
      if (O_POS2) pos_tile(ks);
    }
#pragma unroll
    for (int i = 0; i < 4; i++) {
      at[i][0] = pk2(acc[4 * i] * NLOG2E, acc[4 * i + 1] * NLOG2E);
      at[i][1] = pk2(acc[4 * i + 2] * NLOG2E, acc[4 * i + 3] * NLOG2E);
      PIN(at[i]);
    }
  }
  {
    const u32x4 z = {0u, 0u, 0u, 0u};
    *(u32x4*)(lds + L_S + tid * 16) = z;
    *(u32x4*)(lds + L_S + 8192 + tid * 16) = z;
    if (tid < 256) {
      *(u32x4*)(lds + L_S + 16384 + tid * 16) = z;
    } else {
      const int t = tid - 256, v = t >> 5, q = (t >> 3) & 3, i = t & 7;
      *(u32*)(lds + L_FK + t * 4) = (unsigned)(NN + (i < 4 ? 128 + 32 * q + 4 * v + i : 16 * v + 4 * (i - 4) + q)) << 8;
    }
  }
  u32x4 extB[4];
#pragma unroll
  for (int t = 0; t < 4; t++) {
    const u16* wp = (const u16*)(lds + L_EXT) + 32 * t + r;
    const float w0 = bf2f(wp[(1 + 2 * hh) * 128]) * NLOG2E, w1 = bf2f(wp[(2 + 2 * hh) * 128]) * NLOG2E, w2 = bf2f(wp[0]) * NLOG2E;
    extB[t][0] = 0u;
    extB[t][1] = 0u;
    extB[t][2] = pk2(w0, w1);
    extB[t][3] = hh ? 0u : pk2(w2, 0.f);
    PIN(extB[t]);
  }
  if (O_POS2) asm volatile("s_waitcnt vmcnt(6)" : "+v"(ent)::"memory");
  else asm volatile("global_load_dwordx2 %0, %1, %2 sc1\n\ts_waitcnt vmcnt(0)" : "=&v"(ent) : "v"(WS_TAB + (unsigned)((tid >> 1) * NB + b) * 8), "s"(ws) : "memory");
  barrier();
  STAMP(5);
  if (tid == 0) __builtin_amdgcn_raw_buffer_store_b32(tag, wsr, WS_FB + (unsigned)w * 4, 0, 16);
#pragma unroll
  for (int i = 0; i < 4; i++) *(u32x2*)(lds + L_A + (4 * nb + i) * 2048 + T * 512 + lane * 8) = at[i];

  bool wh_loaded = false, slow = false;
  int lo = 0, quota = MAPW, gq = wv;
  unsigned pin = 0, atmp = 0;
  while (true) {
    wh_loaded = false;
    int tq = tid;
    PIN(tq);
    const int lq = tq & 63;
    int kc, kfirst, kpre, wtot;
    {
      const unsigned tabq = WS_TAB + (unsigned)((tq >> 1) * NB + b) * 8;
      u32x2 gq2 = ent;
      ent[0] = tag + 1u;
#pragma clang loop unroll(disable)
      for (int spin = 0; spin < (1 << 22); spin++) {
        if (gq2[0] != tag) asm volatile("global_load_dwordx2 %0, %1, %2 sc1\n\ts_waitcnt vmcnt(0)" : "=&v"(gq2) : "v"(tabq), "s"(ws) : "memory");
        if (__all(gq2[0] == tag)) break;
        __builtin_amdgcn_s_sleep(1);
      }
      const int o0 = min((int)(gq2[1] & 0xffffu), CH), o1 = min((int)(gq2[1] >> 16), CH);
      kc = max(o1 - o0, 0);
      kfirst = (tq >> 1) * CHS + o0;
      const int inc = wave_incl_scan((tq & 1) ? 0 : kc);
      kpre = inc - kc;
      wtot = __builtin_amdgcn_readlane(inc, 63);
      if (!slow && lq == 0) atomicMax((unsigned*)(lds + L_GC + 32), (unsigned)wtot);
    }
    STAMP(7);
    u32x3 q[4];
    u32x2 pd[4], ps[4];
    int ra1[4], par[4];
    bool four;
    {
      const int map = L_MAP + wv * MAPW * 4;
      const int nw = min(wtot - lo, quota);
      const int jhi = min(kc, lo + quota - kpre);
#pragma clang loop vectorize(disable) unroll(disable)
      for (int j = max(0, lo - kpre) + ((tq ^ max(0, lo - kpre)) & 1); j < jhi; j += 2) *(u32*)(lds + map + (kpre + j - lo) * 4) = (unsigned)(kfirst + j) * 16u;
      unsigned ix[4];
#pragma unroll
      for (int k = 0; k < 4; k++) {
        const unsigned mi = *(const u32*)(lds + map + (64 * k + lq) * 4);
        ix[k] = 64 * k + lq < nw ? mi : 0u;
      }
      STAMP(23);
      u32x4 q4_[4];
      unsigned fb;
      const unsigned fbo = WS_FB + (unsigned)(32 * wv + (lq & 31)) * 4;
#pragma clang loop unroll(disable)
      for (int spin = 0; spin < (1 << 22); spin++) {
        asm volatile("global_load_dwordx4 %0, %5, %9 sc1\n\tglobal_load_dwordx4 %1, %6, %9 sc1\n\tglobal_load_dwordx4 %2, %7, %9 sc1\n\t"
                     "global_load_dwordx4 %3, %8, %9 sc1\n\tglobal_load_dword %4, %10, %11 sc1\n\ts_waitcnt vmcnt(0)"
                     : "=&v"(q4_[0]), "=&v"(q4_[1]), "=&v"(q4_[2]), "=&v"(q4_[3]), "=&v"(fb)
                     : "v"(ix[0]), "v"(ix[1]), "v"(ix[2]), "v"(ix[3]), "s"(ws + WS_R), "v"(fbo), "s"(ws) : "memory");
        bool ok = true;
#pragma unroll
        for (int k = 0; k < 4; k++) {
          const unsigned bad = (((q4_[k][1] ^ (tag << 16)) | (q4_[k][2] ^ (tag << 16))) >> 16) | ((q4_[k][3] ^ ((tag >> 16) << 22)) >> 22);
          ok = ok && (bad == 0 || 64 * k + lq >= nw);
        }
        if (__all(ok)) break;
      }
#pragma unroll
      for (int k = 0; k < 4; k++) {
        q[k][0] = q4_[k][0];
        q[k][1] = (q4_[k][1] & 0xffffu) | (q4_[k][2] << 16);
        q[k][2] = q4_[k][3] & 0x3fffffu;
      }
      STAMP(24);
      if (O_POS2) barrier();
      unsigned rk[4];
#pragma unroll
      for (int k = 0; k < 4; k++) {
        rk[k] = CAPW;
        if (64 * k + lq < nw) rk[k] = atomicAdd((unsigned*)(lds + L_GC) + ((q[k][2] >> 3) & 7u), 1u);
        const unsigned g = (q[k][2] >> 3) & 7u;
        const int dn = n0 + (int)(q[k][2] & 63u) - 3 * (int)g, sn = min((int)(q[k][2] >> 8), NN - 1);
        const int od = L_ROWS + ((6 * dn) & ~3), os = L_ROWS + ((6 * sn) & ~3);
        pd[k][0] = *(const u32*)(lds + od); pd[k][1] = *(const u32*)(lds + od + 4);
        ps[k][0] = *(const u32*)(lds + os); ps[k][1] = *(const u32*)(lds + os + 4);
        par[k] = (dn & 1) * 16;
      }
#pragma unroll
      for (int k = 0; k < 4; k++) {
        const bool ok = rk[k] < CAPW;
        const int e = (int)(((q[k][2] >> 3) & 7u) * CAPW + rk[k]);
        *(u32*)(lds + (ok ? L_R2 + e * 4 : L_DUM + 96)) = q[k][2];
        ra1[k] = ok ? L_R1 + e * 16 : L_DUM + 96;
      }
      four = nw > 192;
#pragma clang loop unroll(disable)
      for (int spin = 0; spin < (1 << 22) && !__all(fb == tag); spin++) {
        __builtin_amdgcn_s_sleep(1);
        asm volatile("global_load_dword %0, %1, %2 sc1\n\ts_waitcnt vmcnt(0)" : "=&v"(fb) : "v"(fbo), "s"(ws) : "memory");
      }
    }
    barrier();
    STAMP(8);
    const int cl = *(const u32*)(lds + L_GC + (lane & 7) * 4);
    const int maxtot = __builtin_amdgcn_readfirstlane(*(const u32*)(lds + L_GC + 32));
    if (!slow) {
      const int kk = (cl << 3) | (lane & 7);
      int rk = 0;
      rk += __builtin_amdgcn_update_dpp(0, kk, 0x121, 0xf, 0xf, false) > kk;
      rk += __builtin_amdgcn_update_dpp(0, kk, 0x122, 0xf, 0xf, false) > kk;
      rk += __builtin_amdgcn_update_dpp(0, kk, 0x123, 0xf, 0xf, false) > kk;
      rk += __builtin_amdgcn_update_dpp(0, kk, 0x124, 0xf, 0xf, false) > kk;
      rk += __builtin_amdgcn_update_dpp(0, kk, 0x125, 0xf, 0xf, false) > kk;
      rk += __builtin_amdgcn_update_dpp(0, kk, 0x126, 0xf, 0xf, false) > kk;
      rk += __builtin_amdgcn_update_dpp(0, kk, 0x127, 0xf, 0xf, false) > kk;
      const int cmax = __any(cl > CAPW) ? CAPW + 1 : 0;
      if (maxtot > MAPW || cmax > CAPW) {
        slow = true;
        quota = CAPW / 8;
        barrier();
        if (tid < 8) *(u32*)(lds + L_GC + tid * 4) = 0u;
        barrier();
        continue;
      }
      gq = __builtin_ctzll(__ballot(rk == (wv < 4 ? wv : 11 - wv)));
    }
    const int n = __builtin_amdgcn_readlane(cl, gq);
    const int swr = swr0 + gq * 2560, srd = srd0 + gq * 2560;
    const int l1 = L_R1 + gq * CAPW * 16, l2 = L_R2 + gq * CAPW * 4;
    const int nt = (n + 31) >> 5;
    u32x4 rows[8], extA, S0, S1, ra, rb, m1p = {0u, 0u, 0u, 0u};
    u32x2 r1;
    unsigned r2;
    int sp;
    f32x16 XA, XB;
    auto list_read = [&](int tl) {
      const int list = tl < nt ? l2 + tl * 128 : L_FK + wv * 128;
      ra = *(const u32x4*)(lds + list + (lane >> 4) * 32);
      rb = *(const u32x4*)(lds + list + (lane >> 4) * 32 + 16);
    };
    auto dma_piece = [&](int, int i) {
      if (i == 0) row_dma<0>(bsrd, ra[0], kdma, lbase + rowbuf, atmp);
      if (i == 1) row_dma<1>(bsrd, ra[1], kdma, lbase + rowbuf, atmp);
      if (i == 2) row_dma<2>(bsrd, ra[2], kdma, lbase + rowbuf, atmp);
      if (i == 3) row_dma<3>(bsrd, ra[3], kdma, lbase + rowbuf, atmp);
      if (i == 4) row_dma<4>(bsrd, rb[0], kdma, lbase + rowbuf, atmp);
      if (i == 5) row_dma<5>(bsrd, rb[1], kdma, lbase + rowbuf, atmp);
      if (i == 6) row_dma<6>(bsrd, rb[2], kdma, lbase + rowbuf, atmp);
      if (i == 7) row_dma<7>(bsrd, rb[3], kdma, lbase + rowbuf, atmp);
    };
    auto meta_read = [&](int tl) {
      r1 = *(const u32x2*)(lds + l1 + (tl * 32 + r) * 16 + hh * 8);
      r2 = *(const u32*)(lds + l2 + (tl * 32 + r) * 4);
    };
    auto meta_use = [&](int tl) {
      const unsigned dl = r2 & 7u;
      const u32x2 oh = *(const u32x2*)(lds + L_OH + dl * 16 + hh * 8);
      extA[0] = oh[0]; extA[1] = oh[1]; extA[2] = r1[0]; extA[3] = r1[1];
      sp = (tl * 32 + r < n) ? swr + (int)dl * 320 : L_DUM;
    };
    auto s_write = [&]() {
      *(u16*)(lds + sp) = (u16)(hh ? extA[3] : 0x3F80u);
      *(u16*)(lds + sp + 80) = (u16)(extA[3] >> 16);
    };
    auto rows_read = [&]() {
#pragma unroll
      for (int j = 0; j < 8; j++) rows[j] = *(const u32x4*)(lds + (j < 4 ? rdA : rdB) + 16 * (j & 3));
    };
    auto rows_pin = [&]() {
      asm volatile("" : "+v"(rows[0]), "+v"(rows[1]), "+v"(rows[2]), "+v"(rows[3]), "+v"(rows[4]), "+v"(rows[5]), "+v"(rows[6]), "+v"(rows[7]));
    };

    list_read(0);
    if (n > 0 && (!O_STAG || wv < 4)) {
#pragma unroll
      for (int i = 0; i < 8; i++) dma_piece(0, i);
    }
    STAMP(18);
    auto finish = [&](int k) {
      const unsigned long long ud = (((unsigned long long)pd[k][1] << 32) | pd[k][0]) >> par[k];
      const unsigned long long us = (((unsigned long long)ps[k][1] << 32) | ps[k][0]) >> ((q[k][2] >> 4) & 16u);
      const unsigned dlo = (unsigned)ud, dhi = (unsigned)(ud >> 32), slo = (unsigned)us, shi = (unsigned)(us >> 32);
      const unsigned dxy = pk2(bf2f(dlo) - bf2f(slo), bfhi(dlo) - bfhi(slo));
      const unsigned dz_ = pk2(bf2f(dhi) - bf2f(shi), 0.f);
      const float dx = bf2f(dxy), dy = bfhi(dxy), dz = bf2f(dz_);
      const unsigned sxy = pk2(dx * dx, dy * dy), sz = pk2(dz * dz, 0.f);
      const unsigned d2 = pk2((bf2f(sxy) + bfhi(sxy)) + bf2f(sz), 0.f) & 0xffffu;
      const u32x4 rec = {q[k][0], d2 | (dxy << 16), q[k][1], (dxy >> 16) | (dz_ << 16)};
      *(u32x4*)(lds + ra1[k]) = rec;
    };
    finish(0);
    finish(1);
    finish(2);
    if (four) finish(3);
    if (lane < 32 * nt - n) {
      const u32x4 z = {0u, 0u, 0u, 0u};
      *(u32x4*)(lds + l1 + (n + lane) * 16) = z;
    }
    if (w == 0 && tid == 0) __builtin_amdgcn_raw_buffer_store_b32(tag, wsr, WS_EP, 0, 16);
    pin = pos[min(3 * n0 + 15 * gq + (lq & 15), 3 * NN - 1)];
#pragma unroll
    for (int t = 0; t < 4; t++) {
      const u32x2 v = *(const u32x2*)(lds + L_A + gq * 2048 + t * 512 + lq * 8);
      extB[t][0] = v[0];
      extB[t][1] = v[1];
    }
    STAMP(19);
    barrier();
    STAMP(20);
    if (O_PRIO == 1 && wv >= 4) __builtin_amdgcn_s_setprio(1);
    if (O_PRIO == 2 && wv < 4) __builtin_amdgcn_s_setprio(1);
    if (n > 0) {
    if (O_STAG && wv >= 4) {
#pragma unroll
      for (int i = 0; i < 8; i++) dma_piece(0, i);
    }
    meta_read(0);
    list_read(1);
    meta_use(0);
    s_write();
    S1 = m1p;
    STAMP(21);
    wait_vm0();
    STAMP(22);
    PIN(pin);
    rows_read();
    rows_pin();
    XA = mfma(rows[0], I0, zero16());
    XA = mfma(rows[1], I1, XA);
    XA = mfma(extA, extB[0], XA);
    STAMP(9);

    const int nfull = O_HALF ? (n + 15) >> 5 : nt;
    int first = 1;
    for (int tl = 0; tl < nfull; tl++) {
      asm volatile("" : "+s"(first));
#pragma unroll
      for (int t = 0; t < 4; t++) {
        f32x16& Xc = (t & 1) ? XB : XA;
        f32x16& Xn = (t & 1) ? XA : XB;
        const int tn = (t + 1) & 3;
        auto gap = [&](int k) {
          SB();
          if (t == 0 && k == 0 && first) {
#pragma unroll
            for (int i = 0; i < 8; i++) dma_piece(1, i);
          }
          if (t == 0 && k == 0) S0 = *(const u32x4*)(lds + srd);
          if (t == 0 && k == 3) S1 = *(const u32x4*)(lds + srd + 32);
          if (t == 0 && k == 4) {
            *(u16*)(lds + sp) = 0;
            *(u16*)(lds + sp + 80) = 0;
          }
          if (t == 2 && k == 0) meta_read(tl + 1);
          if (t == 2 && k == 2) meta_use(tl + 1);
          if (t == 2 && k == 3) wait_vm0();
          if (t == 2 && k >= 3 && k < 7) {
            rows[2 * k - 6] = *(const u32x4*)(lds + (k < 5 ? rdA : rdB) + 16 * ((2 * k - 6) & 3));
            rows[2 * k - 5] = *(const u32x4*)(lds + (k < 5 ? rdA : rdB) + 16 * ((2 * k - 5) & 3));
          }
          if (t == 2 && k == 7) list_read(tl + 2);
          if (t == 3 && k < 8) dma_piece(tl + 2, k);
          if (t == 3 && k == 8) s_write();
          SB();
        };
        f32x2 dd[8];
        if (O_PRIO == 4 && t == 0) __builtin_amdgcn_s_setprio(1);
        if (O_PRIO == 4 && t == 2) __builtin_amdgcn_s_setprio(0);
        if (O_PRIO == 7 && t == 3 && wv >= 4) __builtin_amdgcn_s_setprio(1);
        if (O_PRIO == 7 && t == 2 && wv >= 4) __builtin_amdgcn_s_setprio(0);
        SB();
        Xn = mfma(rows[2 * tn], I0, zero16());
        PIN(Xn);
        SB();
#pragma unroll
        for (int g = 0; g < 4; g++) {
#pragma unroll
          for (int i = 2 * g; i < 2 * g + 2; i++) {
            dd[i][0] = __builtin_amdgcn_exp2f(Xc[2 * i]);
            dd[i][1] = __builtin_amdgcn_exp2f(Xc[2 * i + 1]);
            PIN(dd[i]);
          }
          SB();
          if (g == 0) { Xn = mfma(rows[2 * tn + 1], I1, Xn); PIN(Xn); }
          if (g == 1) { Xn = mfma(extA, extB[tn], Xn); PIN(Xn); }
          if (g == 2) { Y[(t + 3) & 3] = mfma(S1, m1p, Y[(t + 3) & 3]); PIN(Y[(t + 3) & 3]); }
          gap(g);
        }
#pragma unroll
        for (int i = 0; i < 8; i++) { dd[i] += 1.0f; if (i < 7) PIN(dd[i]); }
        gap(4);
#pragma unroll
        for (int g = 0; g < 4; g++) {
#pragma unroll
          for (int i = 2 * g; i < 2 * g + 2; i++) {
            dd[i][0] = __builtin_amdgcn_rcpf(dd[i][0]);
            dd[i][1] = __builtin_amdgcn_rcpf(dd[i][1]);
            PIN(dd[i]);
          }
          gap(5 + g);
        }
        u32x4 m0;
#pragma unroll
        for (int i = 0; i < 4; i++) { f32x2 x = {Xc[2 * i], Xc[2 * i + 1]}; dd[i] *= x; }
#pragma unroll
        for (int i = 0; i < 4; i++) m0[i] = __builtin_bit_cast(unsigned, __builtin_convertvector(dd[i], bf16x2));
        SB();
#pragma unroll
        for (int i = 4; i < 8; i++) { f32x2 x = {Xc[2 * i], Xc[2 * i + 1]}; dd[i] *= x; if (i < 7) PIN(dd[i]); }
        SB();
        Y[t] = mfma(S0, m0, Y[t]);
        PIN(Y[t]);
        SB();
#pragma unroll
        for (int i = 0; i < 4; i++) m1p[i] = __builtin_bit_cast(unsigned, __builtin_convertvector(dd[4 + i], bf16x2));
        PIN(m1p);
        if (t == 2) rows_pin();
      }
      first = 0;
    }
    SB();
    Y[3] = mfma(S1, m1p, Y[3]);
    if (nfull < nt) {
      S0 = *(const u32x4*)(lds + srd);
      *(u16*)(lds + sp) = 0;
      *(u16*)(lds + sp + 80) = 0;
#pragma unroll
      for (int t = 0; t < 4; t++) {
        f32x16& Xc = (t & 1) ? XB : XA;
        f32x16& Xn = (t & 1) ? XA : XB;
        f32x2 dd[4];
        if (t < 2 && nfull == 0) {
#pragma unroll
          for (int i = 0; i < 4; i++) dma_piece(nt, 4 * t + i);
        }
        SB();
        if (t < 3) { Xn = mfma(rows[2 * t + 2], I0, zero16()); PIN(Xn); }
        SB();
#pragma unroll
        for (int i = 0; i < 2; i++) { dd[i][0] = __builtin_amdgcn_exp2f(Xc[2 * i]); dd[i][1] = __builtin_amdgcn_exp2f(Xc[2 * i + 1]); }
        SB();
        if (t < 3) { Xn = mfma(rows[2 * t + 3], I1, Xn); PIN(Xn); }
        SB();
#pragma unroll
        for (int i = 2; i < 4; i++) { dd[i][0] = __builtin_amdgcn_exp2f(Xc[2 * i]); dd[i][1] = __builtin_amdgcn_exp2f(Xc[2 * i + 1]); }
        SB();
        if (t < 3) { Xn = mfma(extA, extB[t + 1], Xn); PIN(Xn); }
        SB();
#pragma unroll
        for (int i = 0; i < 4; i++) dd[i] += 1.0f;
        SB();
#pragma unroll
        for (int i = 0; i < 4; i++) { dd[i][0] = __builtin_amdgcn_rcpf(dd[i][0]); dd[i][1] = __builtin_amdgcn_rcpf(dd[i][1]); }
        SB();
        u32x4 m0;
#pragma unroll
        for (int i = 0; i < 4; i++) { f32x2 x = {Xc[2 * i], Xc[2 * i + 1]}; dd[i] *= x; }
#pragma unroll
        for (int i = 0; i < 4; i++) m0[i] = __builtin_bit_cast(unsigned, __builtin_convertvector(dd[i], bf16x2));
        SB();
        Y[t] = mfma(S0, m0, Y[t]);
        if (t < 3) PIN(Xn);
      }
    }
    wh_loaded = true;
    wait_vm0();
    }
    if (!slow) break;
    lo += quota;
    if (lo >= maxtot) break;
    barrier();
    if (tid < 8) *(u32*)(lds + L_GC + tid * 4) = 0u;
#pragma unroll
    for (int i = 0; i < 8; i++) pos_tile(i);
    wait_vm0();
    barrier();
  }
  STAMP(10);
  if (!wh_loaded) {
    wait_vm0();
    PIN(pin);
    const u32x4 fa = *(const u32x4*)(lds + L_FK + wv * 128 + (lane >> 4) * 32), fb = *(const u32x4*)(lds + L_FK + wv * 128 + (lane >> 4) * 32 + 16);
    row_dma<0>(bsrd, fa[0], kdma, lbase + rowbuf, atmp); row_dma<1>(bsrd, fa[1], kdma, lbase + rowbuf, atmp);
    row_dma<2>(bsrd, fa[2], kdma, lbase + rowbuf, atmp); row_dma<3>(bsrd, fa[3], kdma, lbase + rowbuf, atmp);
    row_dma<4>(bsrd, fb[0], kdma, lbase + rowbuf, atmp); row_dma<5>(bsrd, fb[1], kdma, lbase + rowbuf, atmp);
    row_dma<6>(bsrd, fb[2], kdma, lbase + rowbuf, atmp); row_dma<7>(bsrd, fb[3], kdma, lbase + rowbuf, atmp);
    wait_vm0();
  }

#pragma unroll
  for (int i = 0; i < 3; i++) {
    if (i < 2 || hh == 0) {
      const u32x2 mv = {pk2(Y[0][4 * i] * NLN2, Y[1][4 * i] * NLN2), pk2(Y[2][4 * i] * NLN2, Y[3][4 * i] * NLN2)};
      *(u32x2*)(lds + L_M + (5 * gq + 2 * i + hh) * MSTR + 8 * r) = mv;
    }
  }
  u32x4 hq[8], res[2];
  unsigned wxr[4];
#pragma unroll
  for (int ks = 0; ks < 8; ks++) hq[ks] = *(const u32x4*)(lds + (ha ^ (32 * ks)));
  {
    const int ra_ = (ha ^ (16 * hh)) ^ (64 * T + 32 * hh);
    res[0] = *(const u32x4*)(lds + ra_);
    res[1] = *(const u32x4*)(lds + (ra_ ^ 16));
  }
#pragma unroll
  for (int t = 0; t < 4; t++) wxr[t] = ((const u16*)(lds + L_WX))[32 * t + r];
  unsigned wa = lbase + L_ROWS + 2080 * hh + 256 * q4 + 64 * (T ^ q4) + 32 * (p4 & 1) + 16 * blk + 8 * (p4 >> 1);
  int ma = L_M + min(loc, 39) * MSTR + 16 * hh;
  PIN(wa);
  PIN(ma);
  STAMP(13);
  barrier();
  STAMP(11);
  {
    u32x4 w1[8], w2[8], mq[8];
#pragma unroll
    for (int ks = 0; ks < 8; ks++) w1[ks] = tr_read2(wa + ks * ROWBUF + 4352, wa + ks * ROWBUF + 4352 + 1040);
#pragma unroll
    for (int ks = 0; ks < 8; ks++) {
      w2[ks] = tr_read2(wa + ks * ROWBUF, wa + ks * ROWBUF + 1040);
      mq[ks] = *(const u32x4*)(lds + ma + 32 * ks);
    }
    SB();
    f32x16 P = zero16();
#pragma unroll
    for (int ks = 0; ks < 8; ks++) P = mfma(w1[ks], hq[ks], P);
#pragma unroll
    for (int ks = 0; ks < 8; ks++) P = mfma(w2[ks], mq[ks], P);
    PIN(P);
    SB();
    float zz[9];
    {
      float wxv[4];
#pragma unroll
      for (int t = 0; t < 4; t++) wxv[t] = bf2f(wxr[t]) * NLN2;
#pragma unroll
      for (int i = 0; i < 9; i++) {
        const int j = 4 * (i / 3) + 1 + i % 3;
        zz[i] = Y[0][j] * wxv[0];
        PIN(zz[i]);
        zz[i] = __builtin_fmaf(Y[1][j], wxv[1], zz[i]);
        PIN(zz[i]);
        zz[i] = __builtin_fmaf(Y[2][j], wxv[2], zz[i]);
        PIN(zz[i]);
        zz[i] = __builtin_fmaf(Y[3][j], wxv[3], zz[i]);
        PIN(zz[i]);
      }
    }
    SB();
    f32x2 x[8], e[8];
#pragma unroll
    for (int i = 0; i < 8; i++) {
      x[i][0] = P[2 * i];
      x[i][1] = P[2 * i + 1];
      e[i] = x[i] * NLOG2E;
    }
    SB();
#pragma unroll
    for (int i = 0; i < 8; i++) { e[i][0] = __builtin_amdgcn_exp2f(e[i][0]); e[i][1] = __builtin_amdgcn_exp2f(e[i][1]); }
    SB();
#pragma unroll
    for (int i = 0; i < 8; i++) e[i] += 1.0f;
    SB();
#pragma unroll
    for (int i = 0; i < 8; i++) { e[i][0] = __builtin_amdgcn_rcpf(e[i][0]); e[i][1] = __builtin_amdgcn_rcpf(e[i][1]); }
    SB();
    u32x4 ov[2];
#pragma unroll
    for (int i = 0; i < 8; i++) {
      const unsigned rs = res[i >> 2][i & 3];
      const f32x2 rv = {bf2f(rs), bfhi(rs)};
      const f32x2 o = __builtin_elementwise_fma(x[i], e[i], rv);
      ov[i >> 2][i & 3] = pk2(o[0], o[1]);
    }
    if (valid) {
      u32x4* op = (u32x4*)((char*)out + (unsigned)(n0 + loc) * 256 + 64 * T + 32 * hh);
      op[0] = ov[0];
      op[1] = ov[1];
    }
    STAMP(17);
#define DPP9(ctl)                                                                                                         \
  "v_add_f32_dpp %0, %0, %0 " ctl "\n\tv_add_f32_dpp %1, %1, %1 " ctl "\n\tv_add_f32_dpp %2, %2, %2 " ctl "\n\t"          \
  "v_add_f32_dpp %3, %3, %3 " ctl "\n\tv_add_f32_dpp %4, %4, %4 " ctl "\n\tv_add_f32_dpp %5, %5, %5 " ctl "\n\t"          \
  "v_add_f32_dpp %6, %6, %6 " ctl "\n\tv_add_f32_dpp %7, %7, %7 " ctl "\n\tv_add_f32_dpp %8, %8, %8 " ctl "\n\t"
    asm volatile("s_nop 1\n\t" DPP9("row_shr:1 row_mask:0xf bank_mask:0xf bound_ctrl:1") DPP9("row_shr:2 row_mask:0xf bank_mask:0xf bound_ctrl:1")
                 DPP9("row_shr:4 row_mask:0xf bank_mask:0xf bound_ctrl:1") DPP9("row_shr:8 row_mask:0xf bank_mask:0xf bound_ctrl:1")
                 DPP9("row_bcast:15 row_mask:0xa bank_mask:0xf") "s_nop 0"
                 : "+v"(zz[0]), "+v"(zz[1]), "+v"(zz[2]), "+v"(zz[3]), "+v"(zz[4]), "+v"(zz[5]), "+v"(zz[6]), "+v"(zz[7]), "+v"(zz[8]));
    const int zp = L_S + gq * 2560;
    if (r == 31) {
#pragma unroll
      for (int i = 0; i < 9; i++) *(u32*)(lds + zp + 12 * hh + 24 * (i / 3) + 4 * (i % 3)) = __float_as_uint(zz[i]);
    }
    asm volatile("" ::: "memory");
    const float z = __uint_as_float(*(const u32*)(lds + zp + 4 * (lane & 15)));
    if (lane < 15 && 15 * gq + lane < 3 * nsz) out[(size_t)NN * 128 + (unsigned)(3 * n0 + 15 * gq + lane)] = (u16)pk2(bf2f(pin) + z, 0.f);
  }
  STAMP(12);
  STAMP_OUT();
}

extern "C" void kernel_launch(void* const* d_in, const int* in_sizes, int n_in, void* d_out, int out_size, void* d_ws,
                              size_t ws_size, hipStream_t stream) {
  egnn_fused<<<NB, 512, 0, stream>>>((const u16*)d_in[0], (const u16*)d_in[1], (const u32x2*)d_in[2], (const int*)d_in[3],
                                     (const int*)d_in[4], (const u16*)d_in[5], (const u16*)d_in[6], (const u16*)d_in[7],
                                     (char*)d_ws, (u16*)d_out);
}
```

```cpp
#include <hip/hip_runtime.h>
#include <hip/hip_bf16.h>
#include <stdint.h>

typedef __bf16 bf16x8 __attribute__((ext_vector_type(8)));
typedef __bf16 bf16x2 __attribute__((ext_vector_type(2)));
typedef short s16x4 __attribute__((ext_vector_type(4)));
typedef float f32x2 __attribute__((ext_vector_type(2)));
typedef float f32x4 __attribute__((ext_vector_type(4), may_alias));
typedef float f32x16 __attribute__((ext_vector_type(16)));
typedef unsigned u32x2 __attribute__((ext_vector_type(2), may_alias));
typedef unsigned u32x3 __attribute__((ext_vector_type(3)));
typedef unsigned u32x4 __attribute__((ext_vector_type(4), may_alias));
typedef unsigned u32x2a4 __attribute__((ext_vector_type(2), aligned(4), may_alias));
typedef unsigned short u16 __attribute__((may_alias));
typedef unsigned u32 __attribute__((may_alias));
#define LDSP(T) __attribute__((address_space(3))) T*
#define DI __device__ __forceinline__
#define SB() __builtin_amdgcn_sched_barrier(0)
#define PIN(x) asm volatile("" : "+v"(x))

#define O_PRE 0
#define O_STAG 1
#define O_HALF 1
#define O_SLICE 1
#define O_POS2 0
#define O_OUTAUX 2
#define O_PRIO 7
constexpr int NN = 10000;
constexpr int CH = 1250, CHS = 1280;
constexpr int NB = 256;
constexpr float NLOG2E = -1.4426950408889634f;
constexpr float NLN2 = -0.6931471805599453f;

constexpr unsigned WS_BT = 4096;
constexpr unsigned WS_R = WS_BT + (NN + 256) * 256;
constexpr unsigned WS_TAB = WS_R + NB * CHS * 16;
constexpr unsigned WS_FB = WS_TAB + NB * NB * 8;
constexpr unsigned WS_EP = WS_FB + NB * 4;
constexpr unsigned WS_END = WS_EP + 256;

constexpr int CAPW = 224;
constexpr int ROWBUF = 8496;
constexpr int L_ROWS = 0;
constexpr int L_R1 = 8 * ROWBUF;
constexpr int L_R2 = L_R1 + 8 * CAPW * 16;
constexpr int L_S = L_R2 + 8 * CAPW * 4;
constexpr int L_M = L_S + 8 * 2560;
constexpr int MSTR = 272;
constexpr int L_H = 135168;
constexpr int L_A = L_H + 10240;
constexpr int L_HIST = L_A + 8 * 2048;
constexpr int L_OH = L_HIST + 1024;
constexpr int L_GC = L_OH + 128;
constexpr int L_DUM = L_GC + 64;
constexpr int L_WX = L_DUM + 128;
constexpr int L_END = L_WX + 256;
constexpr int L_W = L_R1;
constexpr int L_EXT = L_W + 65536;
constexpr int L_ST = 47104;
constexpr int L_BS = L_A;
constexpr int L_OFF = L_A + 10496;
constexpr int MAPW = 256;
constexpr int L_MAP = L_M;
constexpr int L_FK = L_HIST;
static_assert(L_ST + 20480 <= L_R1 && L_BS % 256 == 0 && L_W % 32 == 0 && L_W + 65536 <= L_EXT && L_EXT + 1280 <= L_H && L_H % 256 == 0 && L_M + 40 * MSTR <= L_H && L_END <= 163840, "lds map");

DI constexpr int piece_off(int i) { return 1040 * (i & 3) + 4352 * (i >> 2); }
DI float bf2f(unsigned u) { return __uint_as_float(u << 16); }
DI float bfhi(unsigned u) { return __uint_as_float(u & 0xffff0000u); }
DI unsigned pk2(float a, float b) {
  f32x2 v = {a, b};
  return __builtin_bit_cast(unsigned, __builtin_convertvector(v, bf16x2));
}
DI f32x16 mfma(u32x4 a, u32x4 b, f32x16 c) {
  return __builtin_amdgcn_mfma_f32_32x32x16_bf16(__builtin_bit_cast(bf16x8, a), __builtin_bit_cast(bf16x8, b), c, 0, 0, 0);
}
DI f32x16 zero16() {
  f32x16 z;
#pragma unroll
  for (int i = 0; i < 16; i++) z[i] = 0.f;
  return z;
}
DI u32x4 tr_read2(unsigned a0, unsigned a1) {
  const u32x2 lo = __builtin_bit_cast(u32x2, __builtin_amdgcn_ds_read_tr16_b64_v4i16((LDSP(s16x4))(size_t)a0));
  const u32x2 hi = __builtin_bit_cast(u32x2, __builtin_amdgcn_ds_read_tr16_b64_v4i16((LDSP(s16x4))(size_t)a1));
  const u32x4 v = {lo[0], lo[1], hi[0], hi[1]};
  return v;
}
DI int bucket_start(int b) { return (b * 625 + 15) >> 4; }
DI int wave_incl_scan(int x) {
  x += __builtin_amdgcn_update_dpp(0, x, 0x111, 0xf, 0xf, false);
  x += __builtin_amdgcn_update_dpp(0, x, 0x112, 0xf, 0xf, false);
  x += __builtin_amdgcn_update_dpp(0, x, 0x114, 0xf, 0xf, false);
  x += __builtin_amdgcn_update_dpp(0, x, 0x118, 0xf, 0xf, false);
  x += __builtin_amdgcn_update_dpp(0, x, 0x142, 0xa, 0xf, false);
  x += __builtin_amdgcn_update_dpp(0, x, 0x143, 0xc, 0xf, false);
  return x;
}
DI void dma16(const void* sbase, unsigned voff, unsigned lds_base) {
  asm volatile("s_mov_b32 m0, %2\n\ts_nop 0\n\tglobal_load_lds_dwordx4 %0, %1" ::"v"(voff), "s"(sbase), "s"(lds_base) : "memory");
}
DI void dma16_dev(const void* sbase, unsigned voff, unsigned lds_base) {
  asm volatile("s_mov_b32 m0, %2\n\ts_nop 0\n\tglobal_load_lds_dwordx4 %0, %1 sc1" ::"v"(voff), "s"(sbase), "s"(lds_base) : "memory");
}
template <int I>
DI void row_dma(u32x4 srd, unsigned key, unsigned lanec, unsigned lds_base, unsigned& t) {
  if (I & 3)
    asm volatile("v_and_or_b32 %0, %1, %2, %3\n\tbuffer_load_dwordx4 %0, %4, %5 offen offset:%6 sc1 lds"
                 : "+v"(t) : "v"(key), "s"(0xffffff00u), "v"(lanec), "s"(srd), "s"(3120 - 1040 * (I & 3)), "i"(1040 * (I & 3)) : "memory");
  else
    asm volatile("s_mov_b32 m0, %6\n\tv_and_or_b32 %0, %1, %2, %3\n\tbuffer_load_dwordx4 %0, %4, %5 offen sc1 lds"
                 : "+v"(t) : "v"(key), "s"(0xffffff00u), "v"(lanec), "s"(srd), "s"(3120), "s"(lds_base + 4352 * (I >> 2)) : "memory");
}
DI void dma4_dev(const void* sbase, unsigned voff, unsigned lds_base) {
  asm volatile("s_mov_b32 m0, %2\n\ts_nop 0\n\tglobal_load_lds_dword %0, %1 sc1" ::"v"(voff), "s"(sbase), "s"(lds_base) : "memory");
}
DI void wait_vm0() { asm volatile("s_waitcnt vmcnt(0)" ::: "memory"); }
DI void barrier() { asm volatile("s_waitcnt lgkmcnt(0)\n\ts_barrier" ::: "memory"); }

#define STAMP(i)
#define STAMP_OUT()

__global__ __launch_bounds__(512, 2) void egnn_fused(const u16* __restrict__ h, const u16* __restrict__ pos,
                                                     const u32x2* __restrict__ eattr, const int* __restrict__ src,
                                                     const int* __restrict__ dst, const u16* __restrict__ we,
                                                     const u16* __restrict__ wx, const u16* __restrict__ wh,
                                                     char* ws, u16* __restrict__ out) {
  __shared__ __attribute__((aligned(256))) char lds[L_END];
  const unsigned lbase = (unsigned)(size_t)lds;
  const int tid = threadIdx.x, lane = tid & 63;
  const int wv = __builtin_amdgcn_readfirstlane(tid >> 6);
  const int w = blockIdx.x;
  const int b = (w & 7) * 32 + (w >> 3);
  const int n0 = bucket_start(b), nsz = bucket_start(b + 1) - n0;
  const int r = lane & 31, hh = lane >> 5;
  const int blk = (lane >> 4) & 1, q4 = (lane & 15) >> 2, p4 = lane & 3;
  const __amdgpu_buffer_rsrc_t wsr = __builtin_amdgcn_make_buffer_rsrc(ws, 0, WS_END, 0x00020000);
  STAMP(0);

  auto pos_tile = [&](int i) {
    const int j = i < 5 ? 8 * i + wv : i == 5 ? 46 + wv : i == 6 ? 40 + wv : 54 + wv;
    if (i < 6 || (i == 6 && wv < 6) || (i == 7 && (wv < 4 || (wv == 4 && lane < 38))))
      dma16(pos, (unsigned)(j * 1024 + lane * 16), lbase + L_ROWS + j * 1024);
  };

  int d[3], s[3];
  u32x2 a[3];
  unsigned tagv;
  const bool v2 = tid < CH - 1024;
  const unsigned wlb = (unsigned)((lane >> 4) * 256 + 16 * ((lane & 15) ^ ((lane >> 4) << 2)));
  if (O_SLICE && wv < 4) {
    const int j = (w >> 3) * 4 + wv;
    if (j < 64) dma16(we, (unsigned)(j * 1024 + lane * 16), lbase + L_ST + wv * 1024);
    else if (j < 122) dma16(pos, (unsigned)((j - 64) * 1024 + lane * 16), lbase + L_ST + wv * 1024);
  }
  {
    const unsigned e0 = (unsigned)(w * CH + tid) * 4, e2 = v2 ? e0 + 4096 : e0;
    asm volatile("global_load_dword %0, %10, %14\n\tglobal_load_dword %1, %10, %14 offset:2048\n\tglobal_load_dword %2, %11, %14\n\t"
                 "global_load_dword %3, %10, %15\n\tglobal_load_dword %4, %10, %15 offset:2048\n\tglobal_load_dword %5, %11, %15\n\t"
                 "global_load_dwordx2 %6, %12, %16\n\tglobal_load_dwordx2 %7, %13, %16\n\tglobal_load_dwordx2 %8, %18, %16\n\t"
                 "global_load_dword %9, %19, %17 sc1"
                 : "=&v"(d[0]), "=&v"(d[1]), "=&v"(d[2]), "=&v"(s[0]), "=&v"(s[1]), "=&v"(s[2]), "=&v"(a[0]), "=&v"(a[1]), "=&v"(a[2]), "=&v"(tagv)
                 : "v"(e0), "v"(e2), "v"(2 * e0), "v"(2 * e0 + 4096), "s"(dst), "s"(src), "s"(eattr), "s"(ws), "v"(2 * e2), "v"(WS_EP)
                 : "memory");
    const int row = 4 * wv + (lane >> 4), c = lane & 15;
#pragma unroll
    for (int i = 0; i < 4; i++) dma16(we, (wlb ^ (16 * i)) + (32 + 4 * wv + i) * 1024, lbase + L_W + (32 + 4 * wv + i) * 1024);
    dma16(h, (unsigned)(min(n0 + row, NN - 1) * 256 + 16 * (c ^ (row & 15))), lbase + L_H + wv * 1024);
    if (wv < 2) dma16(h, (unsigned)(min(n0 + 32 + row, NN - 1) * 256 + 16 * (c ^ (row & 15))), lbase + L_H + (8 + wv) * 1024);
  }
  if (tid < 256) *(u32*)(lds + L_HIST + tid * 4) = 0u;
  else {
    const int t = tid - 256;
    if (t < 32) {
      const int j = (t >> 2) - 4 * ((t >> 1) & 1) - 2 * (t & 1);
      *(u32*)(lds + L_OH + t * 4) = j == 0 ? 0x3F80u : j == 1 ? 0x3F800000u : 0u;
      if (t < 16) *(u32*)(lds + L_GC + t * 4) = 0u;
    }
  }
  barrier();
  {
    if (wv == 3) dma16(we, 65536 + lane * 16, lbase + L_EXT);
    if (wv == 4 && lane < 16) dma16(we, 66560 + lane * 16, lbase + L_EXT + 1024);
    if (wv == 5 && lane < 16) dma16(wx, lane * 16, lbase + L_WX);
    if (wv == 6 && lane < 16) dma16(wh, (unsigned)(w * 256 + lane * 16), lbase + L_BS + 10240);
  }
  const int T = wv & 3, nb = wv >> 2;
  const int loc = 5 * (4 * nb + (r >> 3)) + (r & 7);
  const bool valid = (r & 7) < 5 && loc < nsz;
  int ha = L_H + min(loc, 39) * 256 + 16 * (hh ^ (min(loc, 39) & 15));
  const unsigned trx0 = lbase + L_W + (unsigned)((8 * hh + q4) * 256), trs0 = (unsigned)((q4 << 2) | (2 * hh));
  unsigned b0 = trx0 + 32768 + 16 * ((unsigned)(8 * (T & 1) + 4 * (p4 & 1) + 2 * blk + (T >> 1)) ^ trs0) + 8 * (p4 >> 1);
  unsigned a0 = trx0 + 16 * ((unsigned)(4 * T + 2 * blk + (p4 >> 1)) ^ trs0) + 8 * (p4 & 1);
  int bo = L_BS + loc * 256 + 16 * ((4 * T + 2 * hh) ^ (loc & 15));
  unsigned gbo = WS_BT + (unsigned)((n0 + (tid >> 4)) * 256 + 16 * ((tid ^ (tid >> 4)) & 15));
  PIN(b0); PIN(a0); PIN(bo); PIN(gbo); PIN(ha);
  u32x4 I0, I1;
  {
    const unsigned one = (r & 1) ? 0x3F800000u : 0x3F80u;
#pragma unroll
    for (int jj = 0; jj < 4; jj++) {
      const bool m = ((r & 7) >> 1) == jj;
      I0[jj] = (m && (r >> 3) == hh) ? one : 0u;
      I1[jj] = (m && (r >> 3) == 2 + hh) ? one : 0u;
    }
  }
  const int rowbuf = L_ROWS + wv * ROWBUF;
  unsigned kdma = 16 * ((lane & 15) ^ (4 * (lane >> 4)));
  const int rd0 = rowbuf + 1040 * (r & 3) + 4352 * ((r >> 2) & 1) + (r >> 3) * 256;
  int rdA = rd0 + 64 * ((2 * hh) ^ (r >> 3)), rdB = rd0 + 64 * ((2 * hh + 1) ^ (r >> 3));
  int swr0 = L_S + 32 * (r >> 4) + 16 * ((r >> 2) & 1) + 8 * ((r >> 3) & 1) + 2 * (r & 3) + 160 * hh;
  int srd0 = L_S + r * 80 + 16 * hh;
  const unsigned long long bbase = (unsigned long long)(ws + WS_BT - 3120);
  const u32x4 bsrd = {(unsigned)bbase, (unsigned)(bbase >> 32) & 0xffffu, (unsigned)((NN + 256) * 256 + 3120), 0x00020000u};
  f32x16 Y[4];
#pragma unroll
  for (int t = 0; t < 4; t++) {
    Y[t] = zero16();
    PIN(Y[t]);
  }
  PIN(I0); PIN(I1); PIN(kdma); PIN(rdA); PIN(rdB); PIN(swr0); PIN(srd0);
  asm volatile("s_waitcnt vmcnt(12)" : "+v"(d[0]), "+v"(d[1]), "+v"(d[2])::"memory");
  STAMP(1);
  int key[3];
  unsigned rank[3];
#pragma unroll
  for (int k = 0; k < 3; k++) {
    key[k] = (int)((unsigned)__umul24((unsigned)d[k], 429497u) >> 24);
    rank[k] = 0;
    if (k < 2 || v2) rank[k] = atomicAdd((unsigned*)(lds + L_HIST) + key[k], 1u);
  }
#pragma unroll
  for (int i = 0; i < 4; i++) dma16(we, (wlb ^ (16 * i)) + (4 * wv + i) * 1024, lbase + L_W + (4 * wv + i) * 1024);
  asm volatile("s_waitcnt vmcnt(9)" : "+v"(s[0]), "+v"(s[1]), "+v"(s[2]), "+v"(a[0]), "+v"(a[1]), "+v"(a[2]), "+v"(tagv)::"memory");
  const unsigned tag = (unsigned)__builtin_amdgcn_readfirstlane((int)tagv) + 1u;
  u32x4 rec[3];
#pragma unroll
  for (int k = 0; k < 3; k++) {
    const int lc = d[k] - bucket_start(key[k]);
    const int g = (lc * 205) >> 10;
    rec[k][0] = a[k][0];
    rec[k][1] = (a[k][1] & 0xffffu) | (tag << 16);
    rec[k][2] = (a[k][1] >> 16) | (tag << 16);
    rec[k][3] = ((unsigned)s[k] << 8) | (unsigned)(lc + 3 * g) | ((tag >> 16) << 22);
    PIN(rec[k]);
  }
  barrier();
  STAMP(2);
  u32x4 off4;
  unsigned oend;
  {
    const u32x4 cnt = *(const u32x4*)(lds + L_HIST + lane * 16);
    const unsigned tot = cnt[0] + cnt[1] + cnt[2] + cnt[3];
    oend = (unsigned)wave_incl_scan((int)tot);
    off4[0] = oend - tot;
    off4[1] = off4[0] + cnt[0];
    off4[2] = off4[1] + cnt[1];
    off4[3] = off4[2] + cnt[2];
    *(u32x4*)(lds + L_OFF + lane * 16) = off4;
    if (wv == 7) {
      const u32x4 g0 = {tag, off4[0] | (off4[1] << 16), tag, off4[1] | (off4[2] << 16)};
      const u32x4 g1 = {tag, off4[2] | (off4[3] << 16), tag, off4[3] | (oend << 16)};
      const unsigned off = WS_TAB + (unsigned)(w * NB + 4 * lane) * 8;
      __builtin_amdgcn_raw_buffer_store_b128(g0, wsr, off, 0, 16);
      __builtin_amdgcn_raw_buffer_store_b128(g1, wsr, off + 16, 0, 16);
    }
#pragma unroll
    for (int k = 0; k < 3; k++) rank[k] += *(const u32*)(lds + L_OFF + key[k] * 4);
#pragma unroll
    for (int k = 0; k < 3; k++)
      if (k < 2 || v2) *(u32x4*)(lds + L_ST + rank[k] * 16) = rec[k];
  }
  if (wv == 7) asm volatile("s_waitcnt vmcnt(2)" ::: "memory");
  else wait_vm0();
  barrier();
  STAMP(4);
  u32x2 at[4], ent;
  {
    u32x4 v0 = *(const u32x4*)(lds + L_ST + tid * 16);
    u32x4 v1 = *(const u32x4*)(lds + L_ST + 8192 + tid * 16);
    u32x4 v2_ = *(const u32x4*)(lds + L_ST + 16384 + (tid & 255) * 16);
    const unsigned b1 = (b0 ^ 16) + 1024, a1 = (a0 ^ 16) + 1024;
    u32x4 hf[8], wf[8];
#pragma unroll
    for (int ks = 0; ks < 8; ks++) {
      hf[ks] = *(const u32x4*)(lds + (ha ^ (32 * ks)));
      wf[ks] = tr_read2(b0 + ks * 4096, b1 + ks * 4096);
    }
    SB();
    __builtin_amdgcn_raw_buffer_store_b128(v0, wsr, WS_R + (unsigned)(w * CHS + tid) * 16, 0, 16);
    __builtin_amdgcn_raw_buffer_store_b128(v1, wsr, WS_R + (unsigned)(w * CHS + 512 + tid) * 16, 0, 16);
    if (tid < 256) __builtin_amdgcn_raw_buffer_store_b128(v2_, wsr, WS_R + (unsigned)(w * CHS + 1024 + tid) * 16, 0, 16);
    f32x16 acc = zero16();
#pragma unroll
    for (int ks = 0; ks < 8; ks++) {
      acc = mfma(wf[ks], hf[ks], acc);
      PIN(acc);
      if (!O_POS2) pos_tile(ks);
    }
    SB();
#pragma unroll
    for (int ks = 0; ks < 8; ks++) wf[ks] = tr_read2(a0 + ks * 4096, a1 + ks * 4096);
    SB();
    u32x4 bt0, bt1;
#pragma unroll
    for (int i = 0; i < 4; i++) {
      bt0[i] = pk2(acc[2 * i] * NLOG2E, acc[2 * i + 1] * NLOG2E);
      bt1[i] = pk2(acc[8 + 2 * i] * NLOG2E, acc[9 + 2 * i] * NLOG2E);
    }
    if ((r & 7) < 5) {
      *(u32x4*)(lds + bo) = bt0;
      *(u32x4*)(lds + (bo ^ 16)) = bt1;
    }
    barrier();
    STAMP(3);
    const u32x4 vb0 = *(const u32x4*)(lds + L_BS + tid * 16);
    const u32x4 vb1 = *(const u32x4*)(lds + L_BS + 8192 + min(tid, 143) * 16);
    __builtin_amdgcn_raw_buffer_store_b128(vb0, wsr, gbo, 0, 16);
    if (tid < 16 * (nsz - 32) || (tid >> 4) == 8)
      __builtin_amdgcn_raw_buffer_store_b128(vb1, wsr, tid < 128 ? gbo + 8192 : WS_BT + (unsigned)((NN + w) * 256 + (tid & 15) * 16), 0, 16);
    if (O_POS2) asm volatile("global_load_dwordx2 %0, %1, %2 sc1" : "=&v"(ent) : "v"(WS_TAB + (unsigned)((tid >> 1) * NB + b) * 8), "s"(ws) : "memory");
    asm volatile("" ::: "memory");
    acc = zero16();
#pragma unroll
    for (int ks = 0; ks < 8; ks++) {
      acc = mfma(hf[ks], wf[ks], acc);
      PIN(acc);
      if (O_POS2) pos_tile(ks);
    }
#pragma unroll
    for (int i = 0; i < 4; i++) {
      at[i][0] = pk2(acc[4 * i] * NLOG2E, acc[4 * i + 1] * NLOG2E);
      at[i][1] = pk2(acc[4 * i + 2] * NLOG2E, acc[4 * i + 3] * NLOG2E);
      PIN(at[i]);
    }
  }
  {
    const u32x4 z = {0u, 0u, 0u, 0u};
    *(u32x4*)(lds + L_S + tid * 16) = z;
    *(u32x4*)(lds + L_S + 8192 + tid * 16) = z;
    if (tid < 256) {
      *(u32x4*)(lds + L_S + 16384 + tid * 16) = z;
    } else {
      const int t = tid - 256, v = t >> 5, q = (t >> 3) & 3, i = t & 7;
      *(u32*)(lds + L_FK + t * 4) = (unsigned)(NN + (i < 4 ? 128 + 32 * q + 4 * v + i : 16 * v + 4 * (i - 4) + q)) << 8;
    }
  }
  u32x4 extB[4];
#pragma unroll
  for (int t = 0; t < 4; t++) {
    const u16* wp = (const u16*)(lds + L_EXT) + 32 * t + r;
    const float w0 = bf2f(wp[(1 + 2 * hh) * 128]) * NLOG2E, w1 = bf2f(wp[(2 + 2 * hh) * 128]) * NLOG2E, w2 = bf2f(wp[0]) * NLOG2E;
    extB[t][0] = 0u;
    extB[t][1] = 0u;
    extB[t][2] = pk2(w0, w1);
    extB[t][3] = hh ? 0u : pk2(w2, 0.f);
    PIN(extB[t]);
  }
  if (O_POS2) asm volatile("s_waitcnt vmcnt(6)" : "+v"(ent)::"memory");
  else asm volatile("global_load_dwordx2 %0, %1, %2 sc1\n\ts_waitcnt vmcnt(0)" : "=&v"(ent) : "v"(WS_TAB + (unsigned)((tid >> 1) * NB + b) * 8), "s"(ws) : "memory");
  barrier();
  STAMP(5);
  if (tid == 0) __builtin_amdgcn_raw_buffer_store_b32(tag, wsr, WS_FB + (unsigned)w * 4, 0, 16);
#pragma unroll
  for (int i = 0; i < 4; i++) *(u32x2*)(lds + L_A + (4 * nb + i) * 2048 + T * 512 + lane * 8) = at[i];

  bool wh_loaded = false, slow = false;
  int lo = 0, quota = MAPW, gq = wv;
  unsigned pin = 0, atmp = 0;
  while (true) {
    wh_loaded = false;
    int tq = tid;
    PIN(tq);
    const int lq = tq & 63;
    int kc, kfirst, kpre, wtot;
    {
      const unsigned tabq = WS_TAB + (unsigned)((tq >> 1) * NB + b) * 8;
      u32x2 gq2 = ent;
      ent[0] = tag + 1u;
#pragma clang loop unroll(disable)
      for (int spin = 0; spin < (1 << 22); spin++) {
        if (gq2[0] != tag) asm volatile("global_load_dwordx2 %0, %1, %2 sc1\n\ts_waitcnt vmcnt(0)" : "=&v"(gq2) : "v"(tabq), "s"(ws) : "memory");
        if (__all(gq2[0] == tag)) break;
        __builtin_amdgcn_s_sleep(1);
      }
      const int o0 = min((int)(gq2[1] & 0xffffu), CH), o1 = min((int)(gq2[1] >> 16), CH);
      kc = max(o1 - o0, 0);
      kfirst = (tq >> 1) * CHS + o0;
      const int inc = wave_incl_scan((tq & 1) ? 0 : kc);
      kpre = inc - kc;
      wtot = __builtin_amdgcn_readlane(inc, 63);
      if (!slow && lq == 0) atomicMax((unsigned*)(lds + L_GC + 32), (unsigned)wtot);
    }
    STAMP(7);
    u32x3 q[4];
    u32x2 pd[4], ps[4];
    int ra1[4], par[4];
    bool four;
    {
      const int map = L_MAP + wv * MAPW * 4;
      const int nw = min(wtot - lo, quota);
      const int jhi = min(kc, lo + quota - kpre);
#pragma clang loop vectorize(disable) unroll(disable)
      for (int j = max(0, lo - kpre) + ((tq ^ max(0, lo - kpre)) & 1); j < jhi; j += 2) *(u32*)(lds + map + (kpre + j - lo) * 4) = (unsigned)(kfirst + j) * 16u;
      unsigned ix[4];
#pragma unroll
      for (int k = 0; k < 4; k++) {
        const unsigned mi = *(const u32*)(lds + map + (64 * k + lq) * 4);
        ix[k] = 64 * k + lq < nw ? mi : 0u;
      }
      STAMP(23);
      u32x4 q4_[4];
      unsigned fb;
      const unsigned fbo = WS_FB + (unsigned)(32 * wv + (lq & 31)) * 4;
#pragma clang loop unroll(disable)
      for (int spin = 0; spin < (1 << 22); spin++) {
        asm volatile("global_load_dwordx4 %0, %5, %9 sc1\n\tglobal_load_dwordx4 %1, %6, %9 sc1\n\tglobal_load_dwordx4 %2, %7, %9 sc1\n\t"
                     "global_load_dwordx4 %3, %8, %9 sc1\n\tglobal_load_dword %4, %10, %11 sc1\n\ts_waitcnt vmcnt(0)"
                     : "=&v"(q4_[0]), "=&v"(q4_[1]), "=&v"(q4_[2]), "=&v"(q4_[3]), "=&v"(fb)
                     : "v"(ix[0]), "v"(ix[1]), "v"(ix[2]), "v"(ix[3]), "s"(ws + WS_R), "v"(fbo), "s"(ws) : "memory");
        bool ok = true;
#pragma unroll
        for (int k = 0; k < 4; k++) {
          const unsigned bad = (((q4_[k][1] ^ (tag << 16)) | (q4_[k][2] ^ (tag << 16))) >> 16) | ((q4_[k][3] ^ ((tag >> 16) << 22)) >> 22);
          ok = ok && (bad == 0 || 64 * k + lq >= nw);
        }
        if (__all(ok)) break;
      }
#pragma unroll
      for (int k = 0; k < 4; k++) {
        q[k][0] = q4_[k][0];
        q[k][1] = (q4_[k][1] & 0xffffu) | (q4_[k][2] << 16);
        q[k][2] = q4_[k][3] & 0x3fffffu;
      }
      STAMP(24);
      if (O_POS2) barrier();
      unsigned rk[4];
#pragma unroll
      for (int k = 0; k < 4; k++) {
        rk[k] = CAPW;
        if (64 * k + lq < nw) rk[k] = atomicAdd((unsigned*)(lds + L_GC) + ((q[k][2] >> 3) & 7u), 1u);
        const unsigned g = (q[k][2] >> 3) & 7u;
        const int dn = n0 + (int)(q[k][2] & 63u) - 3 * (int)g, sn = min((int)(q[k][2] >> 8), NN - 1);
        const int od = L_ROWS + ((6 * dn) & ~3), os = L_ROWS + ((6 * sn) & ~3);
        pd[k][0] = *(const u32*)(lds + od); pd[k][1] = *(const u32*)(lds + od + 4);
        ps[k][0] = *(const u32*)(lds + os); ps[k][1] = *(const u32*)(lds + os + 4);
        par[k] = (dn & 1) * 16;
      }
#pragma unroll
      for (int k = 0; k < 4; k++) {
        const bool ok = rk[k] < CAPW;
        const int e = (int)(((q[k][2] >> 3) & 7u) * CAPW + rk[k]);
        *(u32*)(lds + (ok ? L_R2 + e * 4 : L_DUM + 96)) = q[k][2];
        ra1[k] = ok ? L_R1 + e * 16 : L_DUM + 96;
      }
      four = nw > 192;
#pragma clang loop unroll(disable)
      for (int spin = 0; spin < (1 << 22) && !__all(fb == tag); spin++) {
        __builtin_amdgcn_s_sleep(1);
        asm volatile("global_load_dword %0, %1, %2 sc1\n\ts_waitcnt vmcnt(0)" : "=&v"(fb) : "v"(fbo), "s"(ws) : "memory");
      }
    }
    barrier();
    STAMP(8);
    const int cl = *(const u32*)(lds + L_GC + (lane & 7) * 4);
    const int maxtot = __builtin_amdgcn_readfirstlane(*(const u32*)(lds + L_GC + 32));
    if (!slow) {
      const int kk = (cl << 3) | (lane & 7);
      int rk = 0;
      rk += __builtin_amdgcn_update_dpp(0, kk, 0x121, 0xf, 0xf, false) > kk;
      rk += __builtin_amdgcn_update_dpp(0, kk, 0x122, 0xf, 0xf, false) > kk;
      rk += __builtin_amdgcn_update_dpp(0, kk, 0x123, 0xf, 0xf, false) > kk;
      rk += __builtin_amdgcn_update_dpp(0, kk, 0x124, 0xf, 0xf, false) > kk;
      rk += __builtin_amdgcn_update_dpp(0, kk, 0x125, 0xf, 0xf, false) > kk;
      rk += __builtin_amdgcn_update_dpp(0, kk, 0x126, 0xf, 0xf, false) > kk;
      rk += __builtin_amdgcn_update_dpp(0, kk, 0x127, 0xf, 0xf, false) > kk;
      const int cmax = __any(cl > CAPW) ? CAPW + 1 : 0;
      if (maxtot > MAPW || cmax > CAPW) {
        slow = true;
        quota = CAPW / 8;
        barrier();
        if (tid < 8) *(u32*)(lds + L_GC + tid * 4) = 0u;
        barrier();
        continue;
      }
      gq = __builtin_ctzll(__ballot(rk == (wv < 4 ? wv : 11 - wv)));
    }
    const int n = __builtin_amdgcn_readlane(cl, gq);
    const int swr = swr0 + gq * 2560, srd = srd0 + gq * 2560;
    const int l1 = L_R1 + gq * CAPW * 16, l2 = L_R2 + gq * CAPW * 4;
    const int nt = (n + 31) >> 5;
    u32x4 rows[8], extA, S0, S1, ra, rb, m1p = {0u, 0u, 0u, 0u};
    u32x2 r1;
    unsigned r2;
    int sp;
    f32x16 XA, XB;
    auto list_read = [&](int tl) {
      const int list = tl < nt ? l2 + tl * 128 : L_FK + wv * 128;
      ra = *(const u32x4*)(lds + list + (lane >> 4) * 32);
      rb = *(const u32x4*)(lds + list + (lane >> 4) * 32 + 16);
    };
    auto dma_piece = [&](int, int i) {
      if (i == 0) row_dma<0>(bsrd, ra[0], kdma, lbase + rowbuf, atmp);
      if (i == 1) row_dma<1>(bsrd, ra[1], kdma, lbase + rowbuf, atmp);
      if (i == 2) row_dma<2>(bsrd, ra[2], kdma, lbase + rowbuf, atmp);
      if (i == 3) row_dma<3>(bsrd, ra[3], kdma, lbase + rowbuf, atmp);
      if (i == 4) row_dma<4>(bsrd, rb[0], kdma, lbase + rowbuf, atmp);
      if (i == 5) row_dma<5>(bsrd, rb[1], kdma, lbase + rowbuf, atmp);
      if (i == 6) row_dma<6>(bsrd, rb[2], kdma, lbase + rowbuf, atmp);
      if (i == 7) row_dma<7>(bsrd, rb[3], kdma, lbase + rowbuf, atmp);
    };
    auto meta_read = [&](int tl) {
      r1 = *(const u32x2*)(lds + l1 + (tl * 32 + r) * 16 + hh * 8);
      r2 = *(const u32*)(lds + l2 + (tl * 32 + r) * 4);
    };
    auto meta_use = [&](int tl) {
      const unsigned dl = r2 & 7u;
      const u32x2 oh = *(const u32x2*)(lds + L_OH + dl * 16 + hh * 8);
      extA[0] = oh[0]; extA[1] = oh[1]; extA[2] = r1[0]; extA[3] = r1[1];
      sp = (tl * 32 + r < n) ? swr + (int)dl * 320 : L_DUM;
    };
    auto s_write = [&]() {
      *(u16*)(lds + sp) = (u16)(hh ? extA[3] : 0x3F80u);
      *(u16*)(lds + sp + 80) = (u16)(extA[3] >> 16);
    };
    auto rows_read = [&]() {
#pragma unroll
      for (int j = 0; j < 8; j++) rows[j] = *(const u32x4*)(lds + (j < 4 ? rdA : rdB) + 16 * (j & 3));
    };
    auto rows_pin = [&]() {
      asm volatile("" : "+v"(rows[0]), "+v"(rows[1]), "+v"(rows[2]), "+v"(rows[3]), "+v"(rows[4]), "+v"(rows[5]), "+v"(rows[6]), "+v"(rows[7]));
    };

    list_read(0);
    if (n > 0 && (!O_STAG || wv < 4)) {
#pragma unroll
      for (int i = 0; i < 8; i++) dma_piece(0, i);
    }
    STAMP(18);
    auto finish = [&](int k) {
      const unsigned long long ud = (((unsigned long long)pd[k][1] << 32) | pd[k][0]) >> par[k];
      const unsigned long long us = (((unsigned long long)ps[k][1] << 32) | ps[k][0]) >> ((q[k][2] >> 4) & 16u);
      const unsigned dlo = (unsigned)ud, dhi = (unsigned)(ud >> 32), slo = (unsigned)us, shi = (unsigned)(us >> 32);
      const unsigned dxy = pk2(bf2f(dlo) - bf2f(slo), bfhi(dlo) - bfhi(slo));
      const unsigned dz_ = pk2(bf2f(dhi) - bf2f(shi), 0.f);
      const float dx = bf2f(dxy), dy = bfhi(dxy), dz = bf2f(dz_);
      const unsigned sxy = pk2(dx * dx, dy * dy), sz = pk2(dz * dz, 0.f);
      const unsigned d2 = pk2((bf2f(sxy) + bfhi(sxy)) + bf2f(sz), 0.f) & 0xffffu;
      const u32x4 rec = {q[k][0], d2 | (dxy << 16), q[k][1], (dxy >> 16) | (dz_ << 16)};
      *(u32x4*)(lds + ra1[k]) = rec;
    };
    finish(0);
    finish(1);
    finish(2);
    if (four) finish(3);
    if (lane < 32 * nt - n) {
      const u32x4 z = {0u, 0u, 0u, 0u};
      *(u32x4*)(lds + l1 + (n + lane) * 16) = z;
    }
    if (w == 0 && tid == 0) __builtin_amdgcn_raw_buffer_store_b32(tag, wsr, WS_EP, 0, 16);
    pin = pos[min(3 * n0 + 15 * gq + (lq & 15), 3 * NN - 1)];
#pragma unroll
    for (int t = 0; t < 4; t++) {
      const u32x2 v = *(const u32x2*)(lds + L_A + gq * 2048 + t * 512 + lq * 8);
      extB[t][0] = v[0];
      extB[t][1] = v[1];
    }
    STAMP(19);
    barrier();
    STAMP(20);
    if (O_PRIO == 1 && wv >= 4) __builtin_amdgcn_s_setprio(1);
    if (O_PRIO == 2 && wv < 4) __builtin_amdgcn_s_setprio(1);
    if (n > 0) {
    if (O_STAG && wv >= 4) {
#pragma unroll
      for (int i = 0; i < 8; i++) dma_piece(0, i);
    }
    meta_read(0);
    list_read(1);
    meta_use(0);
    s_write();
    S1 = m1p;
    STAMP(21);
    wait_vm0();
    STAMP(22);
    PIN(pin);
    rows_read();
    rows_pin();
    XA = mfma(rows[0], I0, zero16());
    XA = mfma(rows[1], I1, XA);
    XA = mfma(extA, extB[0], XA);
    STAMP(9);

    const int nfull = O_HALF ? (n + 15) >> 5 : nt;
    int first = 1;
    for (int tl = 0; tl < nfull; tl++) {
      asm volatile("" : "+s"(first));
#pragma unroll
      for (int t = 0; t < 4; t++) {
        f32x16& Xc = (t & 1) ? XB : XA;
        f32x16& Xn = (t & 1) ? XA : XB;
        const int tn = (t + 1) & 3;
        auto gap = [&](int k) {
          SB();
          if (t == 0 && k == 0 && first) {
#pragma unroll
            for (int i = 0; i < 8; i++) dma_piece(1, i);
          }
          if (t == 0 && k == 0) S0 = *(const u32x4*)(lds + srd);
          if (t == 0 && k == 3) S1 = *(const u32x4*)(lds + srd + 32);
          if (t == 0 && k == 4) {
            *(u16*)(lds + sp) = 0;
            *(u16*)(lds + sp + 80) = 0;
          }
          if (t == 2 && k == 0) meta_read(tl + 1);
          if (t == 2 && k == 2) meta_use(tl + 1);
          if (t == 2 && k == 3) wait_vm0();
          if (t == 2 && k >= 3 && k < 7) {
            rows[2 * k - 6] = *(const u32x4*)(lds + (k < 5 ? rdA : rdB) + 16 * ((2 * k - 6) & 3));
            rows[2 * k - 5] = *(const u32x4*)(lds + (k < 5 ? rdA : rdB) + 16 * ((2 * k - 5) & 3));
          }
          if (t == 2 && k == 7) list_read(tl + 2);
          if (t == 3 && k < 8) dma_piece(tl + 2, k);
          if (t == 3 && k == 8) s_write();
          SB();
        };
        f32x2 dd[8];
        if (O_PRIO == 4 && t == 0) __builtin_amdgcn_s_setprio(1);
        if (O_PRIO == 4 && t == 2) __builtin_amdgcn_s_setprio(0);
        if (O_PRIO == 7 && t == 3 && wv >= 4) __builtin_amdgcn_s_setprio(1);
        if (O_PRIO == 7 && t == 2 && wv >= 4) __builtin_amdgcn_s_setprio(0);
        SB();
        Xn = mfma(rows[2 * tn], I0, zero16());
        PIN(Xn);
        SB();
#pragma unroll
        for (int g = 0; g < 4; g++) {
#pragma unroll
          for (int i = 2 * g; i < 2 * g + 2; i++) {
            dd[i][0] = __builtin_amdgcn_exp2f(Xc[2 * i]);
            dd[i][1] = __builtin_amdgcn_exp2f(Xc[2 * i + 1]);
            PIN(dd[i]);
          }
          SB();
          if (g == 0) { Xn = mfma(rows[2 * tn + 1], I1, Xn); PIN(Xn); }
          if (g == 1) { Xn = mfma(extA, extB[tn], Xn); PIN(Xn); }
          if (g == 2) { Y[(t + 3) & 3] = mfma(S1, m1p, Y[(t + 3) & 3]); PIN(Y[(t + 3) & 3]); }
          gap(g);
        }
#pragma unroll
        for (int i = 0; i < 8; i++) { dd[i] += 1.0f; if (i < 7) PIN(dd[i]); }
        gap(4);
#pragma unroll
        for (int g = 0; g < 4; g++) {
#pragma unroll
          for (int i = 2 * g; i < 2 * g + 2; i++) {
            dd[i][0] = __builtin_amdgcn_rcpf(dd[i][0]);
            dd[i][1] = __builtin_amdgcn_rcpf(dd[i][1]);
            PIN(dd[i]);
          }
          gap(5 + g);
        }
        u32x4 m0;
#pragma unroll
        for (int i = 0; i < 4; i++) { f32x2 x = {Xc[2 * i], Xc[2 * i + 1]}; dd[i] *= x; }
#pragma unroll
        for (int i = 0; i < 4; i++) m0[i] = __builtin_bit_cast(unsigned, __builtin_convertvector(dd[i], bf16x2));
        SB();
#pragma unroll
        for (int i = 4; i < 8; i++) { f32x2 x = {Xc[2 * i], Xc[2 * i + 1]}; dd[i] *= x; if (i < 7) PIN(dd[i]); }
        SB();
        Y[t] = mfma(S0, m0, Y[t]);
        PIN(Y[t]);
        SB();
#pragma unroll
        for (int i = 0; i < 4; i++) m1p[i] = __builtin_bit_cast(unsigned, __builtin_convertvector(dd[4 + i], bf16x2));
        PIN(m1p);
        if (t == 2) rows_pin();
      }
      first = 0;
    }
    SB();
    Y[3] = mfma(S1, m1p, Y[3]);
    if (nfull < nt) {
      S0 = *(const u32x4*)(lds + srd);
      *(u16*)(lds + sp) = 0;
      *(u16*)(lds + sp + 80) = 0;
#pragma unroll
      for (int t = 0; t < 4; t++) {
        f32x16& Xc = (t & 1) ? XB : XA;
        f32x16& Xn = (t & 1) ? XA : XB;
        f32x2 dd[4];
        if (t < 2 && nfull == 0) {
#pragma unroll
          for (int i = 0; i < 4; i++) dma_piece(nt, 4 * t + i);
        }
        SB();
        if (t < 3) { Xn = mfma(rows[2 * t + 2], I0, zero16()); PIN(Xn); }
        SB();
#pragma unroll
        for (int i = 0; i < 2; i++) { dd[i][0] = __builtin_amdgcn_exp2f(Xc[2 * i]); dd[i][1] = __builtin_amdgcn_exp2f(Xc[2 * i + 1]); }
        SB();
        if (t < 3) { Xn = mfma(rows[2 * t + 3], I1, Xn); PIN(Xn); }
        SB();
#pragma unroll
        for (int i = 2; i < 4; i++) { dd[i][0] = __builtin_amdgcn_exp2f(Xc[2 * i]); dd[i][1] = __builtin_amdgcn_exp2f(Xc[2 * i + 1]); }
        SB();
        if (t < 3) { Xn = mfma(extA, extB[t + 1], Xn); PIN(Xn); }
        SB();
#pragma unroll
        for (int i = 0; i < 4; i++) dd[i] += 1.0f;
        SB();
#pragma unroll
        for (int i = 0; i < 4; i++) { dd[i][0] = __builtin_amdgcn_rcpf(dd[i][0]); dd[i][1] = __builtin_amdgcn_rcpf(dd[i][1]); }
        SB();
        u32x4 m0;
#pragma unroll
        for (int i = 0; i < 4; i++) { f32x2 x = {Xc[2 * i], Xc[2 * i + 1]}; dd[i] *= x; }
#pragma unroll
        for (int i = 0; i < 4; i++) m0[i] = __builtin_bit_cast(unsigned, __builtin_convertvector(dd[i], bf16x2));
        SB();
        Y[t] = mfma(S0, m0, Y[t]);
        if (t < 3) PIN(Xn);
      }
    }
    wh_loaded = true;
    wait_vm0();
    }
    if (!slow) break;
    lo += quota;
    if (lo >= maxtot) break;
    barrier();
    if (tid < 8) *(u32*)(lds + L_GC + tid * 4) = 0u;
#pragma unroll
    for (int i = 0; i < 8; i++) pos_tile(i);
    wait_vm0();
    barrier();
  }
  STAMP(10);
  if (!wh_loaded) {
    wait_vm0();
    PIN(pin);
    const u32x4 fa = *(const u32x4*)(lds + L_FK + wv * 128 + (lane >> 4) * 32), fb = *(const u32x4*)(lds + L_FK + wv * 128 + (lane >> 4) * 32 + 16);
    row_dma<0>(bsrd, fa[0], kdma, lbase + rowbuf, atmp); row_dma<1>(bsrd, fa[1], kdma, lbase + rowbuf, atmp);
    row_dma<2>(bsrd, fa[2], kdma, lbase + rowbuf, atmp); row_dma<3>(bsrd, fa[3], kdma, lbase + rowbuf, atmp);
    row_dma<4>(bsrd, fb[0], kdma, lbase + rowbuf, atmp); row_dma<5>(bsrd, fb[1], kdma, lbase + rowbuf, atmp);
    row_dma<6>(bsrd, fb[2], kdma, lbase + rowbuf, atmp); row_dma<7>(bsrd, fb[3], kdma, lbase + rowbuf, atmp);
    wait_vm0();
  }

#pragma unroll
  for (int i = 0; i < 3; i++) {
    if (i < 2 || hh == 0) {
      const u32x2 mv = {pk2(Y[0][4 * i] * NLN2, Y[1][4 * i] * NLN2), pk2(Y[2][4 * i] * NLN2, Y[3][4 * i] * NLN2)};
      *(u32x2*)(lds + L_M + (5 * gq + 2 * i + hh) * MSTR + 8 * r) = mv;
    }
  }
  u32x4 hq[8], res[2];
  unsigned wxr[4];
#pragma unroll
  for (int ks = 0; ks < 8; ks++) hq[ks] = *(const u32x4*)(lds + (ha ^ (32 * ks)));
  {
    const int ra_ = (ha ^ (16 * hh)) ^ (64 * T + 32 * hh);
    res[0] = *(const u32x4*)(lds + ra_);
    res[1] = *(const u32x4*)(lds + (ra_ ^ 16));
  }
#pragma unroll
  for (int t = 0; t < 4; t++) wxr[t] = ((const u16*)(lds + L_WX))[32 * t + r];
  unsigned wa = lbase + L_ROWS + 2080 * hh + 256 * q4 + 64 * (T ^ q4) + 32 * (p4 & 1) + 16 * blk + 8 * (p4 >> 1);
  int ma = L_M + min(loc, 39) * MSTR + 16 * hh;
  PIN(wa);
  PIN(ma);
  STAMP(13);
  barrier();
  STAMP(11);
  {
    u32x4 w1[8], w2[8], mq[8];
#pragma unroll
    for (int ks = 0; ks < 8; ks++) w1[ks] = tr_read2(wa + ks * ROWBUF + 4352, wa + ks * ROWBUF + 4352 + 1040);
#pragma unroll
    for (int ks = 0; ks < 8; ks++) {
      w2[ks] = tr_read2(wa + ks * ROWBUF, wa + ks * ROWBUF + 1040);
      mq[ks] = *(const u32x4*)(lds + ma + 32 * ks);
    }
    SB();
    f32x16 P = zero16();
#pragma unroll
    for (int ks = 0; ks < 8; ks++) P = mfma(w1[ks], hq[ks], P);
#pragma unroll
    for (int ks = 0; ks < 8; ks++) P = mfma(w2[ks], mq[ks], P);
    PIN(P);
    SB();
    float zz[9];
    {
      float wxv[4];
#pragma unroll
      for (int t = 0; t < 4; t++) wxv[t] = bf2f(wxr[t]) * NLN2;
#pragma unroll
      for (int i = 0; i < 9; i++) {
        const int j = 4 * (i / 3) + 1 + i % 3;
        zz[i] = Y[0][j] * wxv[0];
        PIN(zz[i]);
        zz[i] = __builtin_fmaf(Y[1][j], wxv[1], zz[i]);
        PIN(zz[i]);
        zz[i] = __builtin_fmaf(Y[2][j], wxv[2], zz[i]);
        PIN(zz[i]);
        zz[i] = __builtin_fmaf(Y[3][j], wxv[3], zz[i]);
        PIN(zz[i]);
      }
    }
    SB();
    f32x2 x[8], e[8];
#pragma unroll
    for (int i = 0; i < 8; i++) {
      x[i][0] = P[2 * i];
      x[i][1] = P[2 * i + 1];
      e[i] = x[i] * NLOG2E;
    }
    SB();
#pragma unroll
    for (int i = 0; i < 8; i++) { e[i][0] = __builtin_amdgcn_exp2f(e[i][0]); e[i][1] = __builtin_amdgcn_exp2f(e[i][1]); }
    SB();
#pragma unroll
    for (int i = 0; i < 8; i++) e[i] += 1.0f;
    SB();
#pragma unroll
    for (int i = 0; i < 8; i++) { e[i][0] = __builtin_amdgcn_rcpf(e[i][0]); e[i][1] = __builtin_amdgcn_rcpf(e[i][1]); }
    SB();
    u32x4 ov[2];
#pragma unroll
    for (int i = 0; i < 8; i++) {
      const unsigned rs = res[i >> 2][i & 3];
      const f32x2 rv = {bf2f(rs), bfhi(rs)};
      const f32x2 o = __builtin_elementwise_fma(x[i], e[i], rv);
      ov[i >> 2][i & 3] = pk2(o[0], o[1]);
    }
    if (valid) {
      const __amdgpu_buffer_rsrc_t osr = __builtin_amdgcn_make_buffer_rsrc(out, 0, NN * 262, 0x00020000);
      const unsigned oo = (unsigned)(n0 + loc) * 256 + 64 * T + 32 * hh;
      __builtin_amdgcn_raw_buffer_store_b128(ov[0], osr, oo, 0, O_OUTAUX);
      __builtin_amdgcn_raw_buffer_store_b128(ov[1], osr, oo + 16, 0, O_OUTAUX);
    }
    STAMP(17);
#define DPP9(ctl)                                                                                                         \
  "v_add_f32_dpp %0, %0, %0 " ctl "\n\tv_add_f32_dpp %1, %1, %1 " ctl "\n\tv_add_f32_dpp %2, %2, %2 " ctl "\n\t"          \
  "v_add_f32_dpp %3, %3, %3 " ctl "\n\tv_add_f32_dpp %4, %4, %4 " ctl "\n\tv_add_f32_dpp %5, %5, %5 " ctl "\n\t"          \
  "v_add_f32_dpp %6, %6, %6 " ctl "\n\tv_add_f32_dpp %7, %7, %7 " ctl "\n\tv_add_f32_dpp %8, %8, %8 " ctl "\n\t"
    asm volatile("s_nop 1\n\t" DPP9("row_shr:1 row_mask:0xf bank_mask:0xf bound_ctrl:1") DPP9("row_shr:2 row_mask:0xf bank_mask:0xf bound_ctrl:1")
                 DPP9("row_shr:4 row_mask:0xf bank_mask:0xf bound_ctrl:1") DPP9("row_shr:8 row_mask:0xf bank_mask:0xf bound_ctrl:1")
                 DPP9("row_bcast:15 row_mask:0xa bank_mask:0xf") "s_nop 0"
                 : "+v"(zz[0]), "+v"(zz[1]), "+v"(zz[2]), "+v"(zz[3]), "+v"(zz[4]), "+v"(zz[5]), "+v"(zz[6]), "+v"(zz[7]), "+v"(zz[8]));
    const int zp = L_S + gq * 2560;
    if (r == 31) {
#pragma unroll
      for (int i = 0; i < 9; i++) *(u32*)(lds + zp + 12 * hh + 24 * (i / 3) + 4 * (i % 3)) = __float_as_uint(zz[i]);
    }
    asm volatile("" ::: "memory");
    const float z = __uint_as_float(*(const u32*)(lds + zp + 4 * (lane & 15)));
    if (lane < 15 && 15 * gq + lane < 3 * nsz) out[(size_t)NN * 128 + (unsigned)(3 * n0 + 15 * gq + lane)] = (u16)pk2(bf2f(pin) + z, 0.f);
  }
  STAMP(12);
  STAMP_OUT();
}

extern "C" void kernel_launch(void* const* d_in, const int* in_sizes, int n_in, void* d_out, int out_size, void* d_ws,
                              size_t ws_size, hipStream_t stream) {
  egnn_fused<<<NB, 512, 0, stream>>>((const u16*)d_in[0], (const u16*)d_in[1], (const u32x2*)d_in[2], (const int*)d_in[3],
                                     (const int*)d_in[4], (const u16*)d_in[5], (const u16*)d_in[6], (const u16*)d_in[7],
                                     (char*)d_ws, (u16*)d_out);
}
```

```cpp
#include <hip/hip_runtime.h>
#include <hip/hip_bf16.h>
#include <stdint.h>

typedef __bf16 bf16x8 __attribute__((ext_vector_type(8)));
typedef __bf16 bf16x2 __attribute__((ext_vector_type(2)));
typedef short s16x4 __attribute__((ext_vector_type(4)));
typedef float f32x2 __attribute__((ext_vector_type(2)));
typedef float f32x16 __attribute__((ext_vector_type(16)));
typedef unsigned u32x2 __attribute__((ext_vector_type(2), may_alias));
typedef unsigned u32x3 __attribute__((ext_vector_type(3)));
typedef unsigned u32x4 __attribute__((ext_vector_type(4), may_alias));
typedef unsigned short u16 __attribute__((may_alias));
typedef unsigned u32 __attribute__((may_alias));
#define LDSP(T) __attribute__((address_space(3))) T*
#define DI __device__ __forceinline__
#define SB() __builtin_amdgcn_sched_barrier(0)
#define PIN(x) asm volatile("" : "+v"(x))

constexpr int NN = 10000;
constexpr int CH = 1250, CHS = 1280;
constexpr int NB = 256;
constexpr float NLOG2E = -1.4426950408889634f;
constexpr float NLN2 = -0.6931471805599453f;

constexpr unsigned WS_BT = 4096;
constexpr unsigned WS_R = WS_BT + (NN + 256) * 256;
constexpr unsigned WS_TAB = WS_R + NB * CHS * 16;
constexpr unsigned WS_FB = WS_TAB + NB * NB * 8;
constexpr unsigned WS_EP = WS_FB + NB * 4;
constexpr unsigned WS_END = WS_EP + 256;

constexpr int CAPW = 224;
constexpr int ROWBUF = 8496;
constexpr int L_ROWS = 0;
constexpr int L_R1 = 8 * ROWBUF;
constexpr int L_R2 = L_R1 + 8 * CAPW * 16;
constexpr int L_S = L_R2 + 8 * CAPW * 4;
constexpr int L_M = L_S + 8 * 2560;
constexpr int MSTR = 272;
constexpr int L_H = 135168;
constexpr int L_A = L_H + 10240;
constexpr int L_HIST = L_A + 8 * 2048;
constexpr int L_OH = L_HIST + 1024;
constexpr int L_GC = L_OH + 128;
constexpr int L_DUM = L_GC + 64;
constexpr int L_WX = L_DUM + 128;
constexpr int L_END = L_WX + 256;
constexpr int L_W = L_R1;
constexpr int L_EXT = L_W + 65536;
constexpr int L_ST = 47104;
constexpr int L_BS = L_A;
constexpr int L_OFF = L_A + 10496;
constexpr int MAPW = 256;
constexpr int L_MAP = L_M;
constexpr int L_FK = L_HIST;
static_assert(L_OH % 128 == 0 && L_ST + 20480 <= L_R1 && L_BS % 256 == 0 && L_W % 32 == 0 && L_W + 65536 <= L_EXT && L_EXT + 1280 <= L_H && L_H % 256 == 0 && L_M + 40 * MSTR <= L_H && L_END <= 163840, "lds map");

DI constexpr int piece_off(int i) { return 1040 * (i & 3) + 4352 * (i >> 2); }
DI float bf2f(unsigned u) { return __uint_as_float(u << 16); }
DI float bfhi(unsigned u) { return __uint_as_float(u & 0xffff0000u); }
DI unsigned pk2(float a, float b) {
  f32x2 v = {a, b};
  return __builtin_bit_cast(unsigned, __builtin_convertvector(v, bf16x2));
}
DI f32x16 mfma(u32x4 a, u32x4 b, f32x16 c) {
  return __builtin_amdgcn_mfma_f32_32x32x16_bf16(__builtin_bit_cast(bf16x8, a), __builtin_bit_cast(bf16x8, b), c, 0, 0, 0);
}
DI f32x16 zero16() {
  f32x16 z;
#pragma unroll
  for (int i = 0; i < 16; i++) z[i] = 0.f;
  return z;
}
DI u32x4 tr_read2(unsigned a0, unsigned a1) {
  const u32x2 lo = __builtin_bit_cast(u32x2, __builtin_amdgcn_ds_read_tr16_b64_v4i16((LDSP(s16x4))(size_t)a0));
  const u32x2 hi = __builtin_bit_cast(u32x2, __builtin_amdgcn_ds_read_tr16_b64_v4i16((LDSP(s16x4))(size_t)a1));
  const u32x4 v = {lo[0], lo[1], hi[0], hi[1]};
  return v;
}
DI int bucket_start(int b) { return (b * 625 + 15) >> 4; }
DI int wave_incl_scan(int x) {
  x += __builtin_amdgcn_update_dpp(0, x, 0x111, 0xf, 0xf, false);
  x += __builtin_amdgcn_update_dpp(0, x, 0x112, 0xf, 0xf, false);
  x += __builtin_amdgcn_update_dpp(0, x, 0x114, 0xf, 0xf, false);
  x += __builtin_amdgcn_update_dpp(0, x, 0x118, 0xf, 0xf, false);
  x += __builtin_amdgcn_update_dpp(0, x, 0x142, 0xa, 0xf, false);
  x += __builtin_amdgcn_update_dpp(0, x, 0x143, 0xc, 0xf, false);
  return x;
}
DI void dma16(const void* sbase, unsigned voff, unsigned lds_base) {
  asm volatile("s_mov_b32 m0, %2\n\ts_nop 0\n\tglobal_load_lds_dwordx4 %0, %1" ::"v"(voff), "s"(sbase), "s"(lds_base) : "memory");
}
template <int I>
DI void row_dma(u32x4 srd, unsigned key, unsigned lanec, unsigned lds_base, unsigned& t) {
  if (I & 3)
    asm volatile("v_and_or_b32 %0, %1, %2, %3\n\tbuffer_load_dwordx4 %0, %4, %5 offen offset:%6 sc1 lds"
                 : "+v"(t) : "v"(key), "s"(0xffffff00u), "v"(lanec), "s"(srd), "s"(3120 - 1040 * (I & 3)), "i"(1040 * (I & 3)) : "memory");
  else
    asm volatile("s_mov_b32 m0, %6\n\tv_and_or_b32 %0, %1, %2, %3\n\tbuffer_load_dwordx4 %0, %4, %5 offen sc1 lds"
                 : "+v"(t) : "v"(key), "s"(0xffffff00u), "v"(lanec), "s"(srd), "s"(3120), "s"(lds_base + 4352 * (I >> 2)) : "memory");
}
DI void wait_vm0() { asm volatile("s_waitcnt vmcnt(0)" ::: "memory"); }
DI void barrier() { asm volatile("s_waitcnt lgkmcnt(0)\n\ts_barrier" ::: "memory"); }

__global__ __launch_bounds__(512, 2) void egnn_fused(const u16* __restrict__ h, const u16* __restrict__ pos,
                                                     const u32x2* __restrict__ eattr, const int* __restrict__ src,
                                                     const int* __restrict__ dst, const u16* __restrict__ we,
                                                     const u16* __restrict__ wx, const u16* __restrict__ wh,
                                                     char* ws, u16* __restrict__ out) {
  __shared__ __attribute__((aligned(256))) char lds[L_END];
  const unsigned lbase = (unsigned)(size_t)lds;
  const int tid = threadIdx.x, lane = tid & 63;
  const int wv = __builtin_amdgcn_readfirstlane(tid >> 6);
  const int w = blockIdx.x;
  const int b = (w & 7) * 32 + (w >> 3);
  const int n0 = bucket_start(b), nsz = bucket_start(b + 1) - n0;
  const int r = lane & 31, hh = lane >> 5;
  const int blk = (lane >> 4) & 1, q4 = (lane & 15) >> 2, p4 = lane & 3;
  const __amdgpu_buffer_rsrc_t wsr = __builtin_amdgcn_make_buffer_rsrc(ws, 0, WS_END, 0x00020000);

  auto pos_tile = [&](int i) {
    const int j = i < 5 ? 8 * i + wv : i == 5 ? 46 + wv : i == 6 ? 40 + wv : 54 + wv;
    if (i < 6 || (i == 6 && wv < 6) || (i == 7 && (wv < 4 || (wv == 4 && lane < 38))))
      dma16(pos, (unsigned)(j * 1024 + lane * 16), lbase + L_ROWS + j * 1024);
  };

  int d[3], s[3];
  u32x2 a[3];
  unsigned tagv;
  const bool v2 = tid < CH - 1024;
  const unsigned wlb = (unsigned)((lane >> 4) * 256 + 16 * ((lane & 15) ^ ((lane >> 4) << 2)));
  if (wv < 4) {
    const int j = (w >> 3) * 4 + wv;
    if (j < 64) dma16(we, (unsigned)(j * 1024 + lane * 16), lbase + L_ST + wv * 1024);
    else if (j < 122) dma16(pos, (unsigned)((j - 64) * 1024 + lane * 16), lbase + L_ST + wv * 1024);
  }
  {
    const unsigned e0 = (unsigned)(w * CH + tid) * 4, e2 = v2 ? e0 + 4096 : e0;
    asm volatile("global_load_dword %0, %10, %14\n\tglobal_load_dword %1, %10, %14 offset:2048\n\tglobal_load_dword %2, %11, %14\n\t"
                 "global_load_dword %3, %10, %15\n\tglobal_load_dword %4, %10, %15 offset:2048\n\tglobal_load_dword %5, %11, %15\n\t"
                 "global_load_dwordx2 %6, %12, %16\n\tglobal_load_dwordx2 %7, %13, %16\n\tglobal_load_dwordx2 %8, %18, %16\n\t"
                 "global_load_dword %9, %19, %17 sc1"
                 : "=&v"(d[0]), "=&v"(d[1]), "=&v"(d[2]), "=&v"(s[0]), "=&v"(s[1]), "=&v"(s[2]), "=&v"(a[0]), "=&v"(a[1]), "=&v"(a[2]), "=&v"(tagv)
                 : "v"(e0), "v"(e2), "v"(2 * e0), "v"(2 * e0 + 4096), "s"(dst), "s"(src), "s"(eattr), "s"(ws), "v"(2 * e2), "v"(WS_EP)
                 : "memory");
    const int row = 4 * wv + (lane >> 4), c = lane & 15;
#pragma unroll
    for (int i = 0; i < 4; i++) dma16(we, (wlb ^ (16 * i)) + (32 + 4 * wv + i) * 1024, lbase + L_W + (32 + 4 * wv + i) * 1024);
    dma16(h, (unsigned)(min(n0 + row, NN - 1) * 256 + 16 * (c ^ (row & 15))), lbase + L_H + wv * 1024);
    if (wv < 2) dma16(h, (unsigned)(min(n0 + 32 + row, NN - 1) * 256 + 16 * (c ^ (row & 15))), lbase + L_H + (8 + wv) * 1024);
  }
  if (tid < 256) *(u32*)(lds + L_HIST + tid * 4) = 0u;
  else {
    const int t = tid - 256;
    if (t < 32) {
      const int j = (t >> 2) - 4 * ((t >> 1) & 1) - 2 * (t & 1);
      *(u32*)(lds + L_OH + t * 4) = j == 0 ? 0x3F80u : j == 1 ? 0x3F800000u : 0u;
      if (t < 16) *(u32*)(lds + L_GC + t * 4) = 0u;
    }
  }
  barrier();
  {
    if (wv == 3) dma16(we, 65536 + lane * 16, lbase + L_EXT);
    if (wv == 4 && lane < 16) dma16(we, 66560 + lane * 16, lbase + L_EXT + 1024);
    if (wv == 5 && lane < 16) dma16(wx, lane * 16, lbase + L_WX);
    if (wv == 6 && lane < 16) dma16(wh, (unsigned)(w * 256 + lane * 16), lbase + L_BS + 10240);
  }
  const int T = wv & 3, nb = wv >> 2;
  const int loc = 5 * (4 * nb + (r >> 3)) + (r & 7);
  const bool valid = (r & 7) < 5 && loc < nsz;
  int ha = L_H + min(loc, 39) * 256 + 16 * (hh ^ (min(loc, 39) & 15));
  const unsigned trx0 = lbase + L_W + (unsigned)((8 * hh + q4) * 256), trs0 = (unsigned)((q4 << 2) | (2 * hh));
  unsigned b0 = trx0 + 32768 + 16 * ((unsigned)(8 * (T & 1) + 4 * (p4 & 1) + 2 * blk + (T >> 1)) ^ trs0) + 8 * (p4 >> 1);
  unsigned a0 = trx0 + 16 * ((unsigned)(4 * T + 2 * blk + (p4 >> 1)) ^ trs0) + 8 * (p4 & 1);
  int bo = L_BS + loc * 256 + 16 * ((4 * T + 2 * hh) ^ (loc & 15));
  unsigned gbo = WS_BT + (unsigned)((n0 + (tid >> 4)) * 256 + 16 * ((tid ^ (tid >> 4)) & 15));
  PIN(b0); PIN(a0); PIN(bo); PIN(gbo); PIN(ha);
  u32x4 I0, I1;
  {
    const unsigned one = (r & 1) ? 0x3F800000u : 0x3F80u;
#pragma unroll
    for (int jj = 0; jj < 4; jj++) {
      const bool m = ((r & 7) >> 1) == jj;
      I0[jj] = (m && (r >> 3) == hh) ? one : 0u;
      I1[jj] = (m && (r >> 3) == 2 + hh) ? one : 0u;
    }
  }
  const int rowbuf = L_ROWS + wv * ROWBUF;
  unsigned kdma = 16 * ((lane & 15) ^ (4 * (lane >> 4)));
  const int rd0 = rowbuf + 1040 * (r & 3) + 4352 * ((r >> 2) & 1) + (r >> 3) * 256;
  int rdA = rd0 + 64 * ((2 * hh) ^ (r >> 3)), rdB = rd0 + 64 * ((2 * hh + 1) ^ (r >> 3));
  int swr0 = L_S + 32 * (r >> 4) + 16 * ((r >> 2) & 1) + 8 * ((r >> 3) & 1) + 2 * (r & 3) + 160 * hh;
  int srd0 = L_S + r * 80 + 16 * hh;
  const unsigned long long bbase = (unsigned long long)(ws + WS_BT - 3120);
  const u32x4 bsrd = {(unsigned)bbase, (unsigned)(bbase >> 32) & 0xffffu, (unsigned)((NN + 256) * 256 + 3120), 0x00020000u};
  f32x16 Y[4];
#pragma unroll
  for (int t = 0; t < 4; t++) {
    Y[t] = zero16();
    PIN(Y[t]);
  }
  PIN(I0); PIN(I1); PIN(kdma); PIN(rdA); PIN(rdB); PIN(swr0); PIN(srd0);
  asm volatile("s_waitcnt vmcnt(12)" : "+v"(d[0]), "+v"(d[1]), "+v"(d[2])::"memory");
  int key[3];
  unsigned rank[3];
#pragma unroll
  for (int k = 0; k < 3; k++) {
    key[k] = (int)((unsigned)__umul24((unsigned)d[k], 429497u) >> 24);
    rank[k] = 0;
    if (k < 2 || v2) rank[k] = atomicAdd((unsigned*)(lds + L_HIST) + key[k], 1u);
  }
#pragma unroll
  for (int i = 0; i < 4; i++) dma16(we, (wlb ^ (16 * i)) + (4 * wv + i) * 1024, lbase + L_W + (4 * wv + i) * 1024);
  asm volatile("s_waitcnt vmcnt(9)" : "+v"(s[0]), "+v"(s[1]), "+v"(s[2]), "+v"(a[0]), "+v"(a[1]), "+v"(a[2]), "+v"(tagv)::"memory");
  const unsigned tag = (unsigned)__builtin_amdgcn_readfirstlane((int)tagv) + 1u;
  u32x4 rec[3];
#pragma unroll
  for (int k = 0; k < 3; k++) {
    const int lc = d[k] - bucket_start(key[k]);
    const int g = (lc * 205) >> 10;
    rec[k][0] = a[k][0];
    rec[k][1] = (a[k][1] & 0xffffu) | (tag << 16);
    rec[k][2] = (a[k][1] >> 16) | (tag << 16);
    rec[k][3] = ((unsigned)s[k] << 8) | (unsigned)(16 * lc - 79 * g) | ((tag >> 16) << 22);
    PIN(rec[k]);
  }
  barrier();
  u32x4 off4;
  unsigned oend;
  {
    const u32x4 cnt = *(const u32x4*)(lds + L_HIST + lane * 16);
    const unsigned tot = cnt[0] + cnt[1] + cnt[2] + cnt[3];
    oend = (unsigned)wave_incl_scan((int)tot);
    off4[0] = oend - tot;
    off4[1] = off4[0] + cnt[0];
    off4[2] = off4[1] + cnt[1];
    off4[3] = off4[2] + cnt[2];
    *(u32x4*)(lds + L_OFF + lane * 16) = off4;
    if (wv == 7) {
      const u32x4 g0 = {tag, off4[0] | (off4[1] << 16), tag, off4[1] | (off4[2] << 16)};
      const u32x4 g1 = {tag, off4[2] | (off4[3] << 16), tag, off4[3] | (oend << 16)};
      const unsigned off = WS_TAB + (unsigned)(w * NB + 4 * lane) * 8;
      __builtin_amdgcn_raw_buffer_store_b128(g0, wsr, off, 0, 16);
      __builtin_amdgcn_raw_buffer_store_b128(g1, wsr, off + 16, 0, 16);
    }
#pragma unroll
    for (int k = 0; k < 3; k++) rank[k] += *(const u32*)(lds + L_OFF + key[k] * 4);
#pragma unroll
    for (int k = 0; k < 3; k++)
      if (k < 2 || v2) *(u32x4*)(lds + L_ST + rank[k] * 16) = rec[k];
  }
  if (wv == 7) asm volatile("s_waitcnt vmcnt(2)" ::: "memory");
  else wait_vm0();
  barrier();
  u32x2 at[4], ent;
  {
    u32x4 v0 = *(const u32x4*)(lds + L_ST + tid * 16);
    u32x4 v1 = *(const u32x4*)(lds + L_ST + 8192 + tid * 16);
    u32x4 v2_ = *(const u32x4*)(lds + L_ST + 16384 + (tid & 255) * 16);
    const unsigned b1 = (b0 ^ 16) + 1024, a1 = (a0 ^ 16) + 1024;
    u32x4 hf[8], wf[8];
#pragma unroll
    for (int ks = 0; ks < 8; ks++) {
      hf[ks] = *(const u32x4*)(lds + (ha ^ (32 * ks)));
      wf[ks] = tr_read2(b0 + ks * 4096, b1 + ks * 4096);
    }
    SB();
    __builtin_amdgcn_raw_buffer_store_b128(v0, wsr, WS_R + (unsigned)(w * CHS + tid) * 16, 0, 16);
    __builtin_amdgcn_raw_buffer_store_b128(v1, wsr, WS_R + (unsigned)(w * CHS + 512 + tid) * 16, 0, 16);
    if (tid < 256) __builtin_amdgcn_raw_buffer_store_b128(v2_, wsr, WS_R + (unsigned)(w * CHS + 1024 + tid) * 16, 0, 16);
    f32x16 acc = zero16();
#pragma unroll
    for (int ks = 0; ks < 8; ks++) {
      acc = mfma(wf[ks], hf[ks], acc);
      PIN(acc);
      pos_tile(ks);
    }
    SB();
#pragma unroll
    for (int ks = 0; ks < 8; ks++) wf[ks] = tr_read2(a0 + ks * 4096, a1 + ks * 4096);
    SB();
    u32x4 bt0, bt1;
#pragma unroll
    for (int i = 0; i < 4; i++) {
      bt0[i] = pk2(acc[2 * i] * NLOG2E, acc[2 * i + 1] * NLOG2E);
      bt1[i] = pk2(acc[8 + 2 * i] * NLOG2E, acc[9 + 2 * i] * NLOG2E);
    }
    if ((r & 7) < 5) {
      *(u32x4*)(lds + bo) = bt0;
      *(u32x4*)(lds + (bo ^ 16)) = bt1;
    }
    barrier();
    const u32x4 vb0 = *(const u32x4*)(lds + L_BS + tid * 16);
    const u32x4 vb1 = *(const u32x4*)(lds + L_BS + 8192 + min(tid, 143) * 16);
    __builtin_amdgcn_raw_buffer_store_b128(vb0, wsr, gbo, 0, 16);
    if (tid < 16 * (nsz - 32) || (tid >> 4) == 8)
      __builtin_amdgcn_raw_buffer_store_b128(vb1, wsr, tid < 128 ? gbo + 8192 : WS_BT + (unsigned)((NN + w) * 256 + (tid & 15) * 16), 0, 16);
    asm volatile("" ::: "memory");
    acc = zero16();
#pragma unroll
    for (int ks = 0; ks < 8; ks++) {
      acc = mfma(hf[ks], wf[ks], acc);
      PIN(acc);
    }
#pragma unroll
    for (int i = 0; i < 4; i++) {
      at[i][0] = pk2(acc[4 * i] * NLOG2E, acc[4 * i + 1] * NLOG2E);
      at[i][1] = pk2(acc[4 * i + 2] * NLOG2E, acc[4 * i + 3] * NLOG2E);
      PIN(at[i]);
    }
  }
  {
    const u32x4 z = {0u, 0u, 0u, 0u};
    *(u32x4*)(lds + L_S + tid * 16) = z;
    *(u32x4*)(lds + L_S + 8192 + tid * 16) = z;
    if (tid < 256) {
      *(u32x4*)(lds + L_S + 16384 + tid * 16) = z;
    } else {
      const int t = tid - 256, v = t >> 5, q = (t >> 3) & 3, i = t & 7;
      *(u32*)(lds + L_FK + t * 4) = (unsigned)(NN + (i < 4 ? 128 + 32 * q + 4 * v + i : 16 * v + 4 * (i - 4) + q)) << 8;
    }
  }
  u32x4 extB[4];
#pragma unroll
  for (int t = 0; t < 4; t++) {
    const u16* wp = (const u16*)(lds + L_EXT) + 32 * t + r;
    const float w0 = bf2f(wp[(1 + 2 * hh) * 128]) * NLOG2E, w1 = bf2f(wp[(2 + 2 * hh) * 128]) * NLOG2E, w2 = bf2f(wp[0]) * NLOG2E;
    extB[t][0] = 0u;
    extB[t][1] = 0u;
    extB[t][2] = pk2(w0, w1);
    extB[t][3] = hh ? 0u : pk2(w2, 0.f);
    PIN(extB[t]);
  }
  asm volatile("global_load_dwordx2 %0, %1, %2 sc1\n\ts_waitcnt vmcnt(0)" : "=&v"(ent) : "v"(WS_TAB + (unsigned)((tid >> 1) * NB + b) * 8), "s"(ws) : "memory");
  barrier();
  if (tid == 0) __builtin_amdgcn_raw_buffer_store_b32(tag, wsr, WS_FB + (unsigned)w * 4, 0, 16);
#pragma unroll
  for (int i = 0; i < 4; i++) *(u32x2*)(lds + L_A + (4 * nb + i) * 2048 + T * 512 + lane * 8) = at[i];

  bool wh_loaded = false, slow = false;
  int lo = 0, quota = MAPW, gq = wv;
  unsigned pin = 0, atmp = 0;
  while (true) {
    wh_loaded = false;
    int tq = tid;
    PIN(tq);
    const int lq = tq & 63;
    int kc, kfirst, kpre, wtot;
    {
      const unsigned tabq = WS_TAB + (unsigned)((tq >> 1) * NB + b) * 8;
      u32x2 gq2 = ent;
      ent[0] = tag + 1u;
#pragma clang loop unroll(disable)
      for (int spin = 0; spin < (1 << 22); spin++) {
        if (gq2[0] != tag) asm volatile("global_load_dwordx2 %0, %1, %2 sc1\n\ts_waitcnt vmcnt(0)" : "=&v"(gq2) : "v"(tabq), "s"(ws) : "memory");
        if (__all(gq2[0] == tag)) break;
        __builtin_amdgcn_s_sleep(1);
      }
      const int o0 = min((int)(gq2[1] & 0xffffu), CH), o1 = min((int)(gq2[1] >> 16), CH);
      kc = max(o1 - o0, 0);
      kfirst = (tq >> 1) * CHS + o0;
      const int inc = wave_incl_scan((tq & 1) ? 0 : kc);
      kpre = inc - kc;
      wtot = __builtin_amdgcn_readlane(inc, 63);
      if (!slow && lq == 0) atomicMax((unsigned*)(lds + L_GC + 32), (unsigned)wtot);
    }
    u32x3 q[4];
    u32x2 pd[4], ps[4];
    int ra1[4], par[4];
    bool four;
    {
      const int map = L_MAP + wv * MAPW * 4;
      const int nw = min(wtot - lo, quota);
      const int jhi = min(kc, lo + quota - kpre);
#pragma clang loop vectorize(disable) unroll(disable)
      for (int j = max(0, lo - kpre) + ((tq ^ max(0, lo - kpre)) & 1); j < jhi; j += 2) *(u32*)(lds + map + (kpre + j - lo) * 4) = (unsigned)(kfirst + j) * 16u;
      unsigned ix[4];
#pragma unroll
      for (int k = 0; k < 4; k++) {
        const unsigned mi = *(const u32*)(lds + map + (64 * k + lq) * 4);
        ix[k] = 64 * k + lq < nw ? mi : 0u;
      }
      u32x4 q4_[4];
      unsigned fb;
      const unsigned fbo = WS_FB + (unsigned)(32 * wv + (lq & 31)) * 4;
#pragma clang loop unroll(disable)
      for (int spin = 0; spin < (1 << 22); spin++) {
        asm volatile("global_load_dwordx4 %0, %5, %9 sc1\n\tglobal_load_dwordx4 %1, %6, %9 sc1\n\tglobal_load_dwordx4 %2, %7, %9 sc1\n\t"
                     "global_load_dwordx4 %3, %8, %9 sc1\n\tglobal_load_dword %4, %10, %11 sc1\n\ts_waitcnt vmcnt(0)"
                     : "=&v"(q4_[0]), "=&v"(q4_[1]), "=&v"(q4_[2]), "=&v"(q4_[3]), "=&v"(fb)
                     : "v"(ix[0]), "v"(ix[1]), "v"(ix[2]), "v"(ix[3]), "s"(ws + WS_R), "v"(fbo), "s"(ws) : "memory");
        bool ok = true;
#pragma unroll
        for (int k = 0; k < 4; k++) {
          const unsigned bad = (((q4_[k][1] ^ (tag << 16)) | (q4_[k][2] ^ (tag << 16))) >> 16) | ((q4_[k][3] ^ ((tag >> 16) << 22)) >> 22);
          ok = ok && (bad == 0 || 64 * k + lq >= nw);
        }
        if (__all(ok)) break;
      }
#pragma unroll
      for (int k = 0; k < 4; k++) {
        q[k][0] = q4_[k][0];
        q[k][1] = (q4_[k][1] & 0xffffu) | (q4_[k][2] << 16);
        q[k][2] = q4_[k][3] & 0x3fffffu;
      }
      unsigned rk[4];
#pragma unroll
      for (int k = 0; k < 4; k++) {
        rk[k] = CAPW;
        if (64 * k + lq < nw) rk[k] = atomicAdd((unsigned*)(lds + L_GC) + (q[k][2] & 7u), 1u);
        const int dn = n0 + 5 * (int)(q[k][2] & 7u) + (int)((q[k][2] >> 4) & 7u), sn = min((int)(q[k][2] >> 8), NN - 1);
        const int od = L_ROWS + ((6 * dn) & ~3), os = L_ROWS + ((6 * sn) & ~3);
        pd[k][0] = *(const u32*)(lds + od); pd[k][1] = *(const u32*)(lds + od + 4);
        ps[k][0] = *(const u32*)(lds + os); ps[k][1] = *(const u32*)(lds + os + 4);
        par[k] = (dn & 1) * 16;
      }
#pragma unroll
      for (int k = 0; k < 4; k++) {
        const bool ok = rk[k] < CAPW;
        const int e = (int)((q[k][2] & 7u) * CAPW + rk[k]);
        *(u32*)(lds + (ok ? L_R2 + e * 4 : L_DUM + 96)) = q[k][2];
        ra1[k] = ok ? L_R1 + e * 16 : L_DUM + 96;
      }
      four = nw > 192;
#pragma clang loop unroll(disable)
      for (int spin = 0; spin < (1 << 22) && !__all(fb == tag); spin++) {
        __builtin_amdgcn_s_sleep(1);
        asm volatile("global_load_dword %0, %1, %2 sc1\n\ts_waitcnt vmcnt(0)" : "=&v"(fb) : "v"(fbo), "s"(ws) : "memory");
      }
    }
    barrier();
    const int cl = *(const u32*)(lds + L_GC + (lane & 7) * 4);
    const int maxtot = __builtin_amdgcn_readfirstlane(*(const u32*)(lds + L_GC + 32));
    if (!slow) {
      const int kk = (cl << 3) | (lane & 7);
      int rk = 0;
      rk += __builtin_amdgcn_update_dpp(0, kk, 0x121, 0xf, 0xf, false) > kk;
      rk += __builtin_amdgcn_update_dpp(0, kk, 0x122, 0xf, 0xf, false) > kk;
      rk += __builtin_amdgcn_update_dpp(0, kk, 0x123, 0xf, 0xf, false) > kk;
      rk += __builtin_amdgcn_update_dpp(0, kk, 0x124, 0xf, 0xf, false) > kk;
      rk += __builtin_amdgcn_update_dpp(0, kk, 0x125, 0xf, 0xf, false) > kk;
      rk += __builtin_amdgcn_update_dpp(0, kk, 0x126, 0xf, 0xf, false) > kk;
      rk += __builtin_amdgcn_update_dpp(0, kk, 0x127, 0xf, 0xf, false) > kk;
      const int cmax = __any(cl > CAPW) ? CAPW + 1 : 0;
      if (maxtot > MAPW || cmax > CAPW) {
        slow = true;
        quota = CAPW / 8;
        barrier();
        if (tid < 8) *(u32*)(lds + L_GC + tid * 4) = 0u;
        barrier();
        continue;
      }
      gq = __builtin_ctzll(__ballot(rk == (wv < 4 ? wv : 11 - wv)));
    }
    const int n = __builtin_amdgcn_readlane(cl, gq);
    const unsigned ohc = (unsigned)(L_OH + hh * 8);
    const int swr = swr0 + gq * 2560 - 20 * (int)ohc, srd = srd0 + gq * 2560;
    const int l1 = L_R1 + gq * CAPW * 16, l2 = L_R2 + gq * CAPW * 4;
    const int nt = (n + 31) >> 5;
    u32x4 rows[8], extA, S0, S1, ra, rb, m1p = {0u, 0u, 0u, 0u};
    unsigned r2;
    int sp;
    f32x16 XA, XB;
    auto list_read = [&](int tl) {
      const int list = tl < nt ? l2 + tl * 128 : L_FK + wv * 128;
      ra = *(const u32x4*)(lds + list + (lane >> 4) * 32);
      rb = *(const u32x4*)(lds + list + (lane >> 4) * 32 + 16);
    };
    auto dma_piece = [&](int, int i) {
      if (i == 0) row_dma<0>(bsrd, ra[0], kdma, lbase + rowbuf, atmp);
      if (i == 1) row_dma<1>(bsrd, ra[1], kdma, lbase + rowbuf, atmp);
      if (i == 2) row_dma<2>(bsrd, ra[2], kdma, lbase + rowbuf, atmp);
      if (i == 3) row_dma<3>(bsrd, ra[3], kdma, lbase + rowbuf, atmp);
      if (i == 4) row_dma<4>(bsrd, rb[0], kdma, lbase + rowbuf, atmp);
      if (i == 5) row_dma<5>(bsrd, rb[1], kdma, lbase + rowbuf, atmp);
      if (i == 6) row_dma<6>(bsrd, rb[2], kdma, lbase + rowbuf, atmp);
      if (i == 7) row_dma<7>(bsrd, rb[3], kdma, lbase + rowbuf, atmp);
    };
    auto meta_read = [&](int tl) {
      const u32x2 r1 = *(const u32x2*)(lds + l1 + (tl * 32 + r) * 16 + hh * 8);
      extA[2] = r1[0];
      extA[3] = r1[1];
      r2 = *(const u32*)(lds + l2 + (tl * 32 + r) * 4);
    };
    auto meta_use = [&](int) {
      const unsigned oa = (r2 & 0x70u) | ohc;
      const u32x2 oh = *(const u32x2*)(lds + oa);
      extA[0] = oh[0];
      extA[1] = oh[1];
      sp = (int)__umul24(oa, 20u) + swr;
    };
    auto s_write = [&]() {
      *(u16*)(lds + sp) = (u16)(hh ? extA[3] : 0x3F80u);
      *(u16*)(lds + sp + 80) = (u16)(extA[3] >> 16);
    };
    auto rows_read = [&]() {
#pragma unroll
      for (int j = 0; j < 8; j++) rows[j] = *(const u32x4*)(lds + (j < 4 ? rdA : rdB) + 16 * (j & 3));
    };
    auto rows_pin = [&]() {
      asm volatile("" : "+v"(rows[0]), "+v"(rows[1]), "+v"(rows[2]), "+v"(rows[3]), "+v"(rows[4]), "+v"(rows[5]), "+v"(rows[6]), "+v"(rows[7]));
    };

    list_read(0);
    if (n > 0 && wv < 4) {
#pragma unroll
      for (int i = 0; i < 8; i++) dma_piece(0, i);
    }
    auto finish = [&](int k) {
      const unsigned long long ud = (((unsigned long long)pd[k][1] << 32) | pd[k][0]) >> par[k];
      const unsigned long long us = (((unsigned long long)ps[k][1] << 32) | ps[k][0]) >> ((q[k][2] >> 4) & 16u);
      const unsigned dlo = (unsigned)ud, dhi = (unsigned)(ud >> 32), slo = (unsigned)us, shi = (unsigned)(us >> 32);
      const unsigned dxy = pk2(bf2f(dlo) - bf2f(slo), bfhi(dlo) - bfhi(slo));
      const unsigned dz_ = pk2(bf2f(dhi) - bf2f(shi), 0.f);
      const float dx = bf2f(dxy), dy = bfhi(dxy), dz = bf2f(dz_);
      const unsigned sxy = pk2(dx * dx, dy * dy), sz = pk2(dz * dz, 0.f);
      const unsigned d2 = pk2((bf2f(sxy) + bfhi(sxy)) + bf2f(sz), 0.f) & 0xffffu;
      const u32x4 rec = {q[k][0], d2 | (dxy << 16), q[k][1], (dxy >> 16) | (dz_ << 16)};
      *(u32x4*)(lds + ra1[k]) = rec;
    };
    finish(0);
    finish(1);
    finish(2);
    if (four) finish(3);
    if (lane < 32 * nt - n) {
      const u32x4 z = {0u, 0u, 0u, 0u};
      *(u32x4*)(lds + l1 + (n + lane) * 16) = z;
      *(u32*)(lds + l2 + (n + lane) * 4) = 0x70u;
    }
    if (w == 0 && tid == 0) __builtin_amdgcn_raw_buffer_store_b32(tag, wsr, WS_EP, 0, 16);
    pin = pos[min(3 * n0 + 15 * gq + (lq & 15), 3 * NN - 1)];
#pragma unroll
    for (int t = 0; t < 4; t++) {
      const u32x2 v = *(const u32x2*)(lds + L_A + gq * 2048 + t * 512 + lq * 8);
      extB[t][0] = v[0];
      extB[t][1] = v[1];
    }
    barrier();
    if (n > 0) {
    if (wv >= 4) {
#pragma unroll
      for (int i = 0; i < 8; i++) dma_piece(0, i);
    }
    meta_read(0);
    list_read(1);
    meta_use(0);
    s_write();
    S1 = m1p;
    wait_vm0();
    PIN(pin);
    rows_read();
    rows_pin();
    XA = mfma(rows[0], I0, zero16());
    XA = mfma(rows[1], I1, XA);
    XA = mfma(extA, extB[0], XA);

    const int nfull = (n + 15) >> 5;
    int first = 1;
    for (int tl = 0; tl < nfull; tl++) {
      asm volatile("" : "+s"(first));
#pragma unroll
      for (int t = 0; t < 4; t++) {
        f32x16& Xc = (t & 1) ? XB : XA;
        f32x16& Xn = (t & 1) ? XA : XB;
        const int tn = (t + 1) & 3;
        auto gap = [&](int k) {
          SB();
          if (t == 0 && k == 0 && first) {
#pragma unroll
            for (int i = 0; i < 8; i++) dma_piece(1, i);
          }
          if (t == 0 && k == 0) S0 = *(const u32x4*)(lds + srd);
          if (t == 0 && k == 3) S1 = *(const u32x4*)(lds + srd + 32);
          if (t == 0 && k == 4) {
            *(u16*)(lds + sp) = 0;
            *(u16*)(lds + sp + 80) = 0;
          }
          if (t == 2 && k == 1) meta_read(tl + 1);
          if (t == 2 && k == 4) meta_use(tl + 1);
          if (t == 2 && k == 3) wait_vm0();
          if (t == 2 && k >= 3 && k < 7) {
            rows[2 * k - 6] = *(const u32x4*)(lds + (k < 5 ? rdA : rdB) + 16 * ((2 * k - 6) & 3));
            rows[2 * k - 5] = *(const u32x4*)(lds + (k < 5 ? rdA : rdB) + 16 * ((2 * k - 5) & 3));
          }
          if (t == 2 && k == 7) list_read(tl + 2);
          if (t == 3 && k < 8) dma_piece(tl + 2, k);
          if (t == 3 && k == 8) s_write();
          SB();
        };
        f32x2 dd[8];
        if (t == 3 && wv >= 4) __builtin_amdgcn_s_setprio(1);
        if (t == 2 && wv >= 4) __builtin_amdgcn_s_setprio(0);
        SB();
        Xn = mfma(rows[2 * tn], I0, zero16());
        PIN(Xn);
        SB();
#pragma unroll
        for (int g = 0; g < 4; g++) {
#pragma unroll
          for (int i = 2 * g; i < 2 * g + 2; i++) {
            dd[i][0] = __builtin_amdgcn_exp2f(Xc[2 * i]);
            dd[i][1] = __builtin_amdgcn_exp2f(Xc[2 * i + 1]);
            PIN(dd[i]);
          }
          SB();
          if (g == 0) { Xn = mfma(rows[2 * tn + 1], I1, Xn); PIN(Xn); }
          if (g == 1) { Xn = mfma(extA, extB[tn], Xn); PIN(Xn); }
          if (g == 2) { Y[(t + 3) & 3] = mfma(S1, m1p, Y[(t + 3) & 3]); PIN(Y[(t + 3) & 3]); }
          gap(g);
        }
#pragma unroll
        for (int i = 0; i < 8; i++) { dd[i] += 1.0f; if (i < 7) PIN(dd[i]); }
        gap(4);
#pragma unroll
        for (int g = 0; g < 4; g++) {
#pragma unroll
          for (int i = 2 * g; i < 2 * g + 2; i++) {
            dd[i][0] = __builtin_amdgcn_rcpf(dd[i][0]);
            dd[i][1] = __builtin_amdgcn_rcpf(dd[i][1]);
            PIN(dd[i]);
          }
          gap(5 + g);
        }
        u32x4 m0;
#pragma unroll
        for (int i = 0; i < 4; i++) { f32x2 x = {Xc[2 * i], Xc[2 * i + 1]}; dd[i] *= x; }
#pragma unroll
        for (int i = 0; i < 4; i++) m0[i] = __builtin_bit_cast(unsigned, __builtin_convertvector(dd[i], bf16x2));
        SB();
#pragma unroll
        for (int i = 4; i < 8; i++) { f32x2 x = {Xc[2 * i], Xc[2 * i + 1]}; dd[i] *= x; if (i < 7) PIN(dd[i]); }
        SB();
        Y[t] = mfma(S0, m0, Y[t]);
        PIN(Y[t]);
        SB();
#pragma unroll
        for (int i = 0; i < 4; i++) m1p[i] = __builtin_bit_cast(unsigned, __builtin_convertvector(dd[4 + i], bf16x2));
        PIN(m1p);
        if (t == 2) rows_pin();
      }
      first = 0;
    }
    SB();
    Y[3] = mfma(S1, m1p, Y[3]);
    S0 = *(const u32x4*)(lds + srd);
    *(u16*)(lds + sp) = 0;
    *(u16*)(lds + sp + 80) = 0;
    if (nfull < nt) {
#pragma unroll
      for (int t = 0; t < 4; t++) {
        f32x16& Xc = (t & 1) ? XB : XA;
        f32x16& Xn = (t & 1) ? XA : XB;
        f32x2 dd[4];
        if (t < 2 && nfull == 0) {
#pragma unroll
          for (int i = 0; i < 4; i++) dma_piece(nt, 4 * t + i);
        }
        SB();
        if (t < 3) { Xn = mfma(rows[2 * t + 2], I0, zero16()); PIN(Xn); }
        SB();
#pragma unroll
        for (int i = 0; i < 2; i++) { dd[i][0] = __builtin_amdgcn_exp2f(Xc[2 * i]); dd[i][1] = __builtin_amdgcn_exp2f(Xc[2 * i + 1]); }
        SB();
        if (t < 3) { Xn = mfma(rows[2 * t + 3], I1, Xn); PIN(Xn); }
        SB();
#pragma unroll
        for (int i = 2; i < 4; i++) { dd[i][0] = __builtin_amdgcn_exp2f(Xc[2 * i]); dd[i][1] = __builtin_amdgcn_exp2f(Xc[2 * i + 1]); }
        SB();
        if (t < 3) { Xn = mfma(extA, extB[t + 1], Xn); PIN(Xn); }
        SB();
#pragma unroll
        for (int i = 0; i < 4; i++) dd[i] += 1.0f;
        SB();
#pragma unroll
        for (int i = 0; i < 4; i++) { dd[i][0] = __builtin_amdgcn_rcpf(dd[i][0]); dd[i][1] = __builtin_amdgcn_rcpf(dd[i][1]); }
        SB();
        u32x4 m0;
#pragma unroll
        for (int i = 0; i < 4; i++) { f32x2 x = {Xc[2 * i], Xc[2 * i + 1]}; dd[i] *= x; }
#pragma unroll
        for (int i = 0; i < 4; i++) m0[i] = __builtin_bit_cast(unsigned, __builtin_convertvector(dd[i], bf16x2));
        SB();
        Y[t] = mfma(S0, m0, Y[t]);
        if (t < 3) PIN(Xn);
      }
    }
    wh_loaded = true;
    wait_vm0();
    }
    if (!slow) break;
    lo += quota;
    if (lo >= maxtot) break;
    barrier();
    if (tid < 8) *(u32*)(lds + L_GC + tid * 4) = 0u;
#pragma unroll
    for (int i = 0; i < 8; i++) pos_tile(i);
    wait_vm0();
    barrier();
  }
  if (!wh_loaded) {
    wait_vm0();
    PIN(pin);
    const u32x4 fa = *(const u32x4*)(lds + L_FK + wv * 128 + (lane >> 4) * 32), fb = *(const u32x4*)(lds + L_FK + wv * 128 + (lane >> 4) * 32 + 16);
    row_dma<0>(bsrd, fa[0], kdma, lbase + rowbuf, atmp); row_dma<1>(bsrd, fa[1], kdma, lbase + rowbuf, atmp);
    row_dma<2>(bsrd, fa[2], kdma, lbase + rowbuf, atmp); row_dma<3>(bsrd, fa[3], kdma, lbase + rowbuf, atmp);
    row_dma<4>(bsrd, fb[0], kdma, lbase + rowbuf, atmp); row_dma<5>(bsrd, fb[1], kdma, lbase + rowbuf, atmp);
    row_dma<6>(bsrd, fb[2], kdma, lbase + rowbuf, atmp); row_dma<7>(bsrd, fb[3], kdma, lbase + rowbuf, atmp);
    wait_vm0();
  }

#pragma unroll
  for (int i = 0; i < 3; i++) {
    if (i < 2 || hh == 0) {
      const u32x2 mv = {pk2(Y[0][4 * i] * NLN2, Y[1][4 * i] * NLN2), pk2(Y[2][4 * i] * NLN2, Y[3][4 * i] * NLN2)};
      *(u32x2*)(lds + L_M + (5 * gq + 2 * i + hh) * MSTR + 8 * r) = mv;
    }
  }
  u32x4 hq[8], res[2];
  unsigned wxr[4];
#pragma unroll
  for (int ks = 0; ks < 8; ks++) hq[ks] = *(const u32x4*)(lds + (ha ^ (32 * ks)));
  {
    const int ra_ = (ha ^ (16 * hh)) ^ (64 * T + 32 * hh);
    res[0] = *(const u32x4*)(lds + ra_);
    res[1] = *(const u32x4*)(lds + (ra_ ^ 16));
  }
#pragma unroll
  for (int t = 0; t < 4; t++) wxr[t] = ((const u16*)(lds + L_WX))[32 * t + r];
  unsigned wa = lbase + L_ROWS + 2080 * hh + 256 * q4 + 64 * (T ^ q4) + 32 * (p4 & 1) + 16 * blk + 8 * (p4 >> 1);
  int ma = L_M + min(loc, 39) * MSTR + 16 * hh;
  PIN(wa);
  PIN(ma);
  barrier();
  {
    u32x4 w1[8], w2[8], mq[8];
#pragma unroll
    for (int ks = 0; ks < 8; ks++) w1[ks] = tr_read2(wa + ks * ROWBUF + 4352, wa + ks * ROWBUF + 4352 + 1040);
#pragma unroll
    for (int ks = 0; ks < 8; ks++) {
      w2[ks] = tr_read2(wa + ks * ROWBUF, wa + ks * ROWBUF + 1040);
      mq[ks] = *(const u32x4*)(lds + ma + 32 * ks);
    }
    SB();
#define DPP9(ctl)                                                                                                         \
  asm volatile("s_nop 1\n\tv_add_f32_dpp %0, %0, %0 " ctl "\n\tv_add_f32_dpp %1, %1, %1 " ctl "\n\tv_add_f32_dpp %2, %2, %2 " ctl "\n\t"    \
               "v_add_f32_dpp %3, %3, %3 " ctl "\n\tv_add_f32_dpp %4, %4, %4 " ctl "\n\tv_add_f32_dpp %5, %5, %5 " ctl "\n\t"              \
               "v_add_f32_dpp %6, %6, %6 " ctl "\n\tv_add_f32_dpp %7, %7, %7 " ctl "\n\tv_add_f32_dpp %8, %8, %8 " ctl "\n\ts_nop 0"       \
               : "+v"(zz[0]), "+v"(zz[1]), "+v"(zz[2]), "+v"(zz[3]), "+v"(zz[4]), "+v"(zz[5]), "+v"(zz[6]), "+v"(zz[7]), "+v"(zz[8]))
    f32x16 P = zero16();
    float zz[9], zsum = 0.f;
    const int zp = L_S + gq * 2560;
#pragma unroll
    for (int ks = 0; ks < 16; ks++) {
      P = mfma(ks < 8 ? w1[ks & 7] : w2[ks & 7], ks < 8 ? hq[ks & 7] : mq[ks & 7], P);
      PIN(P);
      SB();
      if (ks < 4) {
        const float wxv = bf2f(wxr[ks]) * NLN2;
#pragma unroll
        for (int i = 0; i < 9; i++) {
          const float y = Y[ks][4 * (i / 3) + 1 + i % 3];
          zz[i] = ks == 0 ? y * wxv : __builtin_fmaf(y, wxv, zz[i]);
          PIN(zz[i]);
        }
      }
      if (ks == 4) DPP9("row_shr:1 row_mask:0xf bank_mask:0xf bound_ctrl:1");
      if (ks == 5) DPP9("row_shr:2 row_mask:0xf bank_mask:0xf bound_ctrl:1");
      if (ks == 6) DPP9("row_shr:4 row_mask:0xf bank_mask:0xf bound_ctrl:1");
      if (ks == 7) DPP9("row_shr:8 row_mask:0xf bank_mask:0xf bound_ctrl:1");
      if (ks == 8) DPP9("row_bcast:15 row_mask:0xa bank_mask:0xf");
      if (ks == 9) {
        if (r == 31) {
#pragma unroll
          for (int i = 0; i < 9; i++) *(u32*)(lds + zp + 12 * hh + 24 * (i / 3) + 4 * (i % 3)) = __float_as_uint(zz[i]);
        }
        asm volatile("" ::: "memory");
        zsum = __uint_as_float(*(const u32*)(lds + zp + 4 * (lane & 15)));
      }
      if (ks == 13 && lane < 15 && 15 * gq + lane < 3 * nsz)
        out[(size_t)NN * 128 + (unsigned)(3 * n0 + 15 * gq + lane)] = (u16)pk2(bf2f(pin) + zsum, 0.f);
      SB();
    }
    SB();
    f32x2 x[8], e[8], rv[8];
#pragma unroll
    for (int i = 0; i < 8; i++) {
      const unsigned rs = res[i >> 2][i & 3];
      rv[i][0] = bf2f(rs);
      rv[i][1] = bfhi(rs);
      PIN(rv[i]);
    }
    SB();
#pragma unroll
    for (int i = 0; i < 8; i++) {
      x[i][0] = P[2 * i];
      x[i][1] = P[2 * i + 1];
      e[i] = x[i] * NLOG2E;
      if (i < 7) PIN(e[i]);
    }
    SB();
#pragma unroll
    for (int i = 0; i < 8; i++) { e[i][0] = __builtin_amdgcn_exp2f(e[i][0]); e[i][1] = __builtin_amdgcn_exp2f(e[i][1]); PIN(e[i]); }
    SB();
#pragma unroll
    for (int i = 0; i < 8; i++) { e[i] += 1.0f; if (i < 7) PIN(e[i]); }
    SB();
#pragma unroll
    for (int i = 0; i < 8; i++) { e[i][0] = __builtin_amdgcn_rcpf(e[i][0]); e[i][1] = __builtin_amdgcn_rcpf(e[i][1]); PIN(e[i]); }
    SB();
#pragma unroll
    for (int i = 0; i < 8; i++) { e[i] = __builtin_elementwise_fma(x[i], e[i], rv[i]); if (i < 7) PIN(e[i]); }
    SB();
    u32x4 ov[2];
#pragma unroll
    for (int i = 0; i < 8; i++) ov[i >> 2][i & 3] = pk2(e[i][0], e[i][1]);
    PIN(ov[0]);
    PIN(ov[1]);
    if (valid) {
      const __amdgpu_buffer_rsrc_t osr = __builtin_amdgcn_make_buffer_rsrc(out, 0, NN * 262, 0x00020000);
      const unsigned oo = (unsigned)(n0 + loc) * 256 + 64 * T + 32 * hh;
      __builtin_amdgcn_raw_buffer_store_b128(ov[0], osr, oo, 0, 0);
      __builtin_amdgcn_raw_buffer_store_b128(ov[1], osr, oo + 16, 0, 0);
    }
  }
}

extern "C" void kernel_launch(void* const* d_in, const int* in_sizes, int n_in, void* d_out, int out_size, void* d_ws,
                              size_t ws_size, hipStream_t stream) {
  egnn_fused<<<NB, 512, 0, stream>>>((const u16*)d_in[0], (const u16*)d_in[1], (const u32x2*)d_in[2], (const int*)d_in[3],
                                     (const int*)d_in[4], (const u16*)d_in[5], (const u16*)d_in[6], (const u16*)d_in[7],
                                     (char*)d_ws, (u16*)d_out);
}
```

```cpp
#include <hip/hip_runtime.h>
#include <hip/hip_bf16.h>
#include <stdint.h>

typedef __bf16 bf16x8 __attribute__((ext_vector_type(8)));
typedef __bf16 bf16x2 __attribute__((ext_vector_type(2)));
typedef short s16x4 __attribute__((ext_vector_type(4)));
typedef float f32x2 __attribute__((ext_vector_type(2)));
typedef float f32x16 __attribute__((ext_vector_type(16)));
typedef unsigned u32x2 __attribute__((ext_vector_type(2), may_alias));
typedef unsigned u32x3 __attribute__((ext_vector_type(3)));
typedef unsigned u32x4 __attribute__((ext_vector_type(4), may_alias));
typedef unsigned short u16 __attribute__((may_alias));
typedef unsigned u32 __attribute__((may_alias));
#define LDSP(T) __attribute__((address_space(3))) T*
#define DI __device__ __forceinline__
#define SB() __builtin_amdgcn_sched_barrier(0)
#define PIN(x) asm volatile("" : "+v"(x))

constexpr int NN = 10000;
constexpr int CH = 1250, CHS = 1280;
constexpr int NB = 256;
constexpr float NLOG2E = -1.4426950408889634f;
constexpr float NLN2 = -0.6931471805599453f;

constexpr unsigned WS_BT = 4096;
constexpr unsigned WS_R = WS_BT + (NN + 256) * 256;
constexpr unsigned WS_TAB = WS_R + NB * CHS * 16;
constexpr unsigned WS_FB = WS_TAB + NB * NB * 8;
constexpr unsigned WS_EP = WS_FB + NB * 4;
constexpr unsigned WS_END = WS_EP + 256;

constexpr int CAPW = 224;
constexpr int ROWBUF = 8496;
constexpr int L_ROWS = 0;
constexpr int L_R1 = 8 * ROWBUF;
constexpr int L_R2 = L_R1 + 8 * CAPW * 16;
constexpr int L_S = L_R2 + 8 * CAPW * 4;
constexpr int L_M = L_S + 8 * 2560;
constexpr int MSTR = 272;
constexpr int L_H = 135168;
constexpr int L_A = L_H + 10240;
constexpr int L_HIST = L_A + 8 * 2048;
constexpr int L_OH = L_HIST + 1024;
constexpr int L_GC = L_OH + 128;
constexpr int L_DUM = L_GC + 64;
constexpr int L_WX = L_DUM + 128;
constexpr int L_END = L_WX + 256;
constexpr int L_W = L_R1;
constexpr int L_EXT = L_W + 65536;
constexpr int L_ST = 47104;
constexpr int L_BS = L_A;
constexpr int L_OFF = L_A + 10496;
constexpr int MAPW = 256;
constexpr int L_MAP = L_M;
constexpr int L_FK = L_HIST;
static_assert(L_OH % 128 == 0 && L_ST + 20480 <= L_R1 && L_BS % 256 == 0 && L_W % 32 == 0 && L_W + 65536 <= L_EXT && L_EXT + 1280 <= L_H && L_H % 256 == 0 && L_M + 40 * MSTR <= L_H && L_END <= 163840, "lds map");

DI constexpr int piece_off(int i) { return 1040 * (i & 3) + 4352 * (i >> 2); }
DI float bf2f(unsigned u) { return __uint_as_float(u << 16); }
DI float bfhi(unsigned u) { return __uint_as_float(u & 0xffff0000u); }
DI unsigned pk2(float a, float b) {
  f32x2 v = {a, b};
  return __builtin_bit_cast(unsigned, __builtin_convertvector(v, bf16x2));
}
DI f32x16 mfma(u32x4 a, u32x4 b, f32x16 c) {
  return __builtin_amdgcn_mfma_f32_32x32x16_bf16(__builtin_bit_cast(bf16x8, a), __builtin_bit_cast(bf16x8, b), c, 0, 0, 0);
}
DI f32x16 zero16() {
  f32x16 z;
#pragma unroll
  for (int i = 0; i < 16; i++) z[i] = 0.f;
  return z;
}
DI u32x4 tr_read2(unsigned a0, unsigned a1) {
  const u32x2 lo = __builtin_bit_cast(u32x2, __builtin_amdgcn_ds_read_tr16_b64_v4i16((LDSP(s16x4))(size_t)a0));
  const u32x2 hi = __builtin_bit_cast(u32x2, __builtin_amdgcn_ds_read_tr16_b64_v4i16((LDSP(s16x4))(size_t)a1));
  const u32x4 v = {lo[0], lo[1], hi[0], hi[1]};
  return v;
}
DI int bucket_start(int b) { return (b * 625 + 15) >> 4; }
DI int wave_incl_scan(int x) {
  x += __builtin_amdgcn_update_dpp(0, x, 0x111, 0xf, 0xf, false);
  x += __builtin_amdgcn_update_dpp(0, x, 0x112, 0xf, 0xf, false);
  x += __builtin_amdgcn_update_dpp(0, x, 0x114, 0xf, 0xf, false);
  x += __builtin_amdgcn_update_dpp(0, x, 0x118, 0xf, 0xf, false);
  x += __builtin_amdgcn_update_dpp(0, x, 0x142, 0xa, 0xf, false);
  x += __builtin_amdgcn_update_dpp(0, x, 0x143, 0xc, 0xf, false);
  return x;
}
DI void dma16(const void* sbase, unsigned voff, unsigned lds_base) {
  asm volatile("s_mov_b32 m0, %2\n\ts_nop 0\n\tglobal_load_lds_dwordx4 %0, %1" ::"v"(voff), "s"(sbase), "s"(lds_base) : "memory");
}
template <int I>
DI void row_dma(u32x4 srd, unsigned key, unsigned lanec, unsigned lds_base, unsigned& t) {
  if (I & 3)
    asm volatile("v_and_or_b32 %0, %1, %2, %3\n\tbuffer_load_dwordx4 %0, %4, %5 offen offset:%6 sc1 lds"
                 : "+v"(t) : "v"(key), "s"(0xffffff00u), "v"(lanec), "s"(srd), "s"(3120 - 1040 * (I & 3)), "i"(1040 * (I & 3)) : "memory");
  else
    asm volatile("s_mov_b32 m0, %6\n\tv_and_or_b32 %0, %1, %2, %3\n\tbuffer_load_dwordx4 %0, %4, %5 offen sc1 lds"
                 : "+v"(t) : "v"(key), "s"(0xffffff00u), "v"(lanec), "s"(srd), "s"(3120), "s"(lds_base + 4352 * (I >> 2)) : "memory");
}
DI void wait_vm0() { asm volatile("s_waitcnt vmcnt(0)" ::: "memory"); }
DI void barrier() { asm volatile("s_waitcnt lgkmcnt(0)\n\ts_barrier" ::: "memory"); }

__global__ __launch_bounds__(512, 2) void egnn_fused(const u16* __restrict__ h, const u16* __restrict__ pos,
                                                     const u32x2* __restrict__ eattr, const int* __restrict__ src,
                                                     const int* __restrict__ dst, const u16* __restrict__ we,
                                                     const u16* __restrict__ wx, const u16* __restrict__ wh,
                                                     char* ws, u16* __restrict__ out) {
  __shared__ __attribute__((aligned(256))) char lds[L_END];
  const unsigned lbase = (unsigned)(size_t)lds;
  const int tid = threadIdx.x, lane = tid & 63;
  const int wv = __builtin_amdgcn_readfirstlane(tid >> 6);
  const int w = blockIdx.x;
  const int b = (w & 7) * 32 + (w >> 3);
  const int n0 = bucket_start(b), nsz = bucket_start(b + 1) - n0;
  const int r = lane & 31, hh = lane >> 5;
  const int blk = (lane >> 4) & 1, q4 = (lane & 15) >> 2, p4 = lane & 3;
  const __amdgpu_buffer_rsrc_t wsr = __builtin_amdgcn_make_buffer_rsrc(ws, 0, WS_END, 0x00020000);

  auto pos_tile = [&](int i) {
    const int j = i < 5 ? 8 * i + wv : i == 5 ? 46 + wv : i == 6 ? 40 + wv : 54 + wv;
    if (i < 6 || (i == 6 && wv < 6) || (i == 7 && (wv < 4 || (wv == 4 && lane < 38))))
      dma16(pos, (unsigned)(j * 1024 + lane * 16), lbase + L_ROWS + j * 1024);
  };

  int d[3], s[3];
  u32x2 a[3];
  unsigned tagv;
  const bool v2 = tid < CH - 1024;
  const unsigned wlb = (unsigned)((lane >> 4) * 256 + 16 * ((lane & 15) ^ ((lane >> 4) << 2)));
  if (wv < 4) {
    const int j = (w >> 3) * 4 + wv;
    if (j < 64) dma16(we, (unsigned)(j * 1024 + lane * 16), lbase + L_ST + wv * 1024);
    else if (j < 122) dma16(pos, (unsigned)((j - 64) * 1024 + lane * 16), lbase + L_ST + wv * 1024);
  }
  {
    const unsigned e0 = (unsigned)(w * CH + tid) * 4, e2 = v2 ? e0 + 4096 : e0;
    asm volatile("global_load_dword %0, %10, %14\n\tglobal_load_dword %1, %10, %14 offset:2048\n\tglobal_load_dword %2, %11, %14\n\t"
                 "global_load_dword %3, %10, %15\n\tglobal_load_dword %4, %10, %15 offset:2048\n\tglobal_load_dword %5, %11, %15\n\t"
                 "global_load_dwordx2 %6, %12, %16\n\tglobal_load_dwordx2 %7, %13, %16\n\tglobal_load_dwordx2 %8, %18, %16\n\t"
                 "global_load_dword %9, %19, %17 sc1"
                 : "=&v"(d[0]), "=&v"(d[1]), "=&v"(d[2]), "=&v"(s[0]), "=&v"(s[1]), "=&v"(s[2]), "=&v"(a[0]), "=&v"(a[1]), "=&v"(a[2]), "=&v"(tagv)
                 : "v"(e0), "v"(e2), "v"(2 * e0), "v"(2 * e0 + 4096), "s"(dst), "s"(src), "s"(eattr), "s"(ws), "v"(2 * e2), "v"(WS_EP)
                 : "memory");
    const int row = 4 * wv + (lane >> 4), c = lane & 15;
#pragma unroll
    for (int i = 0; i < 4; i++) dma16(we, (wlb ^ (16 * i)) + (32 + 4 * wv + i) * 1024, lbase + L_W + (32 + 4 * wv + i) * 1024);
    dma16(h, (unsigned)(min(n0 + row, NN - 1) * 256 + 16 * (c ^ (row & 15))), lbase + L_H + wv * 1024);
    if (wv < 2) dma16(h, (unsigned)(min(n0 + 32 + row, NN - 1) * 256 + 16 * (c ^ (row & 15))), lbase + L_H + (8 + wv) * 1024);
  }
  if (tid < 256) *(u32*)(lds + L_HIST + tid * 4) = 0u;
  else {
    const int t = tid - 256;
    if (t < 32) {
      const int j = (t >> 2) - 4 * ((t >> 1) & 1) - 2 * (t & 1);
      *(u32*)(lds + L_OH + t * 4) = j == 0 ? 0x3F80u : j == 1 ? 0x3F800000u : 0u;
      if (t < 16) *(u32*)(lds + L_GC + t * 4) = 0u;
    }
  }
  barrier();
  {
    if (wv == 3) dma16(we, 65536 + lane * 16, lbase + L_EXT);
    if (wv == 4 && lane < 16) dma16(we, 66560 + lane * 16, lbase + L_EXT + 1024);
    if (wv == 5 && lane < 16) dma16(wx, lane * 16, lbase + L_WX);
    if (wv == 6 && lane < 16) dma16(wh, (unsigned)(w * 256 + lane * 16), lbase + L_BS + 10240);
  }
  const int T = wv & 3, nb = wv >> 2;
  const int loc = 5 * (4 * nb + (r >> 3)) + (r & 7);
  const bool valid = (r & 7) < 5 && loc < nsz;
  int ha = L_H + min(loc, 39) * 256 + 16 * (hh ^ (min(loc, 39) & 15));
  const unsigned trx0 = lbase + L_W + (unsigned)((8 * hh + q4) * 256), trs0 = (unsigned)((q4 << 2) | (2 * hh));
  unsigned b0 = trx0 + 32768 + 16 * ((unsigned)(8 * (T & 1) + 4 * (p4 & 1) + 2 * blk + (T >> 1)) ^ trs0) + 8 * (p4 >> 1);
  unsigned a0 = trx0 + 16 * ((unsigned)(4 * T + 2 * blk + (p4 >> 1)) ^ trs0) + 8 * (p4 & 1);
  int bo = L_BS + loc * 256 + 16 * ((4 * T + 2 * hh) ^ (loc & 15));
  unsigned gbo = WS_BT + (unsigned)((n0 + (tid >> 4)) * 256 + 16 * ((tid ^ (tid >> 4)) & 15));
  PIN(b0); PIN(a0); PIN(bo); PIN(gbo); PIN(ha);
  u32x4 I0, I1;
  {
    const unsigned one = (r & 1) ? 0x3F800000u : 0x3F80u;
#pragma unroll
    for (int jj = 0; jj < 4; jj++) {
      const bool m = ((r & 7) >> 1) == jj;
      I0[jj] = (m && (r >> 3) == hh) ? one : 0u;
      I1[jj] = (m && (r >> 3) == 2 + hh) ? one : 0u;
    }
  }
  const int rowbuf = L_ROWS + wv * ROWBUF;
  unsigned kdma = 16 * ((lane & 15) ^ (4 * (lane >> 4)));
  const int rd0 = rowbuf + 1040 * (r & 3) + 4352 * ((r >> 2) & 1) + (r >> 3) * 256;
  int rdA = rd0 + 64 * ((2 * hh) ^ (r >> 3)), rdB = rd0 + 64 * ((2 * hh + 1) ^ (r >> 3));
  int swr0 = L_S + 32 * (r >> 4) + 16 * ((r >> 2) & 1) + 8 * ((r >> 3) & 1) + 2 * (r & 3) + 160 * hh;
  int srd0 = L_S + r * 80 + 16 * hh;
  const unsigned long long bbase = (unsigned long long)(ws + WS_BT - 3120);
  const u32x4 bsrd = {(unsigned)bbase, (unsigned)(bbase >> 32) & 0xffffu, (unsigned)((NN + 256) * 256 + 3120), 0x00020000u};
  f32x16 Y[4];
#pragma unroll
  for (int t = 0; t < 4; t++) {
    Y[t] = zero16();
    PIN(Y[t]);
  }
  PIN(I0); PIN(I1); PIN(kdma); PIN(rdA); PIN(rdB); PIN(swr0); PIN(srd0);
  asm volatile("s_waitcnt vmcnt(12)" : "+v"(d[0]), "+v"(d[1]), "+v"(d[2])::"memory");
  int key[3];
  unsigned rank[3];
#pragma unroll
  for (int k = 0; k < 3; k++) {
    key[k] = (int)((unsigned)__umul24((unsigned)d[k], 429497u) >> 24);
    rank[k] = 0;
    if (k < 2 || v2) rank[k] = atomicAdd((unsigned*)(lds + L_HIST) + key[k], 1u);
  }
#pragma unroll
  for (int i = 0; i < 4; i++) dma16(we, (wlb ^ (16 * i)) + (4 * wv + i) * 1024, lbase + L_W + (4 * wv + i) * 1024);
  asm volatile("s_waitcnt vmcnt(9)" : "+v"(s[0]), "+v"(s[1]), "+v"(s[2]), "+v"(a[0]), "+v"(a[1]), "+v"(a[2]), "+v"(tagv)::"memory");
  const unsigned tag = (unsigned)__builtin_amdgcn_readfirstlane((int)tagv) + 1u;
  u32x4 rec[3];
#pragma unroll
  for (int k = 0; k < 3; k++) {
    const int lc = d[k] - bucket_start(key[k]);
    const int g = (lc * 205) >> 10;
    rec[k][0] = a[k][0];
    rec[k][1] = (a[k][1] & 0xffffu) | (tag << 16);
    rec[k][2] = (a[k][1] >> 16) | (tag << 16);
    rec[k][3] = ((unsigned)s[k] << 8) | (unsigned)(16 * lc - 79 * g) | ((tag >> 16) << 22);
    PIN(rec[k]);
  }
  barrier();
  u32x4 off4;
  unsigned oend;
  {
    const u32x4 cnt = *(const u32x4*)(lds + L_HIST + lane * 16);
    const unsigned tot = cnt[0] + cnt[1] + cnt[2] + cnt[3];
    oend = (unsigned)wave_incl_scan((int)tot);
    off4[0] = oend - tot;
    off4[1] = off4[0] + cnt[0];
    off4[2] = off4[1] + cnt[1];
    off4[3] = off4[2] + cnt[2];
    *(u32x4*)(lds + L_OFF + lane * 16) = off4;
    if (wv == 7) {
      const u32x4 g0 = {tag, off4[0] | (off4[1] << 16), tag, off4[1] | (off4[2] << 16)};
      const u32x4 g1 = {tag, off4[2] | (off4[3] << 16), tag, off4[3] | (oend << 16)};
      const unsigned off = WS_TAB + (unsigned)(w * NB + 4 * lane) * 8;
      __builtin_amdgcn_raw_buffer_store_b128(g0, wsr, off, 0, 16);
      __builtin_amdgcn_raw_buffer_store_b128(g1, wsr, off + 16, 0, 16);
    }
#pragma unroll
    for (int k = 0; k < 3; k++) rank[k] += *(const u32*)(lds + L_OFF + key[k] * 4);
#pragma unroll
    for (int k = 0; k < 3; k++)
      if (k < 2 || v2) *(u32x4*)(lds + L_ST + rank[k] * 16) = rec[k];
  }
  if (wv == 7) asm volatile("s_waitcnt vmcnt(2)" ::: "memory");
  else wait_vm0();
  barrier();
  u32x2 at[4], ent;
  {
    u32x4 v0 = *(const u32x4*)(lds + L_ST + tid * 16);
    u32x4 v1 = *(const u32x4*)(lds + L_ST + 8192 + tid * 16);
    u32x4 v2_ = *(const u32x4*)(lds + L_ST + 16384 + (tid & 255) * 16);
    const unsigned b1 = (b0 ^ 16) + 1024, a1 = (a0 ^ 16) + 1024;
    u32x4 hf[8], wf[8];
#pragma unroll
    for (int ks = 0; ks < 8; ks++) {
      hf[ks] = *(const u32x4*)(lds + (ha ^ (32 * ks)));
      wf[ks] = tr_read2(b0 + ks * 4096, b1 + ks * 4096);
    }
    SB();
    __builtin_amdgcn_raw_buffer_store_b128(v0, wsr, WS_R + (unsigned)(w * CHS + tid) * 16, 0, 16);
    __builtin_amdgcn_raw_buffer_store_b128(v1, wsr, WS_R + (unsigned)(w * CHS + 512 + tid) * 16, 0, 16);
    if (tid < 256) __builtin_amdgcn_raw_buffer_store_b128(v2_, wsr, WS_R + (unsigned)(w * CHS + 1024 + tid) * 16, 0, 16);
    f32x16 acc = zero16();
#pragma unroll
    for (int ks = 0; ks < 8; ks++) {
      acc = mfma(wf[ks], hf[ks], acc);
      PIN(acc);
      pos_tile(ks);
    }
    SB();
#pragma unroll
    for (int ks = 0; ks < 8; ks++) wf[ks] = tr_read2(a0 + ks * 4096, a1 + ks * 4096);
    SB();
    u32x4 bt0, bt1;
#pragma unroll
    for (int i = 0; i < 4; i++) {
      bt0[i] = pk2(acc[2 * i] * NLOG2E, acc[2 * i + 1] * NLOG2E);
      bt1[i] = pk2(acc[8 + 2 * i] * NLOG2E, acc[9 + 2 * i] * NLOG2E);
    }
    if ((r & 7) < 5) {
      *(u32x4*)(lds + bo) = bt0;
      *(u32x4*)(lds + (bo ^ 16)) = bt1;
    }
    barrier();
    const u32x4 vb0 = *(const u32x4*)(lds + L_BS + tid * 16);
    const u32x4 vb1 = *(const u32x4*)(lds + L_BS + 8192 + min(tid, 143) * 16);
    __builtin_amdgcn_raw_buffer_store_b128(vb0, wsr, gbo, 0, 16);
    if (tid < 16 * (nsz - 32) || (tid >> 4) == 8)
      __builtin_amdgcn_raw_buffer_store_b128(vb1, wsr, tid < 128 ? gbo + 8192 : WS_BT + (unsigned)((NN + w) * 256 + (tid & 15) * 16), 0, 16);
    asm volatile("" ::: "memory");
    acc = zero16();
#pragma unroll
    for (int ks = 0; ks < 8; ks++) {
      acc = mfma(hf[ks], wf[ks], acc);
      PIN(acc);
    }
#pragma unroll
    for (int i = 0; i < 4; i++) {
      at[i][0] = pk2(acc[4 * i] * NLOG2E, acc[4 * i + 1] * NLOG2E);
      at[i][1] = pk2(acc[4 * i + 2] * NLOG2E, acc[4 * i + 3] * NLOG2E);
      PIN(at[i]);
    }
  }
  {
    const u32x4 z = {0u, 0u, 0u, 0u};
    *(u32x4*)(lds + L_S + tid * 16) = z;
    *(u32x4*)(lds + L_S + 8192 + tid * 16) = z;
    if (tid < 256) {
      *(u32x4*)(lds + L_S + 16384 + tid * 16) = z;
    } else {
      const int t = tid - 256, v = t >> 5, q = (t >> 3) & 3, i = t & 7;
      *(u32*)(lds + L_FK + t * 4) = (unsigned)(NN + (i < 4 ? 128 + 32 * q + 4 * v + i : 16 * v + 4 * (i - 4) + q)) << 8;
    }
  }
  u32x4 extB[4];
#pragma unroll
  for (int t = 0; t < 4; t++) {
    const u16* wp = (const u16*)(lds + L_EXT) + 32 * t + r;
    const float w0 = bf2f(wp[(1 + 2 * hh) * 128]) * NLOG2E, w1 = bf2f(wp[(2 + 2 * hh) * 128]) * NLOG2E, w2 = bf2f(wp[0]) * NLOG2E;
    extB[t][0] = 0u;
    extB[t][1] = 0u;
    extB[t][2] = pk2(w0, w1);
    extB[t][3] = hh ? 0u : pk2(w2, 0.f);
    PIN(extB[t]);
  }
  asm volatile("global_load_dwordx2 %0, %1, %2 sc1\n\ts_waitcnt vmcnt(0)" : "=&v"(ent) : "v"(WS_TAB + (unsigned)((tid >> 1) * NB + b) * 8), "s"(ws) : "memory");
  barrier();
  if (tid == 0) __builtin_amdgcn_raw_buffer_store_b32(tag, wsr, WS_FB + (unsigned)w * 4, 0, 16);
#pragma unroll
  for (int i = 0; i < 4; i++) *(u32x2*)(lds + L_A + (4 * nb + i) * 2048 + T * 512 + lane * 8) = at[i];

  bool wh_loaded = false, slow = false;
  int lo = 0, quota = MAPW, gq = wv;
  unsigned pin = 0, atmp = 0;
  while (true) {
    wh_loaded = false;
    int tq = tid;
    PIN(tq);
    const int lq = tq & 63;
    int kc, kfirst, kpre, wtot;
    {
      const unsigned tabq = WS_TAB + (unsigned)((tq >> 1) * NB + b) * 8;
      u32x2 gq2 = ent;
      ent[0] = tag + 1u;
#pragma clang loop unroll(disable)
      for (int spin = 0; spin < (1 << 22); spin++) {
        if (gq2[0] != tag) asm volatile("global_load_dwordx2 %0, %1, %2 sc1\n\ts_waitcnt vmcnt(0)" : "=&v"(gq2) : "v"(tabq), "s"(ws) : "memory");
        if (__all(gq2[0] == tag)) break;
        __builtin_amdgcn_s_sleep(1);
      }
      const int o0 = min((int)(gq2[1] & 0xffffu), CH), o1 = min((int)(gq2[1] >> 16), CH);
      kc = max(o1 - o0, 0);
      kfirst = (tq >> 1) * CHS + o0;
      const int inc = wave_incl_scan((tq & 1) ? 0 : kc);
      kpre = inc - kc;
      wtot = __builtin_amdgcn_readlane(inc, 63);
      if (!slow && lq == 0) atomicMax((unsigned*)(lds + L_GC + 32), (unsigned)wtot);
    }
    u32x3 q[4];
    u32x2 pd[4], ps[4];
    int ra1[4], par[4];
    bool four;
    {
      const int map = L_MAP + wv * MAPW * 4;
      const int nw = min(wtot - lo, quota);
      const int jhi = min(kc, lo + quota - kpre);
#pragma clang loop vectorize(disable) unroll(disable)
      for (int j = max(0, lo - kpre) + ((tq ^ max(0, lo - kpre)) & 1); j < jhi; j += 2) *(u32*)(lds + map + (kpre + j - lo) * 4) = (unsigned)(kfirst + j) * 16u;
      unsigned ix[4];
#pragma unroll
      for (int k = 0; k < 4; k++) {
        const unsigned mi = *(const u32*)(lds + map + (64 * k + lq) * 4);
        ix[k] = 64 * k + lq < nw ? mi : 0u;
      }
      u32x4 q4_[4];
      unsigned fb;
      const unsigned fbo = WS_FB + (unsigned)(32 * wv + (lq & 31)) * 4;
#pragma clang loop unroll(disable)
      for (int spin = 0; spin < (1 << 22); spin++) {
        asm volatile("global_load_dwordx4 %0, %5, %9 sc1\n\tglobal_load_dwordx4 %1, %6, %9 sc1\n\tglobal_load_dwordx4 %2, %7, %9 sc1\n\t"
                     "global_load_dwordx4 %3, %8, %9 sc1\n\tglobal_load_dword %4, %10, %11 sc1\n\ts_waitcnt vmcnt(0)"
                     : "=&v"(q4_[0]), "=&v"(q4_[1]), "=&v"(q4_[2]), "=&v"(q4_[3]), "=&v"(fb)
                     : "v"(ix[0]), "v"(ix[1]), "v"(ix[2]), "v"(ix[3]), "s"(ws + WS_R), "v"(fbo), "s"(ws) : "memory");
        bool ok = true;
#pragma unroll
        for (int k = 0; k < 4; k++) {
          const unsigned bad = (((q4_[k][1] ^ (tag << 16)) | (q4_[k][2] ^ (tag << 16))) >> 16) | ((q4_[k][3] ^ ((tag >> 16) << 22)) >> 22);
          ok = ok && (bad == 0 || 64 * k + lq >= nw);
        }
        if (__all(ok)) break;
      }
#pragma unroll
      for (int k = 0; k < 4; k++) {
        q[k][0] = q4_[k][0];
        q[k][1] = (q4_[k][1] & 0xffffu) | (q4_[k][2] << 16);
        q[k][2] = q4_[k][3] & 0x3fffffu;
      }
      unsigned rk[4];
#pragma unroll
      for (int k = 0; k < 4; k++) {
        rk[k] = CAPW;
        if (64 * k + lq < nw) rk[k] = atomicAdd((unsigned*)(lds + L_GC) + (q[k][2] & 7u), 1u);
        const int dn = n0 + 5 * (int)(q[k][2] & 7u) + (int)((q[k][2] >> 4) & 7u), sn = min((int)(q[k][2] >> 8), NN - 1);
        const int od = L_ROWS + ((6 * dn) & ~3), os = L_ROWS + ((6 * sn) & ~3);
        pd[k][0] = *(const u32*)(lds + od); pd[k][1] = *(const u32*)(lds + od + 4);
        ps[k][0] = *(const u32*)(lds + os); ps[k][1] = *(const u32*)(lds + os + 4);
        par[k] = (dn & 1) * 16;
      }
#pragma unroll
      for (int k = 0; k < 4; k++) {
        const bool ok = rk[k] < CAPW;
        const int e = (int)((q[k][2] & 7u) * CAPW + rk[k]);
        *(u32*)(lds + (ok ? L_R2 + e * 4 : L_DUM + 96)) = q[k][2];
        ra1[k] = ok ? L_R1 + e * 16 : L_DUM + 96;
      }
      four = nw > 192;
#pragma clang loop unroll(disable)
      for (int spin = 0; spin < (1 << 22) && !__all(fb == tag); spin++) {
        __builtin_amdgcn_s_sleep(1);
        asm volatile("global_load_dword %0, %1, %2 sc1\n\ts_waitcnt vmcnt(0)" : "=&v"(fb) : "v"(fbo), "s"(ws) : "memory");
      }
    }
    barrier();
    const int cl = *(const u32*)(lds + L_GC + (lane & 7) * 4);
    const int maxtot = __builtin_amdgcn_readfirstlane(*(const u32*)(lds + L_GC + 32));
    if (!slow) {
      const int kk = (cl << 3) | (lane & 7);
      int rk = 0;
      rk += __builtin_amdgcn_update_dpp(0, kk, 0x121, 0xf, 0xf, false) > kk;
      rk += __builtin_amdgcn_update_dpp(0, kk, 0x122, 0xf, 0xf, false) > kk;
      rk += __builtin_amdgcn_update_dpp(0, kk, 0x123, 0xf, 0xf, false) > kk;
      rk += __builtin_amdgcn_update_dpp(0, kk, 0x124, 0xf, 0xf, false) > kk;
      rk += __builtin_amdgcn_update_dpp(0, kk, 0x125, 0xf, 0xf, false) > kk;
      rk += __builtin_amdgcn_update_dpp(0, kk, 0x126, 0xf, 0xf, false) > kk;
      rk += __builtin_amdgcn_update_dpp(0, kk, 0x127, 0xf, 0xf, false) > kk;
      const int cmax = __any(cl > CAPW) ? CAPW + 1 : 0;
      if (maxtot > MAPW || cmax > CAPW) {
        slow = true;
        quota = CAPW / 8;
        barrier();
        if (tid < 8) *(u32*)(lds + L_GC + tid * 4) = 0u;
        barrier();
        continue;
      }
      gq = __builtin_ctzll(__ballot(rk == (wv < 4 ? wv : 11 - wv)));
    }
    const int n = __builtin_amdgcn_readlane(cl, gq);
    const unsigned ohc = (unsigned)(L_OH + hh * 8);
    const int swr = swr0 + gq * 2560 - 20 * (int)ohc, srd = srd0 + gq * 2560;
    const int l1 = L_R1 + gq * CAPW * 16, l2 = L_R2 + gq * CAPW * 4;
    const int nt = (n + 31) >> 5;
    u32x4 rows[8], extA, S0, S1, ra, rb, m1p = {0u, 0u, 0u, 0u};
    unsigned r2;
    int sp;
    f32x16 XA, XB;
    auto list_read = [&](int tl) {
      const int list = tl < nt ? l2 + tl * 128 : L_FK + wv * 128;
      ra = *(const u32x4*)(lds + list + (lane >> 4) * 32);
      rb = *(const u32x4*)(lds + list + (lane >> 4) * 32 + 16);
    };
    auto dma_piece = [&](int, int i) {
      if (i == 0) row_dma<0>(bsrd, ra[0], kdma, lbase + rowbuf, atmp);
      if (i == 1) row_dma<1>(bsrd, ra[1], kdma, lbase + rowbuf, atmp);
      if (i == 2) row_dma<2>(bsrd, ra[2], kdma, lbase + rowbuf, atmp);
      if (i == 3) row_dma<3>(bsrd, ra[3], kdma, lbase + rowbuf, atmp);
      if (i == 4) row_dma<4>(bsrd, rb[0], kdma, lbase + rowbuf, atmp);
      if (i == 5) row_dma<5>(bsrd, rb[1], kdma, lbase + rowbuf, atmp);
      if (i == 6) row_dma<6>(bsrd, rb[2], kdma, lbase + rowbuf, atmp);
      if (i == 7) row_dma<7>(bsrd, rb[3], kdma, lbase + rowbuf, atmp);
    };
    auto meta_read = [&](int tl) {
      const u32x2 r1 = *(const u32x2*)(lds + l1 + (tl * 32 + r) * 16 + hh * 8);
      extA[2] = r1[0];
      extA[3] = r1[1];
      r2 = *(const u32*)(lds + l2 + (tl * 32 + r) * 4);
    };
    auto meta_use = [&](int) {
      const unsigned oa = (r2 & 0x70u) | ohc;
      const u32x2 oh = *(const u32x2*)(lds + oa);
      extA[0] = oh[0];
      extA[1] = oh[1];
      sp = (int)__umul24(oa, 20u) + swr;
    };
    auto s_write = [&]() {
      *(u16*)(lds + sp) = (u16)(hh ? extA[3] : 0x3F80u);
      *(u16*)(lds + sp + 80) = (u16)(extA[3] >> 16);
    };
    auto rows_read = [&]() {
#pragma unroll
      for (int j = 0; j < 8; j++) rows[j] = *(const u32x4*)(lds + (j < 4 ? rdA : rdB) + 16 * (j & 3));
    };
    auto rows_pin = [&]() {
      asm volatile("" : "+v"(rows[0]), "+v"(rows[1]), "+v"(rows[2]), "+v"(rows[3]), "+v"(rows[4]), "+v"(rows[5]), "+v"(rows[6]), "+v"(rows[7]));
    };

    list_read(0);
    if (n > 0 && wv < 4) {
#pragma unroll
      for (int i = 0; i < 8; i++) dma_piece(0, i);
    }
    auto finish = [&](int k) {
      const unsigned long long ud = (((unsigned long long)pd[k][1] << 32) | pd[k][0]) >> par[k];
      const unsigned long long us = (((unsigned long long)ps[k][1] << 32) | ps[k][0]) >> ((q[k][2] >> 4) & 16u);
      const unsigned dlo = (unsigned)ud, dhi = (unsigned)(ud >> 32), slo = (unsigned)us, shi = (unsigned)(us >> 32);
      const unsigned dxy = pk2(bf2f(dlo) - bf2f(slo), bfhi(dlo) - bfhi(slo));
      const unsigned dz_ = pk2(bf2f(dhi) - bf2f(shi), 0.f);
      const float dx = bf2f(dxy), dy = bfhi(dxy), dz = bf2f(dz_);
      const unsigned sxy = pk2(dx * dx, dy * dy), sz = pk2(dz * dz, 0.f);
      const unsigned d2 = pk2((bf2f(sxy) + bfhi(sxy)) + bf2f(sz), 0.f) & 0xffffu;
      const u32x4 rec = {q[k][0], d2 | (dxy << 16), q[k][1], (dxy >> 16) | (dz_ << 16)};
      *(u32x4*)(lds + ra1[k]) = rec;
    };
    finish(0);
    finish(1);
    finish(2);
    if (four) finish(3);
    if (lane < 32 * nt - n) {
      const u32x4 z = {0u, 0u, 0u, 0u};
      *(u32x4*)(lds + l1 + (n + lane) * 16) = z;
      *(u32*)(lds + l2 + (n + lane) * 4) = 0x70u;
    }
    if (w == 0 && tid == 0) __builtin_amdgcn_raw_buffer_store_b32(tag, wsr, WS_EP, 0, 16);
    pin = pos[min(3 * n0 + 15 * gq + (lq & 15), 3 * NN - 1)];
#pragma unroll
    for (int t = 0; t < 4; t++) {
      const u32x2 v = *(const u32x2*)(lds + L_A + gq * 2048 + t * 512 + lq * 8);
      extB[t][0] = v[0];
      extB[t][1] = v[1];
    }
    barrier();
    if (n > 0) {
    if (wv >= 4) {
#pragma unroll
      for (int i = 0; i < 8; i++) dma_piece(0, i);
    }
    meta_read(0);
    list_read(1);
    meta_use(0);
    s_write();
    S1 = m1p;
    wait_vm0();
    PIN(pin);
    rows_read();
    rows_pin();
    XA = mfma(rows[0], I0, zero16());
    XA = mfma(rows[1], I1, XA);
    XA = mfma(extA, extB[0], XA);

    const int nfull = (n + 15) >> 5;
    int first = 1;
    for (int tl = 0; tl < nfull; tl++) {
      asm volatile("" : "+s"(first));
#pragma unroll
      for (int t = 0; t < 4; t++) {
        f32x16& Xc = (t & 1) ? XB : XA;
        f32x16& Xn = (t & 1) ? XA : XB;
        const int tn = (t + 1) & 3;
        auto gap = [&](int k) {
          SB();
          if (t == 0 && k == 0 && first) {
#pragma unroll
            for (int i = 0; i < 8; i++) dma_piece(1, i);
          }
          if (t == 0 && k == 0) S0 = *(const u32x4*)(lds + srd);
          if (t == 0 && k == 3) S1 = *(const u32x4*)(lds + srd + 32);
          if (t == 0 && k == 4) {
            *(u16*)(lds + sp) = 0;
            *(u16*)(lds + sp + 80) = 0;
          }
          if (t == 2 && k == 1) meta_read(tl + 1);
          if (t == 2 && k == 4) meta_use(tl + 1);
          if (t == 2 && k == 3) wait_vm0();
          if (t == 2 && k >= 3 && k < 7) {
            rows[2 * k - 6] = *(const u32x4*)(lds + (k < 5 ? rdA : rdB) + 16 * ((2 * k - 6) & 3));
            rows[2 * k - 5] = *(const u32x4*)(lds + (k < 5 ? rdA : rdB) + 16 * ((2 * k - 5) & 3));
          }
          if (t == 2 && k == 7) list_read(tl + 2);
          if (t == 3 && k < 8) dma_piece(tl + 2, k);
          if (t == 3 && k == 8) s_write();
          SB();
        };
        f32x2 dd[8];
        if (t == 3 && wv >= 4) __builtin_amdgcn_s_setprio(1);
        if (t == 2 && wv >= 4) __builtin_amdgcn_s_setprio(0);
        SB();
        Xn = mfma(rows[2 * tn], I0, zero16());
        PIN(Xn);
        SB();
#pragma unroll
        for (int g = 0; g < 4; g++) {
#pragma unroll
          for (int i = 2 * g; i < 2 * g + 2; i++) {
            dd[i][0] = __builtin_amdgcn_exp2f(Xc[2 * i]);
            dd[i][1] = __builtin_amdgcn_exp2f(Xc[2 * i + 1]);
            PIN(dd[i]);
          }
          SB();
          if (g == 0) { Xn = mfma(rows[2 * tn + 1], I1, Xn); PIN(Xn); }
          if (g == 1) { Xn = mfma(extA, extB[tn], Xn); PIN(Xn); }
          if (g == 2) { Y[(t + 3) & 3] = mfma(S1, m1p, Y[(t + 3) & 3]); PIN(Y[(t + 3) & 3]); }
          gap(g);
        }
#pragma unroll
        for (int i = 0; i < 8; i++) { dd[i] += 1.0f; if (i < 7) PIN(dd[i]); }
        gap(4);
#pragma unroll
        for (int g = 0; g < 4; g++) {
#pragma unroll
          for (int i = 2 * g; i < 2 * g + 2; i++) {
            dd[i][0] = __builtin_amdgcn_rcpf(dd[i][0]);
            dd[i][1] = __builtin_amdgcn_rcpf(dd[i][1]);
            PIN(dd[i]);
          }
          gap(5 + g);
        }
        u32x4 m0;
#pragma unroll
        for (int i = 0; i < 4; i++) { f32x2 x = {Xc[2 * i], Xc[2 * i + 1]}; dd[i] *= x; }
#pragma unroll
        for (int i = 0; i < 4; i++) m0[i] = __builtin_bit_cast(unsigned, __builtin_convertvector(dd[i], bf16x2));
        SB();
#pragma unroll
        for (int i = 4; i < 8; i++) { f32x2 x = {Xc[2 * i], Xc[2 * i + 1]}; dd[i] *= x; if (i < 7) PIN(dd[i]); }
        SB();
        Y[t] = mfma(S0, m0, Y[t]);
        PIN(Y[t]);
        SB();
#pragma unroll
        for (int i = 0; i < 4; i++) m1p[i] = __builtin_bit_cast(unsigned, __builtin_convertvector(dd[4 + i], bf16x2));
        PIN(m1p);
        if (t == 2) rows_pin();
      }
      first = 0;
    }
    SB();
    Y[3] = mfma(S1, m1p, Y[3]);
    S0 = *(const u32x4*)(lds + srd);
    *(u16*)(lds + sp) = 0;
    *(u16*)(lds + sp + 80) = 0;
    if (nfull < nt) {
#pragma unroll
      for (int t = 0; t < 4; t++) {
        f32x16& Xc = (t & 1) ? XB : XA;
        f32x16& Xn = (t & 1) ? XA : XB;
        f32x2 dd[4];
        if (t < 2 && nfull == 0) {
#pragma unroll
          for (int i = 0; i < 4; i++) dma_piece(nt, 4 * t + i);
        }
        SB();
        if (t < 3) { Xn = mfma(rows[2 * t + 2], I0, zero16()); PIN(Xn); }
        SB();
#pragma unroll
        for (int i = 0; i < 2; i++) { dd[i][0] = __builtin_amdgcn_exp2f(Xc[2 * i]); dd[i][1] = __builtin_amdgcn_exp2f(Xc[2 * i + 1]); }
        SB();
        if (t < 3) { Xn = mfma(rows[2 * t + 3], I1, Xn); PIN(Xn); }
        SB();
#pragma unroll
        for (int i = 2; i < 4; i++) { dd[i][0] = __builtin_amdgcn_exp2f(Xc[2 * i]); dd[i][1] = __builtin_amdgcn_exp2f(Xc[2 * i + 1]); }
        SB();
        if (t < 3) { Xn = mfma(extA, extB[t + 1], Xn); PIN(Xn); }
        SB();
#pragma unroll
        for (int i = 0; i < 4; i++) dd[i] += 1.0f;
        SB();
#pragma unroll
        for (int i = 0; i < 4; i++) { dd[i][0] = __builtin_amdgcn_rcpf(dd[i][0]); dd[i][1] = __builtin_amdgcn_rcpf(dd[i][1]); }
        SB();
        u32x4 m0;
#pragma unroll
        for (int i = 0; i < 4; i++) { f32x2 x = {Xc[2 * i], Xc[2 * i + 1]}; dd[i] *= x; }
#pragma unroll
        for (int i = 0; i < 4; i++) m0[i] = __builtin_bit_cast(unsigned, __builtin_convertvector(dd[i], bf16x2));
        SB();
        Y[t] = mfma(S0, m0, Y[t]);
        if (t < 3) PIN(Xn);
      }
    }
    wh_loaded = true;
    wait_vm0();
    }
    if (!slow) break;
    lo += quota;
    if (lo >= maxtot) break;
    barrier();
    if (tid < 8) *(u32*)(lds + L_GC + tid * 4) = 0u;
#pragma unroll
    for (int i = 0; i < 8; i++) pos_tile(i);
    wait_vm0();
    barrier();
  }
  if (!wh_loaded) {
    wait_vm0();
    PIN(pin);
    const u32x4 fa = *(const u32x4*)(lds + L_FK + wv * 128 + (lane >> 4) * 32), fb = *(const u32x4*)(lds + L_FK + wv * 128 + (lane >> 4) * 32 + 16);
    row_dma<0>(bsrd, fa[0], kdma, lbase + rowbuf, atmp); row_dma<1>(bsrd, fa[1], kdma, lbase + rowbuf, atmp);
    row_dma<2>(bsrd, fa[2], kdma, lbase + rowbuf, atmp); row_dma<3>(bsrd, fa[3], kdma, lbase + rowbuf, atmp);
    row_dma<4>(bsrd, fb[0], kdma, lbase + rowbuf, atmp); row_dma<5>(bsrd, fb[1], kdma, lbase + rowbuf, atmp);
    row_dma<6>(bsrd, fb[2], kdma, lbase + rowbuf, atmp); row_dma<7>(bsrd, fb[3], kdma, lbase + rowbuf, atmp);
    wait_vm0();
  }

#pragma unroll
  for (int i = 0; i < 3; i++) {
    if (i < 2 || hh == 0) {
      const u32x2 mv = {pk2(Y[0][4 * i] * NLN2, Y[1][4 * i] * NLN2), pk2(Y[2][4 * i] * NLN2, Y[3][4 * i] * NLN2)};
      *(u32x2*)(lds + L_M + (5 * gq + 2 * i + hh) * MSTR + 8 * r) = mv;
    }
  }
  u32x4 hq[8], res[2];
  unsigned wxr[4];
#pragma unroll
  for (int ks = 0; ks < 8; ks++) hq[ks] = *(const u32x4*)(lds + (ha ^ (32 * ks)));
  {
    const int ra_ = (ha ^ (16 * hh)) ^ (64 * T + 32 * hh);
    res[0] = *(const u32x4*)(lds + ra_);
    res[1] = *(const u32x4*)(lds + (ra_ ^ 16));
  }
#pragma unroll
  for (int t = 0; t < 4; t++) wxr[t] = ((const u16*)(lds + L_WX))[32 * t + r];
  unsigned wa = lbase + L_ROWS + 2080 * hh + 256 * q4 + 64 * (T ^ q4) + 32 * (p4 & 1) + 16 * blk + 8 * (p4 >> 1);
  int ma = L_M + min(loc, 39) * MSTR + 16 * hh;
  PIN(wa);
  PIN(ma);
  barrier();
  {
    u32x4 w1[8], w2[8], mq[8];
#pragma unroll
    for (int ks = 0; ks < 8; ks++) w1[ks] = tr_read2(wa + ks * ROWBUF + 4352, wa + ks * ROWBUF + 4352 + 1040);
#pragma unroll
    for (int ks = 0; ks < 8; ks++) {
      w2[ks] = tr_read2(wa + ks * ROWBUF, wa + ks * ROWBUF + 1040);
      mq[ks] = *(const u32x4*)(lds + ma + 32 * ks);
    }
    SB();
    f32x16 P = zero16();
#pragma unroll
    for (int ks = 0; ks < 8; ks++) P = mfma(w1[ks], hq[ks], P);
#pragma unroll
    for (int ks = 0; ks < 8; ks++) P = mfma(w2[ks], mq[ks], P);
    PIN(P);
    SB();
    float zz[9];
    {
      float wxv[4];
#pragma unroll
      for (int t = 0; t < 4; t++) wxv[t] = bf2f(wxr[t]) * NLN2;
#pragma unroll
      for (int i = 0; i < 9; i++) {
        const int j = 4 * (i / 3) + 1 + i % 3;
        zz[i] = Y[0][j] * wxv[0];
        PIN(zz[i]);
        zz[i] = __builtin_fmaf(Y[1][j], wxv[1], zz[i]);
        PIN(zz[i]);
        zz[i] = __builtin_fmaf(Y[2][j], wxv[2], zz[i]);
        PIN(zz[i]);
        zz[i] = __builtin_fmaf(Y[3][j], wxv[3], zz[i]);
        PIN(zz[i]);
      }
    }
    SB();
    f32x2 x[8], e[8];
#pragma unroll
    for (int i = 0; i < 8; i++) {
      x[i][0] = P[2 * i];
      x[i][1] = P[2 * i + 1];
      e[i] = x[i] * NLOG2E;
    }
    SB();
#pragma unroll
    for (int i = 0; i < 8; i++) { e[i][0] = __builtin_amdgcn_exp2f(e[i][0]); e[i][1] = __builtin_amdgcn_exp2f(e[i][1]); }
    SB();
#pragma unroll
    for (int i = 0; i < 8; i++) e[i] += 1.0f;
    SB();
#pragma unroll
    for (int i = 0; i < 8; i++) { e[i][0] = __builtin_amdgcn_rcpf(e[i][0]); e[i][1] = __builtin_amdgcn_rcpf(e[i][1]); }
    SB();
    u32x4 ov[2];
#pragma unroll
    for (int i = 0; i < 8; i++) {
      const unsigned rs = res[i >> 2][i & 3];
      const f32x2 rv = {bf2f(rs), bfhi(rs)};
      const f32x2 o = __builtin_elementwise_fma(x[i], e[i], rv);
      ov[i >> 2][i & 3] = pk2(o[0], o[1]);
    }
    if (valid) {
      const __amdgpu_buffer_rsrc_t osr = __builtin_amdgcn_make_buffer_rsrc(out, 0, NN * 262, 0x00020000);
      const unsigned oo = (unsigned)(n0 + loc) * 256 + 64 * T + 32 * hh;
      __builtin_amdgcn_raw_buffer_store_b128(ov[0], osr, oo, 0, 0);
      __builtin_amdgcn_raw_buffer_store_b128(ov[1], osr, oo + 16, 0, 0);
    }
#define DPP9(ctl)                                                                                                         \
  "v_add_f32_dpp %0, %0, %0 " ctl "\n\tv_add_f32_dpp %1, %1, %1 " ctl "\n\tv_add_f32_dpp %2, %2, %2 " ctl "\n\t"          \
  "v_add_f32_dpp %3, %3, %3 " ctl "\n\tv_add_f32_dpp %4, %4, %4 " ctl "\n\tv_add_f32_dpp %5, %5, %5 " ctl "\n\t"          \
  "v_add_f32_dpp %6, %6, %6 " ctl "\n\tv_add_f32_dpp %7, %7, %7 " ctl "\n\tv_add_f32_dpp %8, %8, %8 " ctl "\n\t"
    asm volatile("s_nop 1\n\t" DPP9("row_shr:1 row_mask:0xf bank_mask:0xf bound_ctrl:1") DPP9("row_shr:2 row_mask:0xf bank_mask:0xf bound_ctrl:1")
                 DPP9("row_shr:4 row_mask:0xf bank_mask:0xf bound_ctrl:1") DPP9("row_shr:8 row_mask:0xf bank_mask:0xf bound_ctrl:1")
                 DPP9("row_bcast:15 row_mask:0xa bank_mask:0xf") "s_nop 0"
                 : "+v"(zz[0]), "+v"(zz[1]), "+v"(zz[2]), "+v"(zz[3]), "+v"(zz[4]), "+v"(zz[5]), "+v"(zz[6]), "+v"(zz[7]), "+v"(zz[8]));
    const int zp = L_S + gq * 2560;
    if (r == 31) {
#pragma unroll
      for (int i = 0; i < 9; i++) *(u32*)(lds + zp + 12 * hh + 24 * (i / 3) + 4 * (i % 3)) = __float_as_uint(zz[i]);
    }
    asm volatile("" ::: "memory");
    const float z = __uint_as_float(*(const u32*)(lds + zp + 4 * (lane & 15)));
    if (lane < 15 && 15 * gq + lane < 3 * nsz) out[(size_t)NN * 128 + (unsigned)(3 * n0 + 15 * gq + lane)] = (u16)pk2(bf2f(pin) + z, 0.f);
  }
}

extern "C" void kernel_launch(void* const* d_in, const int* in_sizes, int n_in, void* d_out, int out_size, void* d_ws,
                              size_t ws_size, hipStream_t stream) {
  egnn_fused<<<NB, 512, 0, stream>>>((const u16*)d_in[0], (const u16*)d_in[1], (const u32x2*)d_in[2], (const int*)d_in[3],
                                     (const int*)d_in[4], (const u16*)d_in[5], (const u16*)d_in[6], (const u16*)d_in[7],
                                     (char*)d_ws, (u16*)d_out);
}
```

```cpp
#include <hip/hip_runtime.h>
#include <hip/hip_bf16.h>
#include <stdint.h>

typedef __bf16 bf16x8 __attribute__((ext_vector_type(8)));
typedef __bf16 bf16x2 __attribute__((ext_vector_type(2)));
typedef short s16x4 __attribute__((ext_vector_type(4)));
typedef float f32x2 __attribute__((ext_vector_type(2)));
typedef float f32x16 __attribute__((ext_vector_type(16)));
typedef unsigned u32x2 __attribute__((ext_vector_type(2), may_alias));
typedef unsigned u32x3 __attribute__((ext_vector_type(3)));
typedef unsigned u32x4 __attribute__((ext_vector_type(4), may_alias));
typedef unsigned short u16 __attribute__((may_alias));
typedef unsigned u32 __attribute__((may_alias));
#define LDSP(T) __attribute__((address_space(3))) T*
#define DI __device__ __forceinline__
#define SB() __builtin_amdgcn_sched_barrier(0)
#define PIN(x) asm volatile("" : "+v"(x))

constexpr int NN = 10000;
constexpr int CH = 1250, CHS = 1280;
constexpr int NB = 256;
constexpr float NLOG2E = -1.4426950408889634f;
constexpr float NLN2 = -0.6931471805599453f;

constexpr unsigned WS_BT = 4096;
constexpr unsigned WS_R = WS_BT + (NN + 256) * 256;
constexpr unsigned WS_TAB = WS_R + NB * CHS * 16;
constexpr unsigned WS_FB = WS_TAB + NB * NB * 8;
constexpr unsigned WS_EP = WS_FB + NB * 4;
constexpr unsigned WS_END = WS_EP + 256;

constexpr int CAPW = 224;
constexpr int ROWBUF = 8496;
constexpr int L_ROWS = 0;
constexpr int L_R1 = 8 * ROWBUF;
constexpr int L_R2 = L_R1 + 8 * CAPW * 16;
constexpr int L_S = L_R2 + 8 * CAPW * 4;
constexpr int L_M = L_S + 8 * 2560;
constexpr int MSTR = 272;
constexpr int L_H = 135168;
constexpr int L_A = L_H + 10240;
constexpr int L_HIST = L_A + 8 * 2048;
constexpr int L_OH = L_HIST + 1024;
constexpr int L_GC = L_OH + 128;
constexpr int L_DUM = L_GC + 64;
constexpr int L_WX = L_DUM + 128;
constexpr int L_END = L_WX + 256;
constexpr int L_W = L_R1;
constexpr int L_EXT = L_W + 65536;
constexpr int L_ST = 47104;
constexpr int L_BS = L_A;
constexpr int L_OFF = L_A + 10496;
constexpr int MAPW = 256;
constexpr int L_MAP = L_M;
constexpr int L_FK = L_HIST;
static_assert(L_OH % 128 == 0 && L_ST + 20480 <= L_R1 && L_BS % 256 == 0 && L_W % 32 == 0 && L_W + 65536 <= L_EXT && L_EXT + 1280 <= L_H && L_H % 256 == 0 && L_M + 40 * MSTR <= L_H && L_END <= 163840, "lds map");

DI constexpr int piece_off(int i) { return 1040 * (i & 3) + 4352 * (i >> 2); }
DI float bf2f(unsigned u) { return __uint_as_float(u << 16); }
DI float bfhi(unsigned u) { return __uint_as_float(u & 0xffff0000u); }
DI unsigned pk2(float a, float b) {
  f32x2 v = {a, b};
  return __builtin_bit_cast(unsigned, __builtin_convertvector(v, bf16x2));
}
DI f32x16 mfma(u32x4 a, u32x4 b, f32x16 c) {
  return __builtin_amdgcn_mfma_f32_32x32x16_bf16(__builtin_bit_cast(bf16x8, a), __builtin_bit_cast(bf16x8, b), c, 0, 0, 0);
}
DI f32x16 zero16() {
  f32x16 z;
#pragma unroll
  for (int i = 0; i < 16; i++) z[i] = 0.f;
  return z;
}
DI u32x4 tr_read2(unsigned a0, unsigned a1) {
  const u32x2 lo = __builtin_bit_cast(u32x2, __builtin_amdgcn_ds_read_tr16_b64_v4i16((LDSP(s16x4))(size_t)a0));
  const u32x2 hi = __builtin_bit_cast(u32x2, __builtin_amdgcn_ds_read_tr16_b64_v4i16((LDSP(s16x4))(size_t)a1));
  const u32x4 v = {lo[0], lo[1], hi[0], hi[1]};
  return v;
}
DI int bucket_start(int b) { return (b * 625 + 15) >> 4; }
DI int wave_incl_scan(int x) {
  x += __builtin_amdgcn_update_dpp(0, x, 0x111, 0xf, 0xf, false);
  x += __builtin_amdgcn_update_dpp(0, x, 0x112, 0xf, 0xf, false);
  x += __builtin_amdgcn_update_dpp(0, x, 0x114, 0xf, 0xf, false);
  x += __builtin_amdgcn_update_dpp(0, x, 0x118, 0xf, 0xf, false);
  x += __builtin_amdgcn_update_dpp(0, x, 0x142, 0xa, 0xf, false);
  x += __builtin_amdgcn_update_dpp(0, x, 0x143, 0xc, 0xf, false);
  return x;
}
DI void dma16(const void* sbase, unsigned voff, unsigned lds_base) {
  asm volatile("s_mov_b32 m0, %2\n\ts_nop 0\n\tglobal_load_lds_dwordx4 %0, %1" ::"v"(voff), "s"(sbase), "s"(lds_base) : "memory");
}
template <int I>
DI void row_dma(u32x4 srd, unsigned key, unsigned lanec, unsigned lds_base, unsigned& t) {
  if (I & 3)
    asm volatile("v_and_or_b32 %0, %1, %2, %3\n\tbuffer_load_dwordx4 %0, %4, %5 offen offset:%6 sc1 lds"
                 : "+v"(t) : "v"(key), "s"(0xffffff00u), "v"(lanec), "s"(srd), "s"(3120 - 1040 * (I & 3)), "i"(1040 * (I & 3)) : "memory");
  else
    asm volatile("s_mov_b32 m0, %6\n\tv_and_or_b32 %0, %1, %2, %3\n\tbuffer_load_dwordx4 %0, %4, %5 offen sc1 lds"
                 : "+v"(t) : "v"(key), "s"(0xffffff00u), "v"(lanec), "s"(srd), "s"(3120), "s"(lds_base + 4352 * (I >> 2)) : "memory");
}
DI void wait_vm0() { asm volatile("s_waitcnt vmcnt(0)" ::: "memory"); }
DI void barrier() { asm volatile("s_waitcnt lgkmcnt(0)\n\ts_barrier" ::: "memory"); }

__global__ __launch_bounds__(512, 2) void egnn_fused(const u16* __restrict__ h, const u16* __restrict__ pos,
                                                     const u32x2* __restrict__ eattr, const int* __restrict__ src,
                                                     const int* __restrict__ dst, const u16* __restrict__ we,
                                                     const u16* __restrict__ wx, const u16* __restrict__ wh,
                                                     char* ws, u16* __restrict__ out) {
  __shared__ __attribute__((aligned(256))) char lds[L_END];
  const unsigned lbase = (unsigned)(size_t)lds;
  const int tid = threadIdx.x, lane = tid & 63;
  const int wv = __builtin_amdgcn_readfirstlane(tid >> 6);
  const int w = blockIdx.x;
  const int b = (w & 7) * 32 + (w >> 3);
  const int n0 = bucket_start(b), nsz = bucket_start(b + 1) - n0;
  const int r = lane & 31, hh = lane >> 5;
  const int blk = (lane >> 4) & 1, q4 = (lane & 15) >> 2, p4 = lane & 3;
  const __amdgpu_buffer_rsrc_t wsr = __builtin_amdgcn_make_buffer_rsrc(ws, 0, WS_END, 0x00020000);

  auto pos_tile = [&](int i) {
    const int j = i < 5 ? 8 * i + wv : i == 5 ? 46 + wv : i == 6 ? 40 + wv : 54 + wv;
    if (i < 6 || (i == 6 && wv < 6) || (i == 7 && (wv < 4 || (wv == 4 && lane < 38))))
      dma16(pos, (unsigned)(j * 1024 + lane * 16), lbase + L_ROWS + j * 1024);
  };

  int d[3], s[3];
  u32x2 a[3];
  unsigned tagv;
  const bool v2 = tid < CH - 1024;
  const unsigned wlb = (unsigned)((lane >> 4) * 256 + 16 * ((lane & 15) ^ ((lane >> 4) << 2)));
  if (wv < 4) {
    const int j = (w >> 3) * 4 + wv;
    if (j < 64) dma16(we, (unsigned)(j * 1024 + lane * 16), lbase + L_ST + wv * 1024);
    else if (j < 122) dma16(pos, (unsigned)((j - 64) * 1024 + lane * 16), lbase + L_ST + wv * 1024);
  }
  {
    const unsigned e0 = (unsigned)(w * CH + tid) * 4, e2 = v2 ? e0 + 4096 : e0;
    asm volatile("global_load_dword %0, %10, %14\n\tglobal_load_dword %1, %10, %14 offset:2048\n\tglobal_load_dword %2, %11, %14\n\t"
                 "global_load_dword %3, %10, %15\n\tglobal_load_dword %4, %10, %15 offset:2048\n\tglobal_load_dword %5, %11, %15\n\t"
                 "global_load_dwordx2 %6, %12, %16\n\tglobal_load_dwordx2 %7, %13, %16\n\tglobal_load_dwordx2 %8, %18, %16\n\t"
                 "global_load_dword %9, %19, %17 sc1"
                 : "=&v"(d[0]), "=&v"(d[1]), "=&v"(d[2]), "=&v"(s[0]), "=&v"(s[1]), "=&v"(s[2]), "=&v"(a[0]), "=&v"(a[1]), "=&v"(a[2]), "=&v"(tagv)
                 : "v"(e0), "v"(e2), "v"(2 * e0), "v"(2 * e0 + 4096), "s"(dst), "s"(src), "s"(eattr), "s"(ws), "v"(2 * e2), "v"(WS_EP)
                 : "memory");
    const int row = 4 * wv + (lane >> 4), c = lane & 15;
#pragma unroll
    for (int i = 0; i < 4; i++) dma16(we, (wlb ^ (16 * i)) + (32 + 4 * wv + i) * 1024, lbase + L_W + (32 + 4 * wv + i) * 1024);
    dma16(h, (unsigned)(min(n0 + row, NN - 1) * 256 + 16 * (c ^ (row & 15))), lbase + L_H + wv * 1024);
    if (wv < 2) dma16(h, (unsigned)(min(n0 + 32 + row, NN - 1) * 256 + 16 * (c ^ (row & 15))), lbase + L_H + (8 + wv) * 1024);
  }
  if (tid < 256) *(u32*)(lds + L_HIST + tid * 4) = 0u;
  else {
    const int t = tid - 256;
    if (t < 32) {
      const int j = (t >> 2) - 4 * ((t >> 1) & 1) - 2 * (t & 1);
      *(u32*)(lds + L_OH + t * 4) = j == 0 ? 0x3F80u : j == 1 ? 0x3F800000u : 0u;
      if (t < 16) *(u32*)(lds + L_GC + t * 4) = 0u;
    }
  }
  barrier();
  {
    if (wv == 3) dma16(we, 65536 + lane * 16, lbase + L_EXT);
    if (wv == 4 && lane < 16) dma16(we, 66560 + lane * 16, lbase + L_EXT + 1024);
    if (wv == 5 && lane < 16) dma16(wx, lane * 16, lbase + L_WX);
    if (wv == 6 && lane < 16) dma16(wh, (unsigned)(w * 256 + lane * 16), lbase + L_BS + 10240);
  }
#pragma unroll
  for (int i = 0; i < 4; i++) dma16(we, (wlb ^ (16 * i)) + (4 * wv + i) * 1024, lbase + L_W + (4 * wv + i) * 1024);
  const int T = wv & 3, nb = wv >> 2;
  const int loc = 5 * (4 * nb + (r >> 3)) + (r & 7);
  const bool valid = (r & 7) < 5 && loc < nsz;
  int ha = L_H + min(loc, 39) * 256 + 16 * (hh ^ (min(loc, 39) & 15));
  const unsigned trx0 = lbase + L_W + (unsigned)((8 * hh + q4) * 256), trs0 = (unsigned)((q4 << 2) | (2 * hh));
  unsigned b0 = trx0 + 32768 + 16 * ((unsigned)(8 * (T & 1) + 4 * (p4 & 1) + 2 * blk + (T >> 1)) ^ trs0) + 8 * (p4 >> 1);
  unsigned a0 = trx0 + 16 * ((unsigned)(4 * T + 2 * blk + (p4 >> 1)) ^ trs0) + 8 * (p4 & 1);
  int bo = L_BS + loc * 256 + 16 * ((4 * T + 2 * hh) ^ (loc & 15));
  unsigned gbo = WS_BT + (unsigned)((n0 + (tid >> 4)) * 256 + 16 * ((tid ^ (tid >> 4)) & 15));
  PIN(b0); PIN(a0); PIN(bo); PIN(gbo); PIN(ha);
  const int rowbuf = L_ROWS + wv * ROWBUF;
  const unsigned long long bbase = (unsigned long long)(ws + WS_BT - 3120);
  const u32x4 bsrd = {(unsigned)bbase, (unsigned)(bbase >> 32) & 0xffffu, (unsigned)((NN + 256) * 256 + 3120), 0x00020000u};
  asm volatile("s_waitcnt vmcnt(16)" : "+v"(d[0]), "+v"(d[1]), "+v"(d[2])::"memory");
  int key[3];
  unsigned rank[3];
#pragma unroll
  for (int k = 0; k < 3; k++) {
    key[k] = (int)((unsigned)__umul24((unsigned)d[k], 429497u) >> 24);
    rank[k] = 0;
    if (k < 2 || v2) rank[k] = atomicAdd((unsigned*)(lds + L_HIST) + key[k], 1u);
  }
  asm volatile("s_waitcnt vmcnt(9)" : "+v"(s[0]), "+v"(s[1]), "+v"(s[2]), "+v"(a[0]), "+v"(a[1]), "+v"(a[2]), "+v"(tagv)::"memory");
  const unsigned tag = (unsigned)__builtin_amdgcn_readfirstlane((int)tagv) + 1u;
  u32x4 rec[3];
#pragma unroll
  for (int k = 0; k < 3; k++) {
    const int lc = d[k] - bucket_start(key[k]);
    const int g = (lc * 205) >> 10;
    rec[k][0] = a[k][0];
    rec[k][1] = (a[k][1] & 0xffffu) | (tag << 16);
    rec[k][2] = (a[k][1] >> 16) | (tag << 16);
    rec[k][3] = ((unsigned)s[k] << 8) | (unsigned)(16 * lc - 79 * g) | ((tag >> 16) << 22);
    PIN(rec[k]);
  }
  barrier();
  u32x4 off4;
  unsigned oend;
  {
    const u32x4 cnt = *(const u32x4*)(lds + L_HIST + lane * 16);
    const unsigned tot = cnt[0] + cnt[1] + cnt[2] + cnt[3];
    oend = (unsigned)wave_incl_scan((int)tot);
    off4[0] = oend - tot;
    off4[1] = off4[0] + cnt[0];
    off4[2] = off4[1] + cnt[1];
    off4[3] = off4[2] + cnt[2];
    *(u32x4*)(lds + L_OFF + lane * 16) = off4;
#pragma unroll
    for (int k = 0; k < 3; k++) rank[k] += *(const u32*)(lds + L_OFF + key[k] * 4);
#pragma unroll
    for (int k = 0; k < 3; k++)
      if (k < 2 || v2) *(u32x4*)(lds + L_ST + rank[k] * 16) = rec[k];
  }
  wait_vm0();
  barrier();
  u32x2 at[4], ent;
  {
    u32x4 v0 = *(const u32x4*)(lds + L_ST + tid * 16);
    u32x4 v1 = *(const u32x4*)(lds + L_ST + 8192 + tid * 16);
    u32x4 v2_ = *(const u32x4*)(lds + L_ST + 16384 + (tid & 255) * 16);
    const unsigned b1 = (b0 ^ 16) + 1024, a1 = (a0 ^ 16) + 1024;
    u32x4 hf[8], wf[8];
#pragma unroll
    for (int ks = 0; ks < 8; ks++) {
      hf[ks] = *(const u32x4*)(lds + (ha ^ (32 * ks)));
      wf[ks] = tr_read2(b0 + ks * 4096, b1 + ks * 4096);
    }
    SB();
    __builtin_amdgcn_raw_buffer_store_b128(v0, wsr, WS_R + (unsigned)(w * CHS + tid) * 16, 0, 16);
    __builtin_amdgcn_raw_buffer_store_b128(v1, wsr, WS_R + (unsigned)(w * CHS + 512 + tid) * 16, 0, 16);
    if (tid < 256) __builtin_amdgcn_raw_buffer_store_b128(v2_, wsr, WS_R + (unsigned)(w * CHS + 1024 + tid) * 16, 0, 16);
    f32x16 acc = zero16();
#pragma unroll
    for (int ks = 0; ks < 8; ks++) {
      acc = mfma(wf[ks], hf[ks], acc);
      PIN(acc);
      pos_tile(ks);
    }
    SB();
#pragma unroll
    for (int ks = 0; ks < 8; ks++) wf[ks] = tr_read2(a0 + ks * 4096, a1 + ks * 4096);
    SB();
    u32x4 bt0, bt1;
#pragma unroll
    for (int i = 0; i < 4; i++) {
      bt0[i] = pk2(acc[2 * i] * NLOG2E, acc[2 * i + 1] * NLOG2E);
      bt1[i] = pk2(acc[8 + 2 * i] * NLOG2E, acc[9 + 2 * i] * NLOG2E);
    }
    if ((r & 7) < 5) {
      *(u32x4*)(lds + bo) = bt0;
      *(u32x4*)(lds + (bo ^ 16)) = bt1;
    }
    barrier();
    if (wv == 7) {
      const u32x4 g0 = {tag, off4[0] | (off4[1] << 16), tag, off4[1] | (off4[2] << 16)};
      const u32x4 g1 = {tag, off4[2] | (off4[3] << 16), tag, off4[3] | (oend << 16)};
      const unsigned off = WS_TAB + (unsigned)(w * NB + 4 * lane) * 8;
      __builtin_amdgcn_raw_buffer_store_b128(g0, wsr, off, 0, 16);
      __builtin_amdgcn_raw_buffer_store_b128(g1, wsr, off + 16, 0, 16);
    }
    const u32x4 vb0 = *(const u32x4*)(lds + L_BS + tid * 16);
    const u32x4 vb1 = *(const u32x4*)(lds + L_BS + 8192 + min(tid, 143) * 16);
    __builtin_amdgcn_raw_buffer_store_b128(vb0, wsr, gbo, 0, 16);
    if (tid < 16 * (nsz - 32) || (tid >> 4) == 8)
      __builtin_amdgcn_raw_buffer_store_b128(vb1, wsr, tid < 128 ? gbo + 8192 : WS_BT + (unsigned)((NN + w) * 256 + (tid & 15) * 16), 0, 16);
    asm volatile("" ::: "memory");
    acc = zero16();
#pragma unroll
    for (int ks = 0; ks < 8; ks++) {
      acc = mfma(hf[ks], wf[ks], acc);
      PIN(acc);
    }
#pragma unroll
    for (int i = 0; i < 4; i++) {
      at[i][0] = pk2(acc[4 * i] * NLOG2E, acc[4 * i + 1] * NLOG2E);
      at[i][1] = pk2(acc[4 * i + 2] * NLOG2E, acc[4 * i + 3] * NLOG2E);
      PIN(at[i]);
    }
  }
  {
    const u32x4 z = {0u, 0u, 0u, 0u};
    *(u32x4*)(lds + L_S + tid * 16) = z;
    *(u32x4*)(lds + L_S + 8192 + tid * 16) = z;
    if (tid < 256) {
      *(u32x4*)(lds + L_S + 16384 + tid * 16) = z;
    } else {
      const int t = tid - 256, v = t >> 5, q = (t >> 3) & 3, i = t & 7;
      *(u32*)(lds + L_FK + t * 4) = (unsigned)(NN + (i < 4 ? 128 + 32 * q + 4 * v + i : 16 * v + 4 * (i - 4) + q)) << 8;
    }
  }
  u32x4 extB[4];
#pragma unroll
  for (int t = 0; t < 4; t++) {
    const u16* wp = (const u16*)(lds + L_EXT) + 32 * t + r;
    const float w0 = bf2f(wp[(1 + 2 * hh) * 128]) * NLOG2E, w1 = bf2f(wp[(2 + 2 * hh) * 128]) * NLOG2E, w2 = bf2f(wp[0]) * NLOG2E;
    extB[t][0] = 0u;
    extB[t][1] = 0u;
    extB[t][2] = pk2(w0, w1);
    extB[t][3] = hh ? 0u : pk2(w2, 0.f);
    PIN(extB[t]);
  }
  asm volatile("global_load_dwordx2 %0, %1, %2 sc1\n\ts_waitcnt vmcnt(0)" : "=&v"(ent) : "v"(WS_TAB + (unsigned)((tid >> 1) * NB + b) * 8), "s"(ws) : "memory");
  barrier();
  if (tid == 0) __builtin_amdgcn_raw_buffer_store_b32(tag, wsr, WS_FB + (unsigned)w * 4, 0, 16);
#pragma unroll
  for (int i = 0; i < 4; i++) *(u32x2*)(lds + L_A + (4 * nb + i) * 2048 + T * 512 + lane * 8) = at[i];

  bool wh_loaded = false, slow = false;
  int lo = 0, quota = MAPW, gq = wv;
  unsigned pin = 0, atmp = 0, kdma = 0;
  f32x16 Y[4];
  bool fresh = true;
  while (true) {
    wh_loaded = false;
    int tq = tid;
    PIN(tq);
    const int lq = tq & 63;
    int kc, kfirst, kpre, wtot;
    {
      const unsigned tabq = WS_TAB + (unsigned)((tq >> 1) * NB + b) * 8;
      u32x2 gq2 = ent;
      ent[0] = tag + 1u;
#pragma clang loop unroll(disable)
      for (int spin = 0; spin < (1 << 22); spin++) {
        if (gq2[0] != tag) asm volatile("global_load_dwordx2 %0, %1, %2 sc1\n\ts_waitcnt vmcnt(0)" : "=&v"(gq2) : "v"(tabq), "s"(ws) : "memory");
        if (__all(gq2[0] == tag)) break;
        __builtin_amdgcn_s_sleep(1);
      }
      const int o0 = min((int)(gq2[1] & 0xffffu), CH), o1 = min((int)(gq2[1] >> 16), CH);
      kc = max(o1 - o0, 0);
      kfirst = (tq >> 1) * CHS + o0;
      const int inc = wave_incl_scan((tq & 1) ? 0 : kc);
      kpre = inc - kc;
      wtot = __builtin_amdgcn_readlane(inc, 63);
      if (!slow && lq == 0) atomicMax((unsigned*)(lds + L_GC + 32), (unsigned)wtot);
    }
    u32x4 I0, I1;
    int rdA, rdB, swr0, srd0;
    u32x3 q[4];
    u32x2 pd[4], ps[4];
    int ra1[4], par[4];
    bool four;
    {
      const int map = L_MAP + wv * MAPW * 4;
      const int nw = min(wtot - lo, quota);
      const int jhi = min(kc, lo + quota - kpre);
#pragma clang loop vectorize(disable) unroll(disable)
      for (int j = max(0, lo - kpre) + ((tq ^ max(0, lo - kpre)) & 1); j < jhi; j += 2) *(u32*)(lds + map + (kpre + j - lo) * 4) = (unsigned)(kfirst + j) * 16u;
      unsigned ix[4];
#pragma unroll
      for (int k = 0; k < 4; k++) {
        const unsigned mi = *(const u32*)(lds + map + (64 * k + lq) * 4);
        ix[k] = 64 * k + lq < nw ? mi : 0u;
      }
      u32x4 q4_[4];
      unsigned fb;
      const unsigned fbo = WS_FB + (unsigned)(32 * wv + (lq & 31)) * 4;
      asm volatile("global_load_dwordx4 %0, %5, %9 sc1\n\tglobal_load_dwordx4 %1, %6, %9 sc1\n\tglobal_load_dwordx4 %2, %7, %9 sc1\n\t"
                   "global_load_dwordx4 %3, %8, %9 sc1\n\tglobal_load_dword %4, %10, %11 sc1"
                   : "=&v"(q4_[0]), "=&v"(q4_[1]), "=&v"(q4_[2]), "=&v"(q4_[3]), "=&v"(fb)
                   : "v"(ix[0]), "v"(ix[1]), "v"(ix[2]), "v"(ix[3]), "s"(ws + WS_R), "v"(fbo), "s"(ws) : "memory");
      {
        const int r = lq & 31, hh = lq >> 5;
        const unsigned one = (r & 1) ? 0x3F800000u : 0x3F80u;
#pragma unroll
        for (int jj = 0; jj < 4; jj++) {
          const bool m = ((r & 7) >> 1) == jj;
          I0[jj] = (m && (r >> 3) == hh) ? one : 0u;
          I1[jj] = (m && (r >> 3) == 2 + hh) ? one : 0u;
        }
        kdma = 16 * ((lq & 15) ^ (4 * (lq >> 4)));
        const int rd0 = rowbuf + 1040 * (r & 3) + 4352 * ((r >> 2) & 1) + (r >> 3) * 256;
        rdA = rd0 + 64 * ((2 * hh) ^ (r >> 3));
        rdB = rd0 + 64 * ((2 * hh + 1) ^ (r >> 3));
        swr0 = L_S + 32 * (r >> 4) + 16 * ((r >> 2) & 1) + 8 * ((r >> 3) & 1) + 2 * (r & 3) + 160 * hh;
        srd0 = L_S + r * 80 + 16 * hh;
        PIN(I0); PIN(I1); PIN(kdma); PIN(rdA); PIN(rdB); PIN(swr0); PIN(srd0);
        if (fresh) {
#pragma unroll
          for (int t = 0; t < 4; t++) {
            Y[t] = zero16();
            PIN(Y[t]);
          }
        }
        fresh = false;
      }
      asm volatile("s_waitcnt vmcnt(0)" : "+v"(q4_[0]), "+v"(q4_[1]), "+v"(q4_[2]), "+v"(q4_[3]), "+v"(fb)::"memory");
#pragma clang loop unroll(disable)
      for (int spin = 0; spin < (1 << 22); spin++) {
        unsigned bad = 0;
#pragma unroll
        for (int k = 0; k < 4; k++) {
          const unsigned bk = (((q4_[k][1] ^ (tag << 16)) | (q4_[k][2] ^ (tag << 16))) >> 16) | ((q4_[k][3] ^ ((tag >> 16) << 22)) >> 22);
          bad |= 64 * k + lq < nw ? bk : 0u;
        }
        if (!__any(bad != 0)) break;
        __builtin_amdgcn_s_sleep(4);
        asm volatile("global_load_dwordx4 %0, %4, %8 sc1\n\tglobal_load_dwordx4 %1, %5, %8 sc1\n\tglobal_load_dwordx4 %2, %6, %8 sc1\n\t"
                     "global_load_dwordx4 %3, %7, %8 sc1\n\ts_waitcnt vmcnt(0)"
                     : "=&v"(q4_[0]), "=&v"(q4_[1]), "=&v"(q4_[2]), "=&v"(q4_[3])
                     : "v"(ix[0]), "v"(ix[1]), "v"(ix[2]), "v"(ix[3]), "s"(ws + WS_R) : "memory");
      }
#pragma unroll
      for (int k = 0; k < 4; k++) {
        q[k][0] = q4_[k][0];
        q[k][1] = (q4_[k][1] & 0xffffu) | (q4_[k][2] << 16);
        q[k][2] = q4_[k][3] & 0x3fffffu;
      }
      four = nw > 192;
      unsigned rk[4];
      auto deal1 = [&](int k) {
        rk[k] = CAPW;
        if (64 * k + lq < nw) rk[k] = atomicAdd((unsigned*)(lds + L_GC) + (q[k][2] & 7u), 1u);
        const int dn = n0 + 5 * (int)(q[k][2] & 7u) + (int)((q[k][2] >> 4) & 7u), sn = min((int)(q[k][2] >> 8), NN - 1);
        const int od = L_ROWS + ((int)__umul24(6u, (unsigned)dn) & ~3), os = L_ROWS + ((6 * sn) & ~3);
        pd[k][0] = *(const u32*)(lds + od); pd[k][1] = *(const u32*)(lds + od + 4);
        ps[k][0] = *(const u32*)(lds + os); ps[k][1] = *(const u32*)(lds + os + 4);
        par[k] = (dn & 1) * 16;
      };
      auto deal2 = [&](int k) {
        const bool ok = rk[k] < CAPW;
        const int e = (int)((q[k][2] & 7u) * CAPW + rk[k]);
        *(u32*)(lds + (ok ? L_R2 + e * 4 : L_DUM + 96)) = q[k][2];
        ra1[k] = ok ? L_R1 + e * 16 : L_DUM + 96;
      };
      deal1(0); deal1(1); deal1(2);
      deal2(0); deal2(1); deal2(2);
      pd[3] = ps[3] = u32x2{0u, 0u};
      par[3] = 0;
      ra1[3] = L_DUM + 96;
      if (four) { deal1(3); deal2(3); }
#pragma clang loop unroll(disable)
      for (int spin = 0; spin < (1 << 22) && !__all(fb == tag); spin++) {
        __builtin_amdgcn_s_sleep(1);
        asm volatile("global_load_dword %0, %1, %2 sc1\n\ts_waitcnt vmcnt(0)" : "=&v"(fb) : "v"(fbo), "s"(ws) : "memory");
      }
    }
    barrier();
    const int cl = *(const u32*)(lds + L_GC + (lane & 7) * 4);
    int ci = *(const u32*)(lds + L_GC + (lane >> 3) * 4);
    PIN(ci);
    const int maxtot = __builtin_amdgcn_readfirstlane(*(const u32*)(lds + L_GC + 32));
    if (!slow) {
      const unsigned long long gt = __ballot(((cl << 3) | (lane & 7)) > ((ci << 3) | (lane >> 3)));
      const int rk = __builtin_popcount((unsigned)(gt >> (8 * (lane & 7))) & 0xffu);
      const int cmax = __any(cl > CAPW) ? CAPW + 1 : 0;
      if (maxtot > MAPW || cmax > CAPW) {
        slow = true;
        quota = CAPW / 8;
        barrier();
        if (tid < 8) *(u32*)(lds + L_GC + tid * 4) = 0u;
        barrier();
        continue;
      }
      gq = __builtin_ctzll(__ballot(rk == (wv < 4 ? wv : 11 - wv)));
    }
    const int n = __builtin_amdgcn_readlane(cl, gq);
    const unsigned ohc = (unsigned)(L_OH + hh * 8);
    const int swr = swr0 + gq * 2560 - 20 * (int)ohc, srd = srd0 + gq * 2560;
    const int l1 = L_R1 + gq * CAPW * 16, l2 = L_R2 + gq * CAPW * 4;
    const int nt = (n + 31) >> 5;
    u32x4 rows[8], extA, S0, S1, ra, rb, m1p = {0u, 0u, 0u, 0u};
    unsigned r2;
    int sp;
    f32x16 XA, XB;
    auto list_read = [&](int tl) {
      const int list = tl < nt ? l2 + tl * 128 : L_FK + wv * 128;
      ra = *(const u32x4*)(lds + list + (lane >> 4) * 32);
      rb = *(const u32x4*)(lds + list + (lane >> 4) * 32 + 16);
    };
    auto dma_piece = [&](int, int i) {
      if (i == 0) row_dma<0>(bsrd, ra[0], kdma, lbase + rowbuf, atmp);
      if (i == 1) row_dma<1>(bsrd, ra[1], kdma, lbase + rowbuf, atmp);
      if (i == 2) row_dma<2>(bsrd, ra[2], kdma, lbase + rowbuf, atmp);
      if (i == 3) row_dma<3>(bsrd, ra[3], kdma, lbase + rowbuf, atmp);
      if (i == 4) row_dma<4>(bsrd, rb[0], kdma, lbase + rowbuf, atmp);
      if (i == 5) row_dma<5>(bsrd, rb[1], kdma, lbase + rowbuf, atmp);
      if (i == 6) row_dma<6>(bsrd, rb[2], kdma, lbase + rowbuf, atmp);
      if (i == 7) row_dma<7>(bsrd, rb[3], kdma, lbase + rowbuf, atmp);
    };
    auto meta_read = [&](int tl) {
      const u32x2 r1 = *(const u32x2*)(lds + l1 + (tl * 32 + r) * 16 + hh * 8);
      extA[2] = r1[0];
      extA[3] = r1[1];
      r2 = *(const u32*)(lds + l2 + (tl * 32 + r) * 4);
    };
    auto meta_use = [&](int) {
      const unsigned oa = (r2 & 0x70u) | ohc;
      const u32x2 oh = *(const u32x2*)(lds + oa);
      extA[0] = oh[0];
      extA[1] = oh[1];
      sp = (int)__umul24(oa, 20u) + swr;
    };
    auto s_write = [&]() {
      *(u16*)(lds + sp) = (u16)(hh ? extA[3] : 0x3F80u);
      *(u16*)(lds + sp + 80) = (u16)(extA[3] >> 16);
    };
    auto rows_read = [&]() {
#pragma unroll
      for (int j = 0; j < 8; j++) rows[j] = *(const u32x4*)(lds + (j < 4 ? rdA : rdB) + 16 * (j & 3));
    };
    auto rows_pin = [&]() {
      asm volatile("" : "+v"(rows[0]), "+v"(rows[1]), "+v"(rows[2]), "+v"(rows[3]), "+v"(rows[4]), "+v"(rows[5]), "+v"(rows[6]), "+v"(rows[7]));
    };

    list_read(0);
    if (n > 0 && wv < 4) {
#pragma unroll
      for (int i = 0; i < 8; i++) dma_piece(0, i);
    }
    auto finish = [&](int k) {
      const unsigned long long ud = (((unsigned long long)pd[k][1] << 32) | pd[k][0]) >> par[k];
      const unsigned long long us = (((unsigned long long)ps[k][1] << 32) | ps[k][0]) >> ((q[k][2] >> 4) & 16u);
      const unsigned dlo = (unsigned)ud, dhi = (unsigned)(ud >> 32), slo = (unsigned)us, shi = (unsigned)(us >> 32);
      const unsigned dxy = pk2(bf2f(dlo) - bf2f(slo), bfhi(dlo) - bfhi(slo));
      const unsigned dz_ = pk2(bf2f(dhi) - bf2f(shi), 0.f);
      const float dx = bf2f(dxy), dy = bfhi(dxy), dz = bf2f(dz_);
      const unsigned sxy = pk2(dx * dx, dy * dy), sz = pk2(dz * dz, 0.f);
      const unsigned d2 = pk2((bf2f(sxy) + bfhi(sxy)) + bf2f(sz), 0.f) & 0xffffu;
      const u32x4 rec = {q[k][0], d2 | (dxy << 16), q[k][1], (dxy >> 16) | (dz_ << 16)};
      *(u32x4*)(lds + ra1[k]) = rec;
    };
    finish(0);
    finish(1);
    finish(2);
    if (four) finish(3);
    if (lane < 32 * nt - n) {
      const u32x4 z = {0u, 0u, 0u, 0u};
      *(u32x4*)(lds + l1 + (n + lane) * 16) = z;
      *(u32*)(lds + l2 + (n + lane) * 4) = 0x70u;
    }
    if (w == 0 && tid == 0) __builtin_amdgcn_raw_buffer_store_b32(tag, wsr, WS_EP, 0, 16);
    pin = pos[min(3 * n0 + 15 * gq + (lq & 15), 3 * NN - 1)];
#pragma unroll
    for (int t = 0; t < 4; t++) {
      const u32x2 v = *(const u32x2*)(lds + L_A + gq * 2048 + t * 512 + lq * 8);
      extB[t][0] = v[0];
      extB[t][1] = v[1];
    }
    barrier();
    if (n > 0) {
      if (wv >= 4) {
#pragma unroll
        for (int i = 0; i < 8; i++) dma_piece(0, i);
      }
      meta_read(0);
      list_read(1);
      meta_use(0);
      s_write();
      S1 = m1p;
      wait_vm0();
      PIN(pin);
      rows_read();
      rows_pin();
      XA = mfma(rows[0], I0, zero16());
      XA = mfma(rows[1], I1, XA);
      XA = mfma(extA, extB[0], XA);

      const int nfull = (n + 15) >> 5;
      int first = 1;
      for (int tl = 0; tl < nfull; tl++) {
        asm volatile("" : "+s"(first));
#pragma unroll
        for (int t = 0; t < 4; t++) {
          f32x16& Xc = (t & 1) ? XB : XA;
          f32x16& Xn = (t & 1) ? XA : XB;
          const int tn = (t + 1) & 3;
          auto gap = [&](int k) {
            SB();
            if (t == 0 && k == 0 && first) {
#pragma unroll
              for (int i = 0; i < 8; i++) dma_piece(1, i);
            }
            if (t == 0 && k == 0) S0 = *(const u32x4*)(lds + srd);
            if (t == 0 && k == 3) S1 = *(const u32x4*)(lds + srd + 32);
            if (t == 0 && k == 4) {
              *(u16*)(lds + sp) = 0;
              *(u16*)(lds + sp + 80) = 0;
            }
            if (t == 2 && k == 1) meta_read(tl + 1);
            if (t == 2 && k == 4) meta_use(tl + 1);
            if (t == 2 && k == 3) wait_vm0();
            if (t == 2 && k >= 3 && k < 7) {
              rows[2 * k - 6] = *(const u32x4*)(lds + (k < 5 ? rdA : rdB) + 16 * ((2 * k - 6) & 3));
              rows[2 * k - 5] = *(const u32x4*)(lds + (k < 5 ? rdA : rdB) + 16 * ((2 * k - 5) & 3));
            }
            if (t == 2 && k == 7) list_read(tl + 2);
            if (t == 3 && k < 8) dma_piece(tl + 2, k);
            if (t == 3 && k == 8) s_write();
            SB();
          };
          f32x2 dd[8];
          if (t == 3 && wv >= 4) __builtin_amdgcn_s_setprio(1);
          if (t == 2 && wv >= 4) __builtin_amdgcn_s_setprio(0);
          SB();
          Xn = mfma(rows[2 * tn], I0, zero16());
          PIN(Xn);
          SB();
#pragma unroll
          for (int g = 0; g < 4; g++) {
#pragma unroll
            for (int i = 2 * g; i < 2 * g + 2; i++) {
              dd[i][0] = __builtin_amdgcn_exp2f(Xc[2 * i]);
              dd[i][1] = __builtin_amdgcn_exp2f(Xc[2 * i + 1]);
              PIN(dd[i]);
            }
            SB();
            if (g == 0) { Xn = mfma(rows[2 * tn + 1], I1, Xn); PIN(Xn); }
            if (g == 1) { Xn = mfma(extA, extB[tn], Xn); PIN(Xn); }
            if (g == 2) { Y[(t + 3) & 3] = mfma(S1, m1p, Y[(t + 3) & 3]); PIN(Y[(t + 3) & 3]); }
            gap(g);
          }
#pragma unroll
          for (int i = 0; i < 8; i++) { dd[i] += 1.0f; if (i < 7) PIN(dd[i]); }
          gap(4);
#pragma unroll
          for (int g = 0; g < 4; g++) {
#pragma unroll
            for (int i = 2 * g; i < 2 * g + 2; i++) {
              dd[i][0] = __builtin_amdgcn_rcpf(dd[i][0]);
              dd[i][1] = __builtin_amdgcn_rcpf(dd[i][1]);
              PIN(dd[i]);
            }
            gap(5 + g);
          }
          u32x4 m0;
#pragma unroll
          for (int i = 0; i < 4; i++) { f32x2 x = {Xc[2 * i], Xc[2 * i + 1]}; dd[i] *= x; }
#pragma unroll
          for (int i = 0; i < 4; i++) m0[i] = __builtin_bit_cast(unsigned, __builtin_convertvector(dd[i], bf16x2));
          SB();
#pragma unroll
          for (int i = 4; i < 8; i++) { f32x2 x = {Xc[2 * i], Xc[2 * i + 1]}; dd[i] *= x; if (i < 7) PIN(dd[i]); }
          SB();
          Y[t] = mfma(S0, m0, Y[t]);
          PIN(Y[t]);
          SB();
#pragma unroll
          for (int i = 0; i < 4; i++) m1p[i] = __builtin_bit_cast(unsigned, __builtin_convertvector(dd[4 + i], bf16x2));
          PIN(m1p);
          if (t == 2) rows_pin();
        }
        first = 0;
      }
      SB();
      Y[3] = mfma(S1, m1p, Y[3]);
      S0 = *(const u32x4*)(lds + srd);
      *(u16*)(lds + sp) = 0;
      *(u16*)(lds + sp + 80) = 0;
      if (nfull < nt) {
#pragma unroll
        for (int t = 0; t < 4; t++) {
          f32x16& Xc = (t & 1) ? XB : XA;
          f32x16& Xn = (t & 1) ? XA : XB;
          f32x2 dd[4];
          if (t < 2 && nfull == 0) {
#pragma unroll
            for (int i = 0; i < 4; i++) dma_piece(nt, 4 * t + i);
          }
          SB();
          if (t < 3) { Xn = mfma(rows[2 * t + 2], I0, zero16()); PIN(Xn); }
          SB();
#pragma unroll
          for (int i = 0; i < 2; i++) { dd[i][0] = __builtin_amdgcn_exp2f(Xc[2 * i]); dd[i][1] = __builtin_amdgcn_exp2f(Xc[2 * i + 1]); }
          SB();
          if (t < 3) { Xn = mfma(rows[2 * t + 3], I1, Xn); PIN(Xn); }
          SB();
#pragma unroll
          for (int i = 2; i < 4; i++) { dd[i][0] = __builtin_amdgcn_exp2f(Xc[2 * i]); dd[i][1] = __builtin_amdgcn_exp2f(Xc[2 * i + 1]); }
          SB();
          if (t < 3) { Xn = mfma(extA, extB[t + 1], Xn); PIN(Xn); }
          SB();
#pragma unroll
          for (int i = 0; i < 4; i++) dd[i] += 1.0f;
          SB();
#pragma unroll
          for (int i = 0; i < 4; i++) { dd[i][0] = __builtin_amdgcn_rcpf(dd[i][0]); dd[i][1] = __builtin_amdgcn_rcpf(dd[i][1]); }
          SB();
          u32x4 m0;
#pragma unroll
          for (int i = 0; i < 4; i++) { f32x2 x = {Xc[2 * i], Xc[2 * i + 1]}; dd[i] *= x; }
#pragma unroll
          for (int i = 0; i < 4; i++) m0[i] = __builtin_bit_cast(unsigned, __builtin_convertvector(dd[i], bf16x2));
          SB();
          Y[t] = mfma(S0, m0, Y[t]);
          if (t < 3) PIN(Xn);
        }
      }
      wh_loaded = true;
      wait_vm0();
    }
    if (!slow) break;
    lo += quota;
    if (lo >= maxtot) break;
    barrier();
    if (tid < 8) *(u32*)(lds + L_GC + tid * 4) = 0u;
#pragma unroll
    for (int i = 0; i < 8; i++) pos_tile(i);
    wait_vm0();
    barrier();
  }
  if (!wh_loaded) {
    wait_vm0();
    PIN(pin);
    const u32x4 fa = *(const u32x4*)(lds + L_FK + wv * 128 + (lane >> 4) * 32), fb = *(const u32x4*)(lds + L_FK + wv * 128 + (lane >> 4) * 32 + 16);
    row_dma<0>(bsrd, fa[0], kdma, lbase + rowbuf, atmp); row_dma<1>(bsrd, fa[1], kdma, lbase + rowbuf, atmp);
    row_dma<2>(bsrd, fa[2], kdma, lbase + rowbuf, atmp); row_dma<3>(bsrd, fa[3], kdma, lbase + rowbuf, atmp);
    row_dma<4>(bsrd, fb[0], kdma, lbase + rowbuf, atmp); row_dma<5>(bsrd, fb[1], kdma, lbase + rowbuf, atmp);
    row_dma<6>(bsrd, fb[2], kdma, lbase + rowbuf, atmp); row_dma<7>(bsrd, fb[3], kdma, lbase + rowbuf, atmp);
    wait_vm0();
  }

#pragma unroll
  for (int i = 0; i < 3; i++) {
    if (i < 2 || hh == 0) {
      const u32x2 mv = {pk2(Y[0][4 * i] * NLN2, Y[1][4 * i] * NLN2), pk2(Y[2][4 * i] * NLN2, Y[3][4 * i] * NLN2)};
      *(u32x2*)(lds + L_M + (5 * gq + 2 * i + hh) * MSTR + 8 * r) = mv;
    }
  }
  u32x4 hq[8], res[2];
  unsigned wxr[4];
#pragma unroll
  for (int ks = 0; ks < 8; ks++) hq[ks] = *(const u32x4*)(lds + (ha ^ (32 * ks)));
  {
    const int ra_ = (ha ^ (16 * hh)) ^ (64 * T + 32 * hh);
    res[0] = *(const u32x4*)(lds + ra_);
    res[1] = *(const u32x4*)(lds + (ra_ ^ 16));
  }
#pragma unroll
  for (int t = 0; t < 4; t++) wxr[t] = ((const u16*)(lds + L_WX))[32 * t + r];
  unsigned wa = lbase + L_ROWS + 2080 * hh + 256 * q4 + 64 * (T ^ q4) + 32 * (p4 & 1) + 16 * blk + 8 * (p4 >> 1);
  int ma = L_M + min(loc, 39) * MSTR + 16 * hh;
  PIN(wa);
  PIN(ma);
  barrier();
  {
    u32x4 w1[8], w2[8], mq[8];
#pragma unroll
    for (int ks = 0; ks < 8; ks++) w1[ks] = tr_read2(wa + ks * ROWBUF + 4352, wa + ks * ROWBUF + 4352 + 1040);
#pragma unroll
    for (int ks = 0; ks < 8; ks++) {
      w2[ks] = tr_read2(wa + ks * ROWBUF, wa + ks * ROWBUF + 1040);
      mq[ks] = *(const u32x4*)(lds + ma + 32 * ks);
    }
    SB();
    f32x16 P = zero16();
    float zz[9];
#pragma unroll
    for (int ks = 0; ks < 16; ks++) {
      P = mfma(ks < 8 ? w1[ks & 7] : w2[ks & 7], ks < 8 ? hq[ks & 7] : mq[ks & 7], P);
      PIN(P);
      SB();
      if (ks < 12) {
        const int t = ks / 3;
        const float wxv = bf2f(wxr[t]) * NLN2;
#pragma unroll
        for (int i = 3 * (ks % 3); i < 3 * (ks % 3) + 3; i++) {
          const float y = Y[t][4 * (i / 3) + 1 + i % 3];
          zz[i] = t == 0 ? y * wxv : __builtin_fmaf(y, wxv, zz[i]);
          PIN(zz[i]);
        }
      }
      SB();
    }
    SB();
    f32x2 x[8], e[8], rv[8];
#pragma unroll
    for (int i = 0; i < 8; i++) {
      const unsigned rs = res[i >> 2][i & 3];
      rv[i][0] = bf2f(rs);
      rv[i][1] = bfhi(rs);
      PIN(rv[i]);
    }
    SB();
#pragma unroll
    for (int i = 0; i < 8; i++) {
      x[i][0] = P[2 * i];
      x[i][1] = P[2 * i + 1];
      e[i] = x[i] * NLOG2E;
      if (i < 7) PIN(e[i]);
    }
    SB();
#pragma unroll
    for (int i = 0; i < 8; i++) { e[i][0] = __builtin_amdgcn_exp2f(e[i][0]); e[i][1] = __builtin_amdgcn_exp2f(e[i][1]); PIN(e[i]); }
    SB();
#pragma unroll
    for (int i = 0; i < 8; i++) { e[i] += 1.0f; if (i < 7) PIN(e[i]); }
    SB();
#pragma unroll
    for (int i = 0; i < 8; i++) { e[i][0] = __builtin_amdgcn_rcpf(e[i][0]); e[i][1] = __builtin_amdgcn_rcpf(e[i][1]); PIN(e[i]); }
    SB();
#pragma unroll
    for (int i = 0; i < 8; i++) { e[i] = __builtin_elementwise_fma(x[i], e[i], rv[i]); if (i < 7) PIN(e[i]); }
    SB();
    u32x4 ov[2];
#pragma unroll
    for (int i = 0; i < 8; i++) ov[i >> 2][i & 3] = pk2(e[i][0], e[i][1]);
    PIN(ov[0]);
    PIN(ov[1]);
    if (valid) {
      const __amdgpu_buffer_rsrc_t osr = __builtin_amdgcn_make_buffer_rsrc(out, 0, NN * 262, 0x00020000);
      const unsigned oo = (unsigned)(n0 + loc) * 256 + 64 * T + 32 * hh;
      __builtin_amdgcn_raw_buffer_store_b128(ov[0], osr, oo, 0, 0);
      __builtin_amdgcn_raw_buffer_store_b128(ov[1], osr, oo + 16, 0, 0);
    }
#define DPP9(ctl)                                                                                                         \
  "v_add_f32_dpp %0, %0, %0 " ctl "\n\tv_add_f32_dpp %1, %1, %1 " ctl "\n\tv_add_f32_dpp %2, %2, %2 " ctl "\n\t"          \
  "v_add_f32_dpp %3, %3, %3 " ctl "\n\tv_add_f32_dpp %4, %4, %4 " ctl "\n\tv_add_f32_dpp %5, %5, %5 " ctl "\n\t"          \
  "v_add_f32_dpp %6, %6, %6 " ctl "\n\tv_add_f32_dpp %7, %7, %7 " ctl "\n\tv_add_f32_dpp %8, %8, %8 " ctl "\n\t"
    asm volatile("s_nop 1\n\t" DPP9("row_shr:1 row_mask:0xf bank_mask:0xf bound_ctrl:1") DPP9("row_shr:2 row_mask:0xf bank_mask:0xf bound_ctrl:1")
                 DPP9("row_shr:4 row_mask:0xf bank_mask:0xf bound_ctrl:1") DPP9("row_shr:8 row_mask:0xf bank_mask:0xf bound_ctrl:1")
                 DPP9("row_bcast:15 row_mask:0xa bank_mask:0xf") "s_nop 0"
                 : "+v"(zz[0]), "+v"(zz[1]), "+v"(zz[2]), "+v"(zz[3]), "+v"(zz[4]), "+v"(zz[5]), "+v"(zz[6]), "+v"(zz[7]), "+v"(zz[8]));
    const int zp = L_S + gq * 2560;
    if (r == 31) {
#pragma unroll
      for (int i = 0; i < 9; i++) *(u32*)(lds + zp + 12 * hh + 24 * (i / 3) + 4 * (i % 3)) = __float_as_uint(zz[i]);
    }
    asm volatile("" ::: "memory");
    const float z = __uint_as_float(*(const u32*)(lds + zp + 4 * (lane & 15)));
    if (lane < 15 && 15 * gq + lane < 3 * nsz) out[(size_t)NN * 128 + (unsigned)(3 * n0 + 15 * gq + lane)] = (u16)pk2(bf2f(pin) + z, 0.f);
  }
}

extern "C" void kernel_launch(void* const* d_in, const int* in_sizes, int n_in, void* d_out, int out_size, void* d_ws,
                              size_t ws_size, hipStream_t stream) {
  egnn_fused<<<NB, 512, 0, stream>>>((const u16*)d_in[0], (const u16*)d_in[1], (const u32x2*)d_in[2], (const int*)d_in[3],
                                     (const int*)d_in[4], (const u16*)d_in[5], (const u16*)d_in[6], (const u16*)d_in[7],
                                     (char*)d_ws, (u16*)d_out);
}
```
